```python
import torch

def _softmax(x, axis=-1):
    return torch.softmax(x, dim=axis)

def _gelu(x):
    return torch.nn.functional.gelu(x, approximate='tanh')

def _silu(x):
    return torch.nn.functional.silu(x)

def _sigmoid(x):
    return torch.sigmoid(x)

def _layernorm(x, axis=-1, eps=1e-05):
    m = x.mean(dim=axis, keepdim=True)
    v = x.var(dim=axis, keepdim=True, unbiased=False)
    return (x - m) / torch.sqrt(v + eps)

def _scatter_add(tgt, idx, src, axis=0):
    return tgt.clone().index_add_(axis, idx, src)

def _segment_sum(src, seg, n):
    out = torch.zeros((n,) + src.shape[1:], dtype=src.dtype)
    return out.index_add_(0, seg, src)

def _pad(x, pw):
    flat = tuple((v for pair in reversed(pw) for v in pair))
    return torch.nn.functional.pad(x, flat)
kernel_name = 'adaLN_cond__grouped_kv_mqa__dense_activation_silu__prompt'

def setup_inputs():

    _w = lambda a: (lambda t: t.to(torch.bfloat16) if t.is_floating_point() else t)(torch.as_tensor(a))

    def _A_setup():
        B, S, D, H = (1, 2048, 512, 32)
        Dh = D // H
        return {'x': _w((lambda *s: torch.randn(*s, dtype=torch.bfloat16) * 0.05)(B, S, D)), 'wq': _w((lambda *s: torch.randn(*s, dtype=torch.bfloat16) * 0.05)(D, H * Dh)), 'wk': _w((lambda *s: torch.randn(*s, dtype=torch.bfloat16) * 0.05)(D, Dh)), 'wv': _w((lambda *s: torch.randn(*s, dtype=torch.bfloat16) * 0.05)(D, Dh)), 'wo': _w((lambda *s: torch.randn(*s, dtype=torch.bfloat16) * 0.05)(H * Dh, D))}
    _A_in = _A_setup()

    def _B_setup():
        B, S, D, F = (1, 2048, 512, 1376)
        return {'x': _w((lambda *s: torch.randn(*s, dtype=torch.bfloat16) * 0.05)(B, S, D)), 'w1': _w((lambda *s: torch.randn(*s, dtype=torch.bfloat16) * 3.2)(D, F) * D ** (-0.5)), 'w2': _w((lambda *s: torch.randn(*s, dtype=torch.bfloat16) * 1.6)(F, D) * F ** (-0.5))}
    _B_in = _B_setup()
    return {'A_x': _A_in['x'], 'A_wq': _A_in['wq'], 'A_wk': _A_in['wk'], 'A_wv': _A_in['wv'], 'A_wo': _A_in['wo'], 'B_x': _B_in['x'], 'B_w1': _B_in['w1'], 'B_w2': _B_in['w2'], 'c': _w((lambda *s: torch.randn(*s, dtype=torch.bfloat16) * 0.1)(1, 512) * 8.0), 'w_mod': _w((lambda *s: torch.randn(*s, dtype=torch.bfloat16) * 0.1)(512, 6 * 512) * 512 ** (-0.5) * 8.0)}

def reference(A_x, A_wq, A_wk, A_wv, A_wo, B_x, B_w1, B_w2, c, w_mod):

    def _A_ref(x, wq, wk, wv, wo):
        B, S, D = x.shape
        H = 32
        Dh = D // H
        q = (lambda x, *d: x.permute(*d))((x @ wq).reshape(B, S, H, Dh), 0, 2, 1, 3)
        k = x @ wk
        v = x @ wv
        scores = torch.einsum('bhqd,bkd->bhqk', q, k) * Dh ** (-0.5)
        p = _softmax(scores, axis=-1)
        o = torch.einsum('bhqk,bkd->bhqd', p, v)
        return (lambda x, *d: x.permute(*d))(o, 0, 2, 1, 3).reshape(B, S, H * Dh) @ wo

    def _B_ref(x, w1, w2):
        h = torch.einsum('bsd,df->bsf', x, w1)
        h = _silu(h)
        out = torch.einsum('bsf,fd->bsd', h, w2)
        return out
    x = A_x
    mod = (_silu(c) @ w_mod).reshape(c.shape[0], 6, 1, -1)
    sa, ca, ga, sb, cb, gb = (mod[:, 0].clone(), mod[:, 1].clone(), mod[:, 2].clone(), mod[:, 3].clone(), mod[:, 4].clone(), mod[:, 5].clone())
    h = _layernorm(x, axis=-1) * (1 + ca) + sa
    x = x + ga * _A_ref(h, A_wq, A_wk, A_wv, A_wo)
    h = _layernorm(x, axis=-1) * (1 + cb) + sb
    x = x + gb * _B_ref(h, B_w1, B_w2)
    return x
```

```cpp
#include <hip/hip_runtime.h>
#include <hip/hip_bf16.h>
typedef __hip_bfloat16 bf16;
constexpr int S = 2048, D = 512, H = 32, DH = 16, F = 1376;
__device__ inline float b2f(bf16 x) { return __bfloat162float(x); }
__device__ inline float rb(float x) { return __bfloat162float(__float2bfloat16(x)); }

__global__ void k_mod(const bf16* c, const bf16* w, float* mod) {
    int n = blockIdx.x * blockDim.x + threadIdx.x;
    float acc = 0;
    for (int k = 0; k < D; ++k) { float cv = b2f(c[k]); float s = rb(cv / (1.f + __expf(-cv))); acc += s * b2f(w[k * 6 * D + n]); }
    mod[n] = rb(acc);
}
__global__ void k_ln(const float* xf, const bf16* xb, const float* sh, const float* sc, float* h) {
    int r = blockIdx.x, t = threadIdx.x; float v[8]; float s = 0;
    for (int i = 0; i < 8; ++i) { v[i] = xf ? xf[r * D + t + 64 * i] : b2f(xb[r * D + t + 64 * i]); s += v[i]; }
    for (int o = 32; o; o >>= 1) s += __shfl_xor(s, o);
    float m = s / D, q = 0;
    for (int i = 0; i < 8; ++i) q += (v[i] - m) * (v[i] - m);
    for (int o = 32; o; o >>= 1) q += __shfl_xor(q, o);
    float rs = rsqrtf(q / D + 1e-5f);
    for (int i = 0; i < 8; ++i) { int c = t + 64 * i; h[r * D + c] = rb((v[i] - m) * rs * (1.f + sc[c]) + sh[c]); }
}
__global__ void k_gemm(const float* A, const bf16* B, float* C, int N, int K, int mode) {
    int n = blockIdx.x * blockDim.x + threadIdx.x, m0 = blockIdx.y * 8; if (n >= N) return;
    float acc[8] = {0};
    for (int k = 0; k < K; ++k) { float b = b2f(B[k * N + n]);
#pragma unroll
        for (int i = 0; i < 8; ++i) acc[i] += A[(m0 + i) * K + k] * b; }
#pragma unroll
    for (int i = 0; i < 8; ++i) { float v = rb(acc[i]); if (mode == 1) v = rb(v / (1.f + __expf(-v))); C[(m0 + i) * N + n] = v; }
}
__global__ void k_attn(const float* q, const float* k, const float* v, float* o) {
    int id = blockIdx.x * blockDim.x + threadIdx.x;
    float qq[16], acc[16] = {0}; float m = -1e30f, l = 0;
#pragma unroll
    for (int d = 0; d < 16; ++d) qq[d] = q[id * 16 + d] * 0.25f;
    for (int j = 0; j < S; ++j) { float s = 0;
#pragma unroll
        for (int d = 0; d < 16; ++d) s += qq[d] * k[j * 16 + d];
        float mn = fmaxf(m, s), a = __expf(m - mn), p = __expf(s - mn); l = l * a + p; m = mn;
#pragma unroll
        for (int d = 0; d < 16; ++d) acc[d] = acc[d] * a + p * v[j * 16 + d]; }
#pragma unroll
    for (int d = 0; d < 16; ++d) o[id * 16 + d] = rb(acc[d] / l);
}
__global__ void k_res(const float* xf, const bf16* xb, const float* g, const float* y, float* of, bf16* ob) {
    int i = blockIdx.x * blockDim.x + threadIdx.x; int c = i % D;
    float x = xf ? xf[i] : b2f(xb[i]); float r = x + rb(g[c] * y[i]);
    if (of) of[i] = rb(r); else ob[i] = __float2bfloat16(r);
}
extern "C" void kernel_launch(void* const* d_in, const int* in_sizes, int n_in, void* d_out, int out_size, void* d_ws, size_t ws_size, hipStream_t stream) {
    const bf16 *x = (const bf16*)d_in[0], *wq = (const bf16*)d_in[1], *wk = (const bf16*)d_in[2], *wv = (const bf16*)d_in[3], *wo = (const bf16*)d_in[4];
    const bf16 *w1 = (const bf16*)d_in[6], *w2 = (const bf16*)d_in[7], *c = (const bf16*)d_in[8], *wm = (const bf16*)d_in[9];
    float* ws = (float*)d_ws; float *mod = ws, *h = mod + 4096, *q = h + S * D, *k = q + S * D, *v = k + S * 16, *o = v + S * 16, *y = o + S * D, *x1 = y + S * D, *h1 = x1 + S * D;
    k_mod<<<6 * D / 64, 64, 0, stream>>>(c, wm, mod);
    k_ln<<<S, 64, 0, stream>>>(nullptr, x, mod, mod + D, h);
    k_gemm<<<dim3(D / 64, S / 8), 64, 0, stream>>>(h, wq, q, D, D, 0);
    k_gemm<<<dim3(1, S / 8), 64, 0, stream>>>(h, wk, k, 16, D, 0);
    k_gemm<<<dim3(1, S / 8), 64, 0, stream>>>(h, wv, v, 16, D, 0);
    k_attn<<<S * H / 64, 64, 0, stream>>>(q, k, v, o);
    k_gemm<<<dim3(D / 64, S / 8), 64, 0, stream>>>(o, wo, y, D, D, 0);
    k_res<<<S * D / 256, 256, 0, stream>>>(nullptr, x, mod + 2 * D, y, x1, nullptr);
    k_ln<<<S, 64, 0, stream>>>(x1, nullptr, mod + 3 * D, mod + 4 * D, h);
    k_gemm<<<dim3((F + 63) / 64, S / 8), 64, 0, stream>>>(h, w1, h1, F, D, 1);
    k_gemm<<<dim3(D / 64, S / 8), 64, 0, stream>>>(h1, w2, y, D, F, 0);
    k_res<<<S * D / 256, 256, 0, stream>>>(x1, nullptr, mod + 5 * D, y, nullptr, (bf16*)d_out);
}
```

```cpp
#include <hip/hip_runtime.h>
#include <hip/hip_bf16.h>

typedef unsigned u32;
typedef unsigned short u16;
typedef unsigned long long u64;
typedef u32 u32x2 __attribute__((ext_vector_type(2)));
typedef u32 u32x4 __attribute__((ext_vector_type(4)));
typedef u32 u32x32 __attribute__((ext_vector_type(32)));
typedef float f32x2 __attribute__((ext_vector_type(2)));
typedef float f32x4 __attribute__((ext_vector_type(4)));
typedef float f32x8 __attribute__((ext_vector_type(8)));
typedef float f32x16 __attribute__((ext_vector_type(16)));
typedef __bf16 bf16x2 __attribute__((ext_vector_type(2)));
typedef __bf16 bf16x8 __attribute__((ext_vector_type(8)));
typedef __bf16 bf16x16 __attribute__((ext_vector_type(16)));
typedef short s16x4 __attribute__((ext_vector_type(4)));
#define LAS __attribute__((address_space(3)))
#define GAS __attribute__((address_space(1)))
#define DEV __device__ __forceinline__
#define STAMPS 0

constexpr int SEQ = 2048, DM = 512, FF = 1376;
constexpr int WS_EPOCH = 0;
constexpr int WS_MOD = 1024;
constexpr int WS_K = 32768, WS_V = WS_K + 65536;
constexpr int WS_O = 196608;
constexpr int WS_X1 = WS_O + 2097152;
constexpr int WS_H1 = WS_X1 + 2097152;
constexpr int WS_WO = WS_H1 + 64 * 86 * 1024;
constexpr int WS_W1 = WS_WO + 512 * 1024;
constexpr int WS_W2 = WS_W1 + 43 * 32 * 1024;
constexpr int WS_END = WS_W2 + 16 * 86 * 1024;
constexpr int WS_STAMP = 16 * 1024 * 1024;
constexpr int OOB = 0x40000000;
constexpr int SC1 = 16;
constexpr u32 POISON = 0xFFFFFFFFu;
constexpr int SPIN = 1 << 20;
constexpr int HS = 1040;
constexpr int L_H = 0, L_WQ = 66560, L_PRE = 132096, L_Q = 149504, L_MOD = 157696, L_END = 163840;
constexpr int L_RED = L_Q;
constexpr int PPL = 1152, PW = 2 * PPL, PSL = 8 * PW + 32 * 48;
constexpr int TS = 272, TP = 32 * TS;
constexpr int L_WPA = 80000, L_WPB = 0;
constexpr int L_H2A = 36864, L_H2B = 80000;
constexpr int L_F1A = 116736, L_F1B = 0;
constexpr int L_F2A = 0, L_F2B = 8 * TP;
static_assert(4 * PSL <= L_WPA && L_WPA + 4 * TP <= L_F1A && L_F1A + 24576 <= L_Q && 4 * TP <= L_H2A && L_H2A + 32 * HS <= L_H2B && L_H2B + 32 * HS <= L_F1A && 16 * TP <= L_Q, "lds map");

constexpr float LOG2E = 1.4426950408889634f, LN_2 = 0.6931471805599453f;

DEV float bf_lo(u32 x) { return __builtin_bit_cast(float, x << 16); }
DEV float bf_hi(u32 x) { return __builtin_bit_cast(float, x & 0xFFFF0000u); }
DEV u32 pk2(float a, float b) { f32x2 v = {a, b}; return __builtin_bit_cast(u32, __builtin_convertvector(v, bf16x2)); }
DEV u32 clean(u32 x) { return x < 0xFFFFFFFEu ? x : 0xFFFFFFFEu; }
DEV float dot2(u32 a, u32 b, float c) { return __builtin_amdgcn_fdot2_f32_bf16(__builtin_bit_cast(bf16x2, a), __builtin_bit_cast(bf16x2, b), c, false); }
DEV u32 umax3(u32 a, u32 b, u32 c) { return max(max(a, b), c); }
DEV u32 pmax(u32 m, u32x4 v) { return umax3(umax3(m, v.x, v.y), v.z, v.w); }
DEV f32x16 mfma32(u32x4 a, u32x4 b, f32x16 c) { return __builtin_amdgcn_mfma_f32_32x32x16_bf16(__builtin_bit_cast(bf16x8, a), __builtin_bit_cast(bf16x8, b), c, 0, 0, 0); }
DEV f32x4 mfma16(u32x4 a, u32x4 b, f32x4 c) { return __builtin_amdgcn_mfma_f32_16x16x32_bf16(__builtin_bit_cast(bf16x8, a), __builtin_bit_cast(bf16x8, b), c, 0, 0, 0); }
DEV u32x2 trread(LAS char* p) { return __builtin_bit_cast(u32x2, __builtin_amdgcn_ds_read_tr16_b64_v4i16((LAS s16x4*)p)); }
DEV u32x4 cat(u32x2 a, u32x2 b) { return u32x4{a.x, a.y, b.x, b.y}; }
template <int CTRL> DEV float dpp_add(float v) { return v + __builtin_bit_cast(float, __builtin_amdgcn_update_dpp(0, __builtin_bit_cast(int, v), CTRL, 0xF, 0xF, true)); }
DEV float swap32_add(float a, float b) { auto r = __builtin_amdgcn_permlane32_swap(__builtin_bit_cast(u32, a), __builtin_bit_cast(u32, b), false, false); u32 x = r[0], y = r[1]; return __builtin_bit_cast(float, x) + __builtin_bit_cast(float, y); }
DEV float swap16_add(float a, float b) { auto r = __builtin_amdgcn_permlane16_swap(__builtin_bit_cast(u32, a), __builtin_bit_cast(u32, b), false, false); u32 x = r[0], y = r[1]; return __builtin_bit_cast(float, x) + __builtin_bit_cast(float, y); }
DEV float row_sum(float v) { v = dpp_add<0x128>(v); v = dpp_add<0x124>(v); v = dpp_add<0x122>(v); return dpp_add<0x121>(v); }
DEV float rdlane(float v, int l) { return __builtin_bit_cast(float, __builtin_amdgcn_readlane(__builtin_bit_cast(int, v), l)); }
DEV void glds16(const void* g, u32 lds_base) { u32 sv; asm volatile("s_mov_b32 %0, m0\n\ts_mov_b32 m0, %2\n\ts_nop 0\n\tglobal_load_lds_dwordx4 %1, off\n\ts_mov_b32 m0, %0" : "=&s"(sv) : "v"(g), "s"(lds_base) : "memory"); }
#define SB() __builtin_amdgcn_sched_barrier(0)
#define BAR() do { SB(); asm volatile("s_waitcnt lgkmcnt(0)\n\ts_barrier" ::: "memory"); SB(); } while (0)
#define VM0() do { SB(); asm volatile("s_waitcnt vmcnt(0)" ::: "memory"); SB(); } while (0)
#define LD128(off, so) __builtin_amdgcn_raw_buffer_load_b128(ws, (off), (so), SC1)
#define ST128(v, off, so) __builtin_amdgcn_raw_buffer_store_b128((v), ws, (off), (so), SC1)
#define ST64(v, off, so) __builtin_amdgcn_raw_buffer_store_b64((v), ws, (off), (so), SC1)
#define STAMP(i)

DEV void stats4(const u32x4* v, float* mean, float* rstd) {
    float s[4], q[4];
#pragma unroll
    for (int i = 0; i < 4; ++i) {
        const u32 one = 0x3F803F80u;
        s[i] = dot2(v[i].w, one, dot2(v[i].z, one, dot2(v[i].y, one, dot2(v[i].x, one, 0.f))));
        q[i] = dot2(v[i].w, v[i].w, dot2(v[i].z, v[i].z, dot2(v[i].y, v[i].y, dot2(v[i].x, v[i].x, 0.f))));
    }
    float ss = row_sum(swap16_add(swap32_add(s[0], s[1]), swap32_add(s[2], s[3])));
    float qq = row_sum(swap16_add(swap32_add(q[0], q[1]), swap32_add(q[2], q[3])));
    float m = ss * (1.f / DM), var = fmaxf(qq * (1.f / DM) - m * m, 0.f), rs = __builtin_amdgcn_rsqf(var + 1e-5f);
    mean[0] = rdlane(m, 0); mean[2] = rdlane(m, 16); mean[1] = rdlane(m, 32); mean[3] = rdlane(m, 48);
    rstd[0] = rdlane(rs, 0); rstd[2] = rdlane(rs, 16); rstd[1] = rdlane(rs, 32); rstd[3] = rdlane(rs, 48);
}
DEV f32x8 unpack8(u32x4 v) { return f32x8{bf_lo(v.x), bf_hi(v.x), bf_lo(v.y), bf_hi(v.y), bf_lo(v.z), bf_hi(v.z), bf_lo(v.w), bf_hi(v.w)}; }
DEV u32x4 pack8(f32x8 v) { return u32x4{pk2(v[0], v[1]), pk2(v[2], v[3]), pk2(v[4], v[5]), pk2(v[6], v[7])}; }

#define EX(b, i) "v_exp_f32 v[" #b "+" #i "], v[" #b "+" #i "]\n\t"
#define PV(o, vv, p) "v_smfmac_f32_32x32x32_bf16 v[" #o ":" #o "+15], v[" #vv ":" #vv "+3], v[" #p ":" #p "+7], %[idx]\n\t"
#define QK(s, k, q) "v_mfma_f32_32x32x16_bf16 v[" #s ":" #s "+15], v[" #k ":" #k "+3], v[" #q ":" #q "+3], 0\n\t"
#define TILE(c, n, oprev, vprev, knext, qnext, o, v0, X1, X2) \
    EX(c, 0) EX(c, 1) PV(oprev, vprev, n + 8) EX(c, 2) EX(c, 3) QK(n, knext, qnext) EX(c, 4) EX(c, 5) EX(c, 6) X1 EX(c, 7) EX(c, 8) EX(c, 9) \
    PV(o, v0, c) EX(c, 10) EX(c, 11) EX(c, 12) X2 EX(c, 13) EX(c, 14) EX(c, 15)
#define WRO(o) "ds_write_b128 %[pa], v[" #o ":" #o "+3]\n\tds_write_b128 %[pa], v[" #o "+4:" #o "+7] offset:1152\n\tds_write_b32 %[la], v[" #o "+8]\n\t"
#define ZERO(o) "v_mfma_f32_32x32x8bf16_1k v[" #o ":" #o "+15], %[z], %[z], 0\n\t"
#define STEP(o, op, q, qn, WR) \
    TILE(0, 16, op, 156, 68, q, o, 96, "", "") \
    TILE(16, 0, o, 100, 72, q, o, 104, WR, "ds_read_b128 v[" #qn ":" #qn "+3], %[qa]\n\t") \
    TILE(0, 16, o, 108, 76, q, o, 112, "", "") \
    TILE(16, 0, o, 116, 80, q, o, 120, "", "") \
    TILE(0, 16, o, 124, 84, q, o, 128, "s_waitcnt lgkmcnt(0)\n\t", ZERO(op)) \
    TILE(16, 0, o, 132, 88, q, o, 136, "", "") \
    TILE(0, 16, o, 140, 92, q, o, 144, "", "") \
    TILE(16, 0, o, 148, 64, qn, o, 152, "", "")
#define ATT_OPS(pa_, la_, qa_) \
    : "+{v[0:15]}"(S0), "+{v[16:31]}"(S1), "+{v[32:47]}"(O0), "+{v[48:63]}"(O1), "+{v[160:163]}"(Q0), "+{v[164:167]}"(Q1) \
    : "{v[64:95]}"(Kr), "{v[96:127]}"(Va), "{v[128:159]}"(Vb), [idx] "v"(idx), [z] "v"(z2), [pa] "v"(pa_), [la] "v"(la_), [qa] "v"(qa_) : "memory"

__global__ void __launch_bounds__(512, 2) adaln_block(const u16* __restrict__ x, const u16* __restrict__ wq, const u16* __restrict__ wk, const u16* __restrict__ wv,
                                                      const u16* __restrict__ wo, const u16* __restrict__ w1, const u16* __restrict__ w2, const u16* __restrict__ cvec,
                                                      const u16* __restrict__ wm, u16* __restrict__ out, char* wsp) {
    extern __shared__ __attribute__((aligned(16))) char smem[];
    LAS char* L = (LAS char*)smem;
    const u32 lds0 = (u32)(size_t)L;
    const int tid = threadIdx.x, lane = tid & 63, w = __builtin_amdgcn_readfirstlane(tid >> 6);
    const int r32 = lane & 31, hi = lane >> 5;
    const int b = blockIdx.x, xc = b & 7, rr = b >> 3, g = xc * 4 + (rr >> 3), j = rr & 7, cu = g * 8 + j;
    const __amdgpu_buffer_rsrc_t ws = __builtin_amdgcn_make_buffer_rsrc(wsp, 0, WS_END, 0x00020000);
    const u32x4 poison4 = {POISON, POISON, POISON, POISON};
    STAMP(0);

    if (b < 64 && w < 2) ST128(poison4, WS_K + b * 2048 + w * 1024 + lane * 16, 0);
    ST128(poison4, WS_O + ((g * 2 + (w >> 2)) * 32 + j * 4 + (w & 3)) * 1024 + lane * 16, 0);
    const u32 tag = __builtin_amdgcn_raw_buffer_load_b32(ws, WS_EPOCH, 0, SC1) + 1u;
    const bool modp = rr < 24;
    const int mp = ((rr >> 2) * 8 + xc) * 4 + (rr & 3);
    u32x4 wm0, wm1; u32 c0, c1;
    if (modp) {
        const int row = tid >> 1;
        wm0 = *(const GAS u32x4*)(wm + row * 3072 + mp * 16 + (tid & 1) * 8);
        wm1 = *(const GAS u32x4*)(wm + (row + 256) * 3072 + mp * 16 + (tid & 1) * 8);
        c0 = cvec[row]; c1 = cvec[row + 256];
    }
    SB();
    u32x4 xr[8];
#pragma unroll
    for (int i = 0; i < 8; ++i) xr[i] = *(const GAS u32x4*)(x + (g * 64 + w + 8 * ((i + j) & 7)) * DM + lane * 8);
    SB();
    const int l_kv = L_H + (j < 4 ? 32 * HS : 0);
#pragma unroll
    for (int i = 0; i < 2; ++i) {
        glds16(wk + (w * 2 + i) * 512 + lane * 8, lds0 + l_kv + (w * 2 + i) * 1024);
        glds16(wv + (w * 2 + i) * 512 + lane * 8, lds0 + l_kv + 16384 + (w * 2 + i) * 1024);
    }
#pragma unroll
    for (int i = 0; i < 8; ++i) glds16(wq + ((w * 8 + i) * 8 + (lane >> 3)) * DM + j * 64 + (lane & 7) * 8, lds0 + L_WQ + (w * 8 + i) * 1024);
    const int item = w * 256 + b;
    int pre_dst = 0, pre_step = 0, pre_perm = 1; bool pre_two = true;
    if (item < 1648) {
        const u16* src; int ld, m0, ks;
        if (item < 256) { src = wo; ld = DM; m0 = item & 7; ks = item >> 3; pre_step = 32 * 1024; pre_dst = WS_WO + ks * 1024; }
        else if (item < 944) { const int it = item - 256; src = w2; ld = DM; m0 = it & 7; ks = it >> 3; pre_step = 86 * 1024; pre_dst = WS_W2 + ks * 1024; }
        else { const int it = item - 944; src = w1; ld = FF; m0 = it >> 5; ks = it & 31; pre_step = 32 * 1024; pre_dst = WS_W1 + ks * 1024; pre_perm = 0; pre_two = m0 < 21; }
        pre_dst += 2 * m0 * pre_step;
        const int ch = pre_two ? (lane & 7) : (lane & 3);
#pragma unroll
        for (int i = 0; i < 2; ++i) glds16(src + (ks * 16 + i * 8 + (lane >> 3)) * ld + m0 * 64 + ch * 8, lds0 + L_PRE + w * 2048 + i * 1024);
    }
    SB();
    STAMP(1);
    if (modp) {
        const float a0 = bf_lo(c0), a1 = bf_lo(c1);
        const u32 sp = pk2(a0 * __builtin_amdgcn_rcpf(1.f + __builtin_amdgcn_exp2f(-LOG2E * a0)), a1 * __builtin_amdgcn_rcpf(1.f + __builtin_amdgcn_exp2f(-LOG2E * a1)));
        f32x8 acc = unpack8(wm0) * bf_lo(sp) + unpack8(wm1) * bf_hi(sp);
#pragma unroll
        for (int i = 0; i < 8; ++i) { float t = acc[i]; t = dpp_add<0x128>(t); t = dpp_add<0x124>(t); acc[i] = dpp_add<0x122>(t); }
        if ((lane & 14) == 0) {
            LAS f32x4* p = (LAS f32x4*)(L + L_RED + (w * 4 + (lane >> 4)) * 64 + (lane & 1) * 32);
            p[0] = f32x4{acc[0], acc[1], acc[2], acc[3]}; p[1] = f32x4{acc[4], acc[5], acc[6], acc[7]};
        }
    }
    BAR();
    if (modp && w == 0) {
        float t = 0.f;
#pragma unroll
        for (int i = 0; i < 8; ++i) t += *(LAS float*)(L + L_RED + ((lane >> 4) * 8 + i) * 64 + (lane & 15) * 4);
        t = swap32_add(t, t); t = swap16_add(t, t);
        const float nb = __builtin_bit_cast(float, __builtin_amdgcn_update_dpp(0, __builtin_bit_cast(int, t), 0xB1, 0xF, 0xF, true));
        if (lane < 16 && !(lane & 1)) ST64((u32x2{pk2(t, nb), tag}), WS_MOD + mp * 128 + lane * 4, 0);
    }
    STAMP(2);
    f32x8 n1[8];
    {
        float mean[8], rstd[8];
        stats4(xr, mean, rstd); stats4(xr + 4, mean + 4, rstd + 4);
#pragma unroll
        for (int i = 0; i < 8; ++i) n1[i] = unpack8(xr[i]) * rstd[i] - mean[i] * rstd[i];
    }
    VM0();
    STAMP(3);
    if (item < 1648) {
        const int row = (pre_perm ? 4 * hi : 8 * hi) + ((lane & 15) >> 2), rs = pre_perm ? 8 : 4;
        LAS char* p = L + L_PRE + w * 2048 + row * 128 + ((lane >> 4) & 1) * 32 + (lane & 3) * 8;
        const u32x4 f0 = cat(trread(p), trread(p + rs * 128)), f1 = cat(trread(p + 64), trread(p + 64 + rs * 128));
        ST128(f0, pre_dst + lane * 16, 0);
        if (pre_two) ST128(f1, pre_dst + pre_step + lane * 16, 0);
    }
    BAR();
    u32x4 wf[16];
    {
        LAS char* p = L + L_WQ + ((w >> 2) * 256 + 8 * hi + ((lane & 15) >> 2)) * 128 + (w & 1) * 64 + ((lane >> 4) & 1) * 32 + (lane & 3) * 8;
#pragma unroll
        for (int s = 0; s < 16; ++s) wf[s] = cat(trread(p + s * 2048), trread(p + s * 2048 + 512));
    }
    u32x4 kf[16];
    if (w < 2) {
        LAS char* p = L + l_kv + w * 16384 + (8 * (lane >> 4) + ((lane & 15) >> 2)) * 32 + (lane & 3) * 8;
#pragma unroll
        for (int s = 0; s < 16; ++s) kf[s] = cat(trread(p + s * 1024), trread(p + s * 1024 + 128));
    }
    STAMP(4);
    {
        const int G = w * 64 + lane, off = WS_MOD + (G >> 3) * 128 + (G & 7) * 8;
        u32x2 v;
        for (int it = 0; it < SPIN; ++it) {
            asm volatile("" ::: "memory");
            v = __builtin_amdgcn_raw_buffer_load_b64(ws, off, 0, SC1);
            if (__all(v.y == tag)) break;
        }
        *(LAS u32*)(L + L_MOD + G * 4) = v.x;
    }
    VM0();
    BAR();
    STAMP(5);
    const f32x8 sa = unpack8(*(LAS u32x4*)(L + L_MOD + lane * 16)), ca = unpack8(*(LAS u32x4*)(L + L_MOD + 1024 + lane * 16)) + 1.f;
    *(LAS u32x4*)(L + L_H + (w + 8 * j) * HS + lane * 16) = pack8(n1[0] * ca + sa);
    BAR();
    if (w < 2) {
        f32x4 acc = {0.f, 0.f, 0.f, 0.f};
        LAS char* p = L + L_H + (8 * j + (lane & 7)) * HS + (lane >> 4) * 16;
        const int mg = lane >> 4, nn = lane & 15;
        if (w == 0) {
#pragma unroll
            for (int s = 0; s < 16; ++s) acc = mfma16(kf[s], *(LAS u32x4*)(p + s * 64), acc);
            if (nn < 8) ST64((u32x2{clean(pk2(acc[0], acc[1])), clean(pk2(acc[2], acc[3]))}), WS_K + (cu * 8 + nn) * 32 + (mg & 1) * 16 + (mg >> 1) * 8, 0);
        } else {
#pragma unroll
            for (int s = 0; s < 16; ++s) acc = mfma16(*(LAS u32x4*)(p + s * 64), kf[s], acc);
            if (mg < 2) ST64((u32x2{clean(pk2(acc[0], acc[1])), clean(pk2(acc[2], acc[3]))}), WS_V + cu * 256 + nn * 16 + mg * 8, 0);
        }
    }
    STAMP(6);
#pragma unroll
    for (int i = 1; i < 8; ++i) *(LAS u32x4*)(L + L_H + (w + 8 * ((i + j) & 7)) * HS + lane * 16) = pack8(n1[i] * ca + sa);
    BAR();
    STAMP(7);
    u32x4 kq[8], vq[16];
    const int koff = WS_K + w * 8192 + r32 * 32 + hi * 16, voff = WS_V + w * 8192 + hi * 256 + r32 * 16;
#pragma unroll
    for (int i = 0; i < 16; ++i) vq[i] = u32x4{0x3F803F80u, 0x3F803F80u, 0x3F803F80u, 0x3F803F80u};
#pragma unroll
    for (int i = 0; i < 8; ++i) kq[i] = LD128(koff + i * 1024, 0);
    if (r32 < 16) {
#pragma unroll
        for (int i = 0; i < 16; ++i) vq[i] = LD128(voff + i * 512, 0);
    }
    SB();
    {
        f32x16 acc = {};
        LAS char* p = L + L_H + (((w >> 1) & 1) * 32 + r32) * HS + (w >> 2) * 512 + hi * 16;
#pragma unroll
        for (int s = 0; s < 16; ++s) acc = mfma32(wf[s], *(LAS u32x4*)(p + s * 32), acc);
        const int kh = w >> 2;
        f32x8 keep, send;
#pragma unroll
        for (int i = 0; i < 8; ++i) { keep[i] = kh ? acc[8 + i] : acc[i]; send[i] = kh ? acc[i] : acc[8 + i]; }
        LAS f32x4* xq = (LAS f32x4*)(L + L_PRE + w * 2048 + lane * 16);
        xq[0] = f32x4{send[0], send[1], send[2], send[3]}; xq[64] = f32x4{send[4], send[5], send[6], send[7]};
        BAR();
        LAS f32x4* yq = (LAS f32x4*)(L + L_PRE + (w ^ 4) * 2048 + lane * 16);
        const f32x4 y0 = yq[0], y1 = yq[64];
        keep += f32x8{y0[0], y0[1], y0[2], y0[3], y1[0], y1[1], y1[2], y1[3]};
        *(LAS u32x4*)(L + L_Q + (((w >> 1) & 1) * 4 + (w & 1) * 2 + kh) * 1024 + lane * 16) = pack8(keep * (0.25f * LOG2E));
    }
    STAMP(8);
    for (int it = 0; it < SPIN; ++it) {
        u32 m = 0;
#pragma unroll
        for (int i = 0; i < 8; ++i) m = pmax(m, kq[i]);
#pragma unroll
        for (int i = 0; i < 16; ++i) m = pmax(m, vq[i]);
        if (!__any(m == POISON)) break;
        asm volatile("" ::: "memory");
#pragma unroll
        for (int i = 0; i < 8; ++i) kq[i] = LD128(koff + i * 1024, 0);
        if (r32 < 16) {
#pragma unroll
            for (int i = 0; i < 16; ++i) vq[i] = LD128(voff + i * 512, 0);
        }
    }
    BAR();
    STAMP(9);
    if (b == 0 && tid == 0) __builtin_amdgcn_raw_buffer_store_b32(tag, ws, WS_EPOCH, 0, SC1);
    const int T0 = j < 3 ? 6 * j : 5 * j + 3, NT = j < 3 ? 6 : 5;
    ST128(poison4, WS_X1 + (g * 64 + w * 8 + (lane >> 3)) * 1024 + j * 128 + (lane & 7) * 16, 0);
#pragma unroll
    for (int i = 0; i < 3; ++i) {
        const int p = w * 3 + i, hf = p >= 12, ks = p - hf * 12;
        if (ks < 2 * NT) ST128(poison4, WS_H1 + ((g * 2 + hf) * 86 + 2 * T0 + ks) * 1024 + lane * 16, 0);
    }
    {
        const int G = 512 + w * 64 + lane, off = WS_MOD + (G >> 3) * 128 + (G & 7) * 8;
        u32x2 v0, v1;
        for (int it = 0; it < SPIN; ++it) {
            asm volatile("" ::: "memory");
            v0 = __builtin_amdgcn_raw_buffer_load_b64(ws, off, 0, SC1);
            v1 = __builtin_amdgcn_raw_buffer_load_b64(ws, off + 8192, 0, SC1);
            if (__all(v0.y == tag && v1.y == tag)) break;
        }
        *(LAS u32*)(L + L_MOD + G * 4) = v0.x; *(LAS u32*)(L + L_MOD + 2048 + G * 4) = v1.x;
    }
    u32x4 wof[8], w1f[24];
    const int wo_off = WS_WO + ((2 * j + (w & 1)) * 32 + (w >> 1) * 8) * 1024 + lane * 16;
#pragma unroll
    for (int i = 0; i < 8; ++i) wof[i] = LD128(wo_off + i * 1024, 0);
    int w1_off[3];
#pragma unroll
    for (int a = 0; a < 3; ++a) {
        const int tl = w < 2 ? 3 * w + a : w - 2, ks = w < 2 ? 24 : 8 * a;
        w1_off[a] = (tl < NT ? WS_W1 + ((T0 + tl) * 32 + ks) * 1024 : OOB) + lane * 16;
    }
#pragma unroll
    for (int i = 0; i < 6; ++i) w1f[i] = LD128(w1_off[0] + i * 1024, 0);
    SB();

    u32x32 Kr, Va, Vb;
#pragma unroll
    for (int i = 0; i < 8; ++i)
#pragma unroll
        for (int e = 0; e < 4; ++e) { Kr[4 * i + e] = kq[i][e]; Va[4 * i + e] = vq[i][e]; Vb[4 * i + e] = vq[8 + i][e]; }
    const u32 idx = 0xDDDDDDDDu;
    const u32x2 z2 = {0u, 0u};
    u32x4 of[8];
    const int o_off = WS_O + (g * 64 + (w >> 1) * 8) * 1024 + lane * 16;
    const int tk = w * 4 + (lane >> 4), nq = lane & 15;
    u32x2 xres[2];
#pragma unroll 1
    for (int bt = 0; bt < 2; ++bt) {
        f32x16 S0, S1 = {}, O0 = {}, O1 = {};
        u32x4 Q0 = *(LAS u32x4*)(L + L_Q + bt * 4096 + lane * 16), Q1 = {};
        S0 = mfma32(kq[0], Q0, f32x16{});
        const u32 pa = lds0 + w * PW + lane * 16, la = lds0 + 8 * PW + r32 * 48 + w * 4, qa = lds0 + L_Q + bt * 4096 + lane * 16;
        asm volatile("s_nop 7\n\ts_nop 7\n\t" STEP(32, 48, 160, 164, "") ATT_OPS(pa, la, qa + 1024));
        if (bt) BAR();
        asm volatile(STEP(48, 32, 164, 160, WRO(32)) ATT_OPS(pa, la, qa + 2048));
        asm volatile(STEP(32, 48, 160, 164, WRO(48)) ATT_OPS(pa + PSL, la + PSL, qa + 3072));
        asm volatile(STEP(48, 32, 164, 160, WRO(32)) ATT_OPS(pa + 2 * PSL, la + 2 * PSL, qa + 3072));
        asm volatile("s_nop 1\n\t" PV(48, 156, 24) "s_nop 7\n\ts_nop 7\n\t" WRO(48) ATT_OPS(pa + 3 * PSL, la + 3 * PSL, qa));
        if (bt == 0) VM0();
        BAR();
        STAMP(10 + 2 * bt);
        {
            const int sl = w >> 1, q = lane >> 1, rg = lane & 1;
            LAS char* p = L + sl * PSL + rg * PPL + ((w & 1) * 32 + q) * 16;
            f32x4 o = *(LAS f32x4*)p;
#pragma unroll
            for (int i = 1; i < 8; ++i) o += *(LAS f32x4*)(p + i * PW);
            const f32x4 l4 = *(LAS f32x4*)(L + sl * PSL + 8 * PW + q * 48) + *(LAS f32x4*)(L + sl * PSL + 8 * PW + q * 48 + 16);
            const float l = (l4[0] + l4[1]) + (l4[2] + l4[3]);
            const float am = fmaxf(fmaxf(fabsf(o[0]), fabsf(o[1])), fmaxf(fabsf(o[2]), fabsf(o[3])));
            const float sm = (o[0] + o[1]) + (o[2] + o[3]);
            const int dst = WS_O + ((g * 2 + bt) * 32 + j * 4 + sl) * 1024;
            const bool ok = l > 0x1p-60f && l < 0x1p100f && am < 0x1p120f && sm == sm;
            if (__builtin_expect(__all(ok), 1)) {
                o *= __builtin_amdgcn_rcpf(l);
                ST64((u32x2{clean(pk2(o[0], o[1])), clean(pk2(o[2], o[3]))}), dst + (w & 1) * 512 + lane * 8, 0);
            } else {
                const u32x4 Q = *(LAS u32x4*)(L + L_Q + (bt * 4 + sl) * 1024 + lane * 16);
                f32x16 O = {}; float m = -3e38f;
#pragma unroll 1
                for (int t = 0; t < 64; ++t) {
                    const u32x4 kk = LD128(WS_K + t * 1024 + r32 * 32 + hi * 16, 0);
                    u32x4 v0 = {0x3F803F80u, 0x3F803F80u, 0x3F803F80u, 0x3F803F80u}, v1 = v0;
                    if (r32 < 16) { v0 = LD128(WS_V + t * 1024 + hi * 256 + r32 * 16, 0); v1 = LD128(WS_V + t * 1024 + 512 + hi * 256 + r32 * 16, 0); }
                    f32x16 s = mfma32(kk, Q, f32x16{});
                    float tm = m;
#pragma unroll
                    for (int i = 0; i < 16; ++i) tm = fmaxf(tm, s[i]);
                    auto sw = __builtin_amdgcn_permlane32_swap(__builtin_bit_cast(u32, tm), __builtin_bit_cast(u32, tm), false, false);
                    const u32 sw0 = sw[0], sw1 = sw[1];
                    tm = fmaxf(__builtin_bit_cast(float, sw0), __builtin_bit_cast(float, sw1));
                    O *= __builtin_amdgcn_exp2f(m - tm);
                    m = tm;
#pragma unroll
                    for (int i = 0; i < 16; ++i) s[i] = __builtin_amdgcn_exp2f(s[i] - m);
                    O = __builtin_amdgcn_smfmac_f32_32x32x32_bf16(__builtin_bit_cast(bf16x8, v0), __builtin_bit_cast(bf16x16, __builtin_shufflevector(s, s, 0, 1, 2, 3, 4, 5, 6, 7)), O, (int)idx, 0, 0);
                    O = __builtin_amdgcn_smfmac_f32_32x32x32_bf16(__builtin_bit_cast(bf16x8, v1), __builtin_bit_cast(bf16x16, __builtin_shufflevector(s, s, 8, 9, 10, 11, 12, 13, 14, 15)), O, (int)idx, 0, 0);
                }
                const float inv = __builtin_amdgcn_rcpf(O[8]);
                const u32x4 r = {clean(pk2(O[0] * inv, O[1] * inv)), clean(pk2(O[2] * inv, O[3] * inv)), clean(pk2(O[4] * inv, O[5] * inv)), clean(pk2(O[6] * inv, O[7] * inv))};
                if (hi == (w & 1)) ST128(r, dst + lane * 16, 0);
            }
        }
        STAMP(11 + 2 * bt);
    }

#define LOAD_O(hf) _Pragma("unroll") for (int i = 0; i < 8; ++i) of[i] = LD128(o_off + (hf) * 32768 + i * 1024, 0)
#define VALID(N, arr, RELOAD) _Pragma("unroll 1") for (int it = 0; it < SPIN; ++it) { u32 m = 0; _Pragma("unroll") for (int i = 0; i < N; ++i) m = pmax(m, arr[i]); if (!__any(m == POISON)) break; asm volatile("" ::: "memory"); RELOAD; }
#define WRITE_TILE(base, acc) _Pragma("unroll") for (int q = 0; q < 4; ++q) *(LAS f32x4*)((base) + q * 32) = f32x4{acc[4 * q], acc[4 * q + 1], acc[4 * q + 2], acc[4 * q + 3]}
#define WO_GEMM(hf, lp) { VALID(8, of, LOAD_O(hf)); f32x16 acc = {}; _Pragma("unroll") for (int i = 0; i < 8; ++i) acc = mfma32(wof[i], of[i], acc); \
        WRITE_TILE(L + (lp) + (w >> 1) * TP + r32 * TS + (w & 1) * 128 + hi * 16, acc); }
#define X1_FIN(hf, lp) { LAS char* p = L + (lp) + tk * TS + nq * 16; \
        const f32x4 a = (*(LAS f32x4*)p + *(LAS f32x4*)(p + TP)) + (*(LAS f32x4*)(p + 2 * TP) + *(LAS f32x4*)(p + 3 * TP)); \
        const u32x2 xv = xres[hf]; \
        xres[hf] = u32x2{clean(pk2(bf_lo(xv.x) + ga[0] * a[0], bf_hi(xv.x) + ga[1] * a[1])), clean(pk2(bf_lo(xv.y) + ga[2] * a[2], bf_hi(xv.y) + ga[3] * a[3]))}; \
        ST64(xres[hf], WS_X1 + (g * 64 + (hf) * 32 + tk) * 1024 + j * 128 + nq * 8, 0); }
#define LOAD_X1(hf, arr) _Pragma("unroll") for (int i = 0; i < 4; ++i) arr[i] = LD128(WS_X1 + (g * 64 + (hf) * 32 + w * 4 + i) * 1024 + lane * 16, 0)
#define LN2(hf, arr, lp) { VALID(4, arr, LOAD_X1(hf, arr)); float mean[4], rstd[4]; stats4(arr, mean, rstd); \
        _Pragma("unroll") for (int i = 0; i < 4; ++i) *(LAS u32x4*)(L + (lp) + (w * 4 + i) * HS + lane * 16) = pack8((unpack8(arr[i]) * rstd[i] - mean[i] * rstd[i]) * cb + sb); }
#define FFN1(lh, lp, acc) { LAS char* p = L + (lh) + r32 * HS + hi * 16; \
        if (w < 2) { u32x4 bq[8]; _Pragma("unroll") for (int s = 0; s < 8; ++s) bq[s] = *(LAS u32x4*)(p + (24 + s) * 32); \
            _Pragma("unroll") for (int a = 0; a < 3; ++a) { acc = f32x16{}; _Pragma("unroll") for (int s = 0; s < 8; ++s) acc = mfma32(w1f[8 * a + s], bq[s], acc); \
                _Pragma("unroll") for (int q = 0; q < 4; ++q) *(LAS f32x4*)(L + (lp) + (3 * w + a) * 4096 + q * 1024 + lane * 16) = f32x4{acc[4 * q], acc[4 * q + 1], acc[4 * q + 2], acc[4 * q + 3]}; } \
        } else { acc = f32x16{}; _Pragma("unroll") for (int s = 0; s < 24; ++s) acc = mfma32(w1f[s], *(LAS u32x4*)(p + s * 32), acc); } }
#define SILU(hf, lp, acc) if (w >= 2 && w - 2 < NT) { u32 pk[8]; \
        _Pragma("unroll") for (int q = 0; q < 4; ++q) { const f32x4 t = *(LAS f32x4*)(L + (lp) + (w - 2) * 4096 + q * 1024 + lane * 16); float y[4]; \
            _Pragma("unroll") for (int e = 0; e < 4; ++e) { const float z = acc[4 * q + e] + t[e]; y[e] = z * __builtin_amdgcn_rcpf(1.f + __builtin_amdgcn_exp2f(-z)); } \
            pk[2 * q] = clean(pk2(y[0], y[1])); pk[2 * q + 1] = clean(pk2(y[2], y[3])); } \
        const int dst = WS_H1 + ((g * 2 + (hf)) * 86 + 2 * (T0 + w - 2)) * 1024 + lane * 16; \
        ST128((u32x4{pk[0], pk[1], pk[2], pk[3]}), dst, 0); ST128((u32x4{pk[4], pk[5], pk[6], pk[7]}), dst + 1024, 0); }
#define LOAD_H1(hf, arr) _Pragma("unroll") for (int i = 0; i < 11; ++i) arr[i] = LD128((i >= 9 ? h1_off2 : h1_off) + (hf) * 86 * 1024 + i * 1024, 0)
#define FFN2(hf, arr, lp) { VALID(11, arr, LOAD_H1(hf, arr)); f32x16 a0 = {}, a1 = {}; \
        _Pragma("unroll") for (int i = 0; i < 11; ++i) { a0 = mfma32(w2f[i], arr[i], a0); a1 = mfma32(w2f[11 + i], arr[i], a1); } \
        WRITE_TILE(L + (lp) + w * TP + r32 * TS + hi * 16, a0); WRITE_TILE(L + (lp) + w * TP + r32 * TS + 128 + hi * 16, a1); }
#define FINAL(hf, lp) { LAS char* p = L + (lp) + tk * TS + nq * 16; f32x4 a = *(LAS f32x4*)p; \
        _Pragma("unroll") for (int i = 1; i < 8; ++i) a += *(LAS f32x4*)(p + i * TP); \
        const u32x2 xv = xres[hf]; \
        *(GAS u32x2*)(out + (g * 64 + (hf) * 32 + tk) * DM + j * 64 + nq * 4) = u32x2{pk2(bf_lo(xv.x) + gb[0] * a[0], bf_hi(xv.x) + gb[1] * a[1]), pk2(bf_lo(xv.y) + gb[2] * a[2], bf_hi(xv.y) + gb[3] * a[3])}; }

    LOAD_O(0);
#pragma unroll
    for (int hf = 0; hf < 2; ++hf) xres[hf] = *(const GAS u32x2*)(x + (g * 64 + hf * 32 + tk) * DM + j * 64 + nq * 4);
#pragma unroll
    for (int i = 6; i < 24; ++i) w1f[i] = LD128(w1_off[i >> 3] + (i & 7) * 1024, 0);
    SB();
    f32x4 ga, gb;
    { const u32x2 t = *(LAS u32x2*)(L + L_MOD + 2048 + j * 128 + nq * 8); ga = f32x4{bf_lo(t.x), bf_hi(t.x), bf_lo(t.y), bf_hi(t.y)}; }
    { const u32x2 t = *(LAS u32x2*)(L + L_MOD + 5120 + j * 128 + nq * 8); gb = f32x4{bf_lo(t.x), bf_hi(t.x), bf_lo(t.y), bf_hi(t.y)} * LN_2; }
    const f32x8 sb = unpack8(*(LAS u32x4*)(L + L_MOD + 3072 + lane * 16)) * LOG2E, cb = (unpack8(*(LAS u32x4*)(L + L_MOD + 4096 + lane * 16)) + 1.f) * LOG2E;
    WO_GEMM(0, L_WPA);
    BAR();
    STAMP(14);
    LOAD_O(1);
    SB();
    X1_FIN(0, L_WPA);
    WO_GEMM(1, L_WPB);
    VM0();
    BAR();
    STAMP(15);
    u32x4 xa[4], xb[4];
    LOAD_X1(0, xa);
    SB();
    X1_FIN(1, L_WPB);
    u32x4 w2f[22];
    const int w2_off = WS_W2 + (2 * j * 86 + 11 * w) * 1024 + lane * 16, w2_off2 = w == 7 ? OOB : w2_off;
    const int h1_off = WS_H1 + (g * 2 * 86 + 11 * w) * 1024 + lane * 16, h1_off2 = w == 7 ? OOB : h1_off;
    VM0();
    STAMP(16);
    LOAD_X1(1, xb);
#pragma unroll
    for (int i = 0; i < 11; ++i) w2f[i] = LD128((i >= 9 ? w2_off2 : w2_off) + i * 1024, 0);
    SB();
    LN2(0, xa, L_H2A);
    BAR();
    STAMP(17);
    f32x16 accA, accB;
    FFN1(L_H2A, L_F1A, accA);
    LN2(1, xb, L_H2B);
    BAR();
    STAMP(18);
    SILU(0, L_F1A, accA);
    SB();
#pragma unroll
    for (int i = 0; i < 11; ++i) w2f[11 + i] = LD128((i >= 9 ? w2_off2 : w2_off) + (86 + i) * 1024, 0);
    SB();
    FFN1(L_H2B, L_F1B, accB);
    BAR();
    STAMP(19);
    u32x4 ha[11], hb[11];
    LOAD_H1(0, ha);
    SB();
    SILU(1, L_F1B, accB);
    VM0();
    BAR();
    STAMP(20);
    LOAD_H1(1, hb);
    SB();
    FFN2(0, ha, L_F2A);
    BAR();
    STAMP(21);
    FINAL(0, L_F2A);
    FFN2(1, hb, L_F2B);
    BAR();
    STAMP(22);
    FINAL(1, L_F2B);
    STAMP(23);
}

extern "C" void kernel_launch(void* const* d_in, const int* in_sizes, int n_in, void* d_out, int out_size, void* d_ws, size_t ws_size, hipStream_t stream) {
    (void)hipFuncSetAttribute((const void*)adaln_block, hipFuncAttributeMaxDynamicSharedMemorySize, L_END);
    adaln_block<<<256, 512, L_END, stream>>>((const u16*)d_in[0], (const u16*)d_in[1], (const u16*)d_in[2], (const u16*)d_in[3], (const u16*)d_in[4],
                                              (const u16*)d_in[6], (const u16*)d_in[7], (const u16*)d_in[8], (const u16*)d_in[9], (u16*)d_out, (char*)d_ws);
}
```

```cpp
#define PROBE 0
#define PROBE_B 0
#include <hip/hip_runtime.h>
#include <hip/hip_bf16.h>

typedef unsigned u32;
typedef unsigned short u16;
typedef unsigned long long u64;
typedef u32 u32x2 __attribute__((ext_vector_type(2)));
typedef u32 u32x4 __attribute__((ext_vector_type(4)));
typedef u32 u32x32 __attribute__((ext_vector_type(32)));
typedef float f32x2 __attribute__((ext_vector_type(2)));
typedef float f32x4 __attribute__((ext_vector_type(4)));
typedef float f32x8 __attribute__((ext_vector_type(8)));
typedef float f32x16 __attribute__((ext_vector_type(16)));
typedef __bf16 bf16x2 __attribute__((ext_vector_type(2)));
typedef __bf16 bf16x8 __attribute__((ext_vector_type(8)));
typedef __bf16 bf16x16 __attribute__((ext_vector_type(16)));
typedef short s16x4 __attribute__((ext_vector_type(4)));
#define LAS __attribute__((address_space(3)))
#define GAS __attribute__((address_space(1)))
#define DEV __device__ __forceinline__
#define STAMPS 0

constexpr int SEQ = 2048, DM = 512, FF = 1376;
constexpr int WS_EPOCH = 0;
constexpr int WS_MOD = 1024;
constexpr int WS_K = 32768, WS_V = WS_K + 65536;
constexpr int WS_O = 196608;
constexpr int WS_X1 = WS_O + 2097152;
constexpr int WS_H1 = WS_X1 + 2097152;
constexpr int WS_WO = WS_H1 + 64 * 86 * 1024;
constexpr int WS_W1 = WS_WO + 512 * 1024;
constexpr int WS_W2 = WS_W1 + 43 * 32 * 1024;
constexpr int WS_END = WS_W2 + 16 * 86 * 1024;
constexpr int WS_STAMP = 16 * 1024 * 1024;
constexpr int OOB = 0x40000000;
constexpr int SC1 = 16;
constexpr u32 POISON = 0xFFFFFFFFu;
constexpr int SPIN = 1 << 20;
constexpr int HS = 1040;
constexpr int L_H = 0, L_WQ = 66560, L_PRE = 132096, L_Q = 149504, L_MOD = 157696, L_END = 163840;
constexpr int L_RED = L_Q;
constexpr int PPL = 1152, PW = 2 * PPL, PSL = 8 * PW + 32 * 48;
constexpr int TS = 272, TP = 32 * TS;
constexpr int L_WPA = 80000, L_WPB = 0;
constexpr int L_H2A = 36864, L_H2B = 80000;
constexpr int L_F1A = 116736, L_F1B = 0;
constexpr int L_F2A = 0, L_F2B = 8 * TP;
static_assert(4 * PSL <= L_WPA && L_WPA + 4 * TP <= L_F1A && L_F1A + 24576 <= L_Q && 4 * TP <= L_H2A && L_H2A + 32 * HS <= L_H2B && L_H2B + 32 * HS <= L_F1A && 16 * TP <= L_Q, "lds map");

constexpr float LOG2E = 1.4426950408889634f, LN_2 = 0.6931471805599453f;

DEV float bf_lo(u32 x) { return __builtin_bit_cast(float, x << 16); }
DEV float bf_hi(u32 x) { return __builtin_bit_cast(float, x & 0xFFFF0000u); }
DEV u32 pk2(float a, float b) { f32x2 v = {a, b}; return __builtin_bit_cast(u32, __builtin_convertvector(v, bf16x2)); }
DEV u32 clean(u32 x) { return x < 0xFFFFFFFEu ? x : 0xFFFFFFFEu; }
DEV float dot2(u32 a, u32 b, float c) { return __builtin_amdgcn_fdot2_f32_bf16(__builtin_bit_cast(bf16x2, a), __builtin_bit_cast(bf16x2, b), c, false); }
DEV u32 umax3(u32 a, u32 b, u32 c) { return max(max(a, b), c); }
DEV u32 pmax(u32 m, u32x4 v) { return umax3(umax3(m, v.x, v.y), v.z, v.w); }
DEV f32x16 mfma32(u32x4 a, u32x4 b, f32x16 c) { return __builtin_amdgcn_mfma_f32_32x32x16_bf16(__builtin_bit_cast(bf16x8, a), __builtin_bit_cast(bf16x8, b), c, 0, 0, 0); }
DEV f32x4 mfma16(u32x4 a, u32x4 b, f32x4 c) { return __builtin_amdgcn_mfma_f32_16x16x32_bf16(__builtin_bit_cast(bf16x8, a), __builtin_bit_cast(bf16x8, b), c, 0, 0, 0); }
DEV u32x2 trread(LAS char* p) { return __builtin_bit_cast(u32x2, __builtin_amdgcn_ds_read_tr16_b64_v4i16((LAS s16x4*)p)); }
DEV u32x4 cat(u32x2 a, u32x2 b) { return u32x4{a.x, a.y, b.x, b.y}; }
template <int CTRL> DEV float dpp_add(float v) { return v + __builtin_bit_cast(float, __builtin_amdgcn_update_dpp(0, __builtin_bit_cast(int, v), CTRL, 0xF, 0xF, true)); }
DEV float swap32_add(float a, float b) { auto r = __builtin_amdgcn_permlane32_swap(__builtin_bit_cast(u32, a), __builtin_bit_cast(u32, b), false, false); u32 x = r[0], y = r[1]; return __builtin_bit_cast(float, x) + __builtin_bit_cast(float, y); }
DEV float swap16_add(float a, float b) { auto r = __builtin_amdgcn_permlane16_swap(__builtin_bit_cast(u32, a), __builtin_bit_cast(u32, b), false, false); u32 x = r[0], y = r[1]; return __builtin_bit_cast(float, x) + __builtin_bit_cast(float, y); }
DEV float row_sum(float v) { v = dpp_add<0x128>(v); v = dpp_add<0x124>(v); v = dpp_add<0x122>(v); return dpp_add<0x121>(v); }
DEV float rdlane(float v, int l) { return __builtin_bit_cast(float, __builtin_amdgcn_readlane(__builtin_bit_cast(int, v), l)); }
DEV void glds16(const void* g, u32 lds_base) { u32 sv; asm volatile("s_mov_b32 %0, m0\n\ts_mov_b32 m0, %2\n\ts_nop 0\n\tglobal_load_lds_dwordx4 %1, off\n\ts_mov_b32 m0, %0" : "=&s"(sv) : "v"(g), "s"(lds_base) : "memory"); }
#define SB() __builtin_amdgcn_sched_barrier(0)
#define BAR() do { SB(); asm volatile("s_waitcnt lgkmcnt(0)\n\ts_barrier" ::: "memory"); SB(); } while (0)
#define VM0() do { SB(); asm volatile("s_waitcnt vmcnt(0)" ::: "memory"); SB(); } while (0)
#define LD128(off, so) __builtin_amdgcn_raw_buffer_load_b128(ws, (off), (so), SC1)
#define ST128(v, off, so) __builtin_amdgcn_raw_buffer_store_b128((v), ws, (off), (so), SC1)
#define ST64(v, off, so) __builtin_amdgcn_raw_buffer_store_b64((v), ws, (off), (so), SC1)
#define STAMP(i) do { if ((i) == 0) { tp0 = __builtin_amdgcn_s_memrealtime(); tpx = tp0; } else if ((i) == PROBE) { SB(); tpx = __builtin_amdgcn_s_memrealtime(); SB(); } if ((i) == 23 && blockIdx.x == PROBE_B && w == 0) { while (__builtin_amdgcn_s_memrealtime() < tp0 + 10000 + 4 * (tpx - tp0)) __builtin_amdgcn_s_sleep(1); } } while (0)

DEV void stats4(const u32x4* v, float* mean, float* rstd) {
    float s[4], q[4];
#pragma unroll
    for (int i = 0; i < 4; ++i) {
        const u32 one = 0x3F803F80u;
        s[i] = dot2(v[i].w, one, dot2(v[i].z, one, dot2(v[i].y, one, dot2(v[i].x, one, 0.f))));
        q[i] = dot2(v[i].w, v[i].w, dot2(v[i].z, v[i].z, dot2(v[i].y, v[i].y, dot2(v[i].x, v[i].x, 0.f))));
    }
    float ss = row_sum(swap16_add(swap32_add(s[0], s[1]), swap32_add(s[2], s[3])));
    float qq = row_sum(swap16_add(swap32_add(q[0], q[1]), swap32_add(q[2], q[3])));
    float m = ss * (1.f / DM), var = fmaxf(qq * (1.f / DM) - m * m, 0.f), rs = __builtin_amdgcn_rsqf(var + 1e-5f);
    mean[0] = rdlane(m, 0); mean[2] = rdlane(m, 16); mean[1] = rdlane(m, 32); mean[3] = rdlane(m, 48);
    rstd[0] = rdlane(rs, 0); rstd[2] = rdlane(rs, 16); rstd[1] = rdlane(rs, 32); rstd[3] = rdlane(rs, 48);
}
DEV f32x8 unpack8(u32x4 v) { return f32x8{bf_lo(v.x), bf_hi(v.x), bf_lo(v.y), bf_hi(v.y), bf_lo(v.z), bf_hi(v.z), bf_lo(v.w), bf_hi(v.w)}; }
DEV u32x4 pack8(f32x8 v) { return u32x4{pk2(v[0], v[1]), pk2(v[2], v[3]), pk2(v[4], v[5]), pk2(v[6], v[7])}; }

#define EX(b, i) "v_exp_f32 v[" #b "+" #i "], v[" #b "+" #i "]\n\t"
#define PV(o, vv, p) "v_smfmac_f32_32x32x32_bf16 v[" #o ":" #o "+15], v[" #vv ":" #vv "+3], v[" #p ":" #p "+7], %[idx]\n\t"
#define QK(s, k, q) "v_mfma_f32_32x32x16_bf16 v[" #s ":" #s "+15], v[" #k ":" #k "+3], v[" #q ":" #q "+3], 0\n\t"
#define TILE(c, n, oprev, vprev, knext, qnext, o, v0, X1, X2) \
    EX(c, 0) EX(c, 1) PV(oprev, vprev, n + 8) EX(c, 2) EX(c, 3) QK(n, knext, qnext) EX(c, 4) EX(c, 5) EX(c, 6) X1 EX(c, 7) EX(c, 8) EX(c, 9) \
    PV(o, v0, c) EX(c, 10) EX(c, 11) EX(c, 12) X2 EX(c, 13) EX(c, 14) EX(c, 15)
#define WRO(o) "ds_write_b128 %[pa], v[" #o ":" #o "+3]\n\tds_write_b128 %[pa], v[" #o "+4:" #o "+7] offset:1152\n\tds_write_b32 %[la], v[" #o "+8]\n\t"
#define ZERO(o) "v_mfma_f32_32x32x8bf16_1k v[" #o ":" #o "+15], %[z], %[z], 0\n\t"
#define STEP(o, op, q, qn, WR) \
    TILE(0, 16, op, 156, 68, q, o, 96, "", "") \
    TILE(16, 0, o, 100, 72, q, o, 104, WR, "ds_read_b128 v[" #qn ":" #qn "+3], %[qa]\n\t") \
    TILE(0, 16, o, 108, 76, q, o, 112, "", "") \
    TILE(16, 0, o, 116, 80, q, o, 120, "", "") \
    TILE(0, 16, o, 124, 84, q, o, 128, "s_waitcnt lgkmcnt(0)\n\t", ZERO(op)) \
    TILE(16, 0, o, 132, 88, q, o, 136, "", "") \
    TILE(0, 16, o, 140, 92, q, o, 144, "", "") \
    TILE(16, 0, o, 148, 64, qn, o, 152, "", "")
#define ATT_OPS(pa_, la_, qa_) \
    : "+{v[0:15]}"(S0), "+{v[16:31]}"(S1), "+{v[32:47]}"(O0), "+{v[48:63]}"(O1), "+{v[160:163]}"(Q0), "+{v[164:167]}"(Q1) \
    : "{v[64:95]}"(Kr), "{v[96:127]}"(Va), "{v[128:159]}"(Vb), [idx] "v"(idx), [z] "v"(z2), [pa] "v"(pa_), [la] "v"(la_), [qa] "v"(qa_) : "memory"

__global__ void __launch_bounds__(512, 2) adaln_block(const u16* __restrict__ x, const u16* __restrict__ wq, const u16* __restrict__ wk, const u16* __restrict__ wv,
                                                      const u16* __restrict__ wo, const u16* __restrict__ w1, const u16* __restrict__ w2, const u16* __restrict__ cvec,
                                                      const u16* __restrict__ wm, u16* __restrict__ out, char* wsp) {
    extern __shared__ __attribute__((aligned(16))) char smem[];
    LAS char* L = (LAS char*)smem;
    const u32 lds0 = (u32)(size_t)L;
    const int tid = threadIdx.x, lane = tid & 63, w = __builtin_amdgcn_readfirstlane(tid >> 6);
    const int r32 = lane & 31, hi = lane >> 5;
    const int b = blockIdx.x, xc = b & 7, rr = b >> 3, g = xc * 4 + (rr >> 3), j = rr & 7, cu = g * 8 + j;
    const __amdgpu_buffer_rsrc_t ws = __builtin_amdgcn_make_buffer_rsrc(wsp, 0, WS_END, 0x00020000);
    const u32x4 poison4 = {POISON, POISON, POISON, POISON};
    u64 tp0, tpx;
    STAMP(0);

    if (b < 64 && w < 2) ST128(poison4, WS_K + b * 2048 + w * 1024 + lane * 16, 0);
    ST128(poison4, WS_O + ((g * 2 + (w >> 2)) * 32 + j * 4 + (w & 3)) * 1024 + lane * 16, 0);
    const u32 tag = __builtin_amdgcn_raw_buffer_load_b32(ws, WS_EPOCH, 0, SC1) + 1u;
    const bool modp = rr < 24;
    const int mp = ((rr >> 2) * 8 + xc) * 4 + (rr & 3);
    u32x4 wm0, wm1; u32 c0, c1;
    if (modp) {
        const int row = tid >> 1;
        wm0 = *(const GAS u32x4*)(wm + row * 3072 + mp * 16 + (tid & 1) * 8);
        wm1 = *(const GAS u32x4*)(wm + (row + 256) * 3072 + mp * 16 + (tid & 1) * 8);
        c0 = cvec[row]; c1 = cvec[row + 256];
    }
    SB();
    u32x4 xr[8];
#pragma unroll
    for (int i = 0; i < 8; ++i) xr[i] = *(const GAS u32x4*)(x + (g * 64 + w + 8 * ((i + j) & 7)) * DM + lane * 8);
    SB();
    const int l_kv = L_H + (j < 4 ? 32 * HS : 0);
#pragma unroll
    for (int i = 0; i < 2; ++i) {
        glds16(wk + (w * 2 + i) * 512 + lane * 8, lds0 + l_kv + (w * 2 + i) * 1024);
        glds16(wv + (w * 2 + i) * 512 + lane * 8, lds0 + l_kv + 16384 + (w * 2 + i) * 1024);
    }
#pragma unroll
    for (int i = 0; i < 8; ++i) glds16(wq + ((w * 8 + i) * 8 + (lane >> 3)) * DM + j * 64 + (lane & 7) * 8, lds0 + L_WQ + (w * 8 + i) * 1024);
    const int item = w * 256 + b;
    int pre_dst = 0, pre_step = 0, pre_perm = 1; bool pre_two = true;
    if (item < 1648) {
        const u16* src; int ld, m0, ks;
        if (item < 256) { src = wo; ld = DM; m0 = item & 7; ks = item >> 3; pre_step = 32 * 1024; pre_dst = WS_WO + ks * 1024; }
        else if (item < 944) { const int it = item - 256; src = w2; ld = DM; m0 = it & 7; ks = it >> 3; pre_step = 86 * 1024; pre_dst = WS_W2 + ks * 1024; }
        else { const int it = item - 944; src = w1; ld = FF; m0 = it >> 5; ks = it & 31; pre_step = 32 * 1024; pre_dst = WS_W1 + ks * 1024; pre_perm = 0; pre_two = m0 < 21; }
        pre_dst += 2 * m0 * pre_step;
        const int ch = pre_two ? (lane & 7) : (lane & 3);
#pragma unroll
        for (int i = 0; i < 2; ++i) glds16(src + (ks * 16 + i * 8 + (lane >> 3)) * ld + m0 * 64 + ch * 8, lds0 + L_PRE + w * 2048 + i * 1024);
    }
    SB();
    STAMP(1);
    if (modp) {
        const float a0 = bf_lo(c0), a1 = bf_lo(c1);
        const u32 sp = pk2(a0 * __builtin_amdgcn_rcpf(1.f + __builtin_amdgcn_exp2f(-LOG2E * a0)), a1 * __builtin_amdgcn_rcpf(1.f + __builtin_amdgcn_exp2f(-LOG2E * a1)));
        f32x8 acc = unpack8(wm0) * bf_lo(sp) + unpack8(wm1) * bf_hi(sp);
#pragma unroll
        for (int i = 0; i < 8; ++i) { float t = acc[i]; t = dpp_add<0x128>(t); t = dpp_add<0x124>(t); acc[i] = dpp_add<0x122>(t); }
        if ((lane & 14) == 0) {
            LAS f32x4* p = (LAS f32x4*)(L + L_RED + (w * 4 + (lane >> 4)) * 64 + (lane & 1) * 32);
            p[0] = f32x4{acc[0], acc[1], acc[2], acc[3]}; p[1] = f32x4{acc[4], acc[5], acc[6], acc[7]};
        }
    }
    BAR();
    if (modp && w == 0) {
        float t = 0.f;
#pragma unroll
        for (int i = 0; i < 8; ++i) t += *(LAS float*)(L + L_RED + ((lane >> 4) * 8 + i) * 64 + (lane & 15) * 4);
        t = swap32_add(t, t); t = swap16_add(t, t);
        const float nb = __builtin_bit_cast(float, __builtin_amdgcn_update_dpp(0, __builtin_bit_cast(int, t), 0xB1, 0xF, 0xF, true));
        if (lane < 16 && !(lane & 1)) ST64((u32x2{pk2(t, nb), tag}), WS_MOD + mp * 128 + lane * 4, 0);
    }
    STAMP(2);
    f32x8 n1[8];
    {
        float mean[8], rstd[8];
        stats4(xr, mean, rstd); stats4(xr + 4, mean + 4, rstd + 4);
#pragma unroll
        for (int i = 0; i < 8; ++i) n1[i] = unpack8(xr[i]) * rstd[i] - mean[i] * rstd[i];
    }
    VM0();
    STAMP(3);
    if (item < 1648) {
        const int row = (pre_perm ? 4 * hi : 8 * hi) + ((lane & 15) >> 2), rs = pre_perm ? 8 : 4;
        LAS char* p = L + L_PRE + w * 2048 + row * 128 + ((lane >> 4) & 1) * 32 + (lane & 3) * 8;
        const u32x4 f0 = cat(trread(p), trread(p + rs * 128)), f1 = cat(trread(p + 64), trread(p + 64 + rs * 128));
        ST128(f0, pre_dst + lane * 16, 0);
        if (pre_two) ST128(f1, pre_dst + pre_step + lane * 16, 0);
    }
    BAR();
    u32x4 wf[16];
    {
        LAS char* p = L + L_WQ + ((w >> 2) * 256 + 8 * hi + ((lane & 15) >> 2)) * 128 + (w & 1) * 64 + ((lane >> 4) & 1) * 32 + (lane & 3) * 8;
#pragma unroll
        for (int s = 0; s < 16; ++s) wf[s] = cat(trread(p + s * 2048), trread(p + s * 2048 + 512));
    }
    u32x4 kf[16];
    if (w < 2) {
        LAS char* p = L + l_kv + w * 16384 + (8 * (lane >> 4) + ((lane & 15) >> 2)) * 32 + (lane & 3) * 8;
#pragma unroll
        for (int s = 0; s < 16; ++s) kf[s] = cat(trread(p + s * 1024), trread(p + s * 1024 + 128));
    }
    STAMP(4);
    {
        const int G = w * 64 + lane, off = WS_MOD + (G >> 3) * 128 + (G & 7) * 8;
        u32x2 v;
        for (int it = 0; it < SPIN; ++it) {
            asm volatile("" ::: "memory");
            v = __builtin_amdgcn_raw_buffer_load_b64(ws, off, 0, SC1);
            if (__all(v.y == tag)) break;
        }
        *(LAS u32*)(L + L_MOD + G * 4) = v.x;
    }
    VM0();
    BAR();
    STAMP(5);
    const f32x8 sa = unpack8(*(LAS u32x4*)(L + L_MOD + lane * 16)), ca = unpack8(*(LAS u32x4*)(L + L_MOD + 1024 + lane * 16)) + 1.f;
    *(LAS u32x4*)(L + L_H + (w + 8 * j) * HS + lane * 16) = pack8(n1[0] * ca + sa);
    BAR();
    if (w < 2) {
        f32x4 acc = {0.f, 0.f, 0.f, 0.f};
        LAS char* p = L + L_H + (8 * j + (lane & 7)) * HS + (lane >> 4) * 16;
        const int mg = lane >> 4, nn = lane & 15;
        if (w == 0) {
#pragma unroll
            for (int s = 0; s < 16; ++s) acc = mfma16(kf[s], *(LAS u32x4*)(p + s * 64), acc);
            if (nn < 8) ST64((u32x2{clean(pk2(acc[0], acc[1])), clean(pk2(acc[2], acc[3]))}), WS_K + (cu * 8 + nn) * 32 + (mg & 1) * 16 + (mg >> 1) * 8, 0);
        } else {
#pragma unroll
            for (int s = 0; s < 16; ++s) acc = mfma16(*(LAS u32x4*)(p + s * 64), kf[s], acc);
            if (mg < 2) ST64((u32x2{clean(pk2(acc[0], acc[1])), clean(pk2(acc[2], acc[3]))}), WS_V + cu * 256 + nn * 16 + mg * 8, 0);
        }
    }
    STAMP(6);
#pragma unroll
    for (int i = 1; i < 8; ++i) *(LAS u32x4*)(L + L_H + (w + 8 * ((i + j) & 7)) * HS + lane * 16) = pack8(n1[i] * ca + sa);
    BAR();
    STAMP(7);
    u32x4 kq[8], vq[16];
    const int koff = WS_K + w * 8192 + r32 * 32 + hi * 16, voff = WS_V + w * 8192 + hi * 256 + r32 * 16;
#pragma unroll
    for (int i = 0; i < 16; ++i) vq[i] = u32x4{0x3F803F80u, 0x3F803F80u, 0x3F803F80u, 0x3F803F80u};
#pragma unroll
    for (int i = 0; i < 8; ++i) kq[i] = LD128(koff + i * 1024, 0);
    if (r32 < 16) {
#pragma unroll
        for (int i = 0; i < 16; ++i) vq[i] = LD128(voff + i * 512, 0);
    }
    SB();
    {
        f32x16 acc = {};
        LAS char* p = L + L_H + (((w >> 1) & 1) * 32 + r32) * HS + (w >> 2) * 512 + hi * 16;
#pragma unroll
        for (int s = 0; s < 16; ++s) acc = mfma32(wf[s], *(LAS u32x4*)(p + s * 32), acc);
        const int kh = w >> 2;
        f32x8 keep, send;
#pragma unroll
        for (int i = 0; i < 8; ++i) { keep[i] = kh ? acc[8 + i] : acc[i]; send[i] = kh ? acc[i] : acc[8 + i]; }
        LAS f32x4* xq = (LAS f32x4*)(L + L_PRE + w * 2048 + lane * 16);
        xq[0] = f32x4{send[0], send[1], send[2], send[3]}; xq[64] = f32x4{send[4], send[5], send[6], send[7]};
        BAR();
        LAS f32x4* yq = (LAS f32x4*)(L + L_PRE + (w ^ 4) * 2048 + lane * 16);
        const f32x4 y0 = yq[0], y1 = yq[64];
        keep += f32x8{y0[0], y0[1], y0[2], y0[3], y1[0], y1[1], y1[2], y1[3]};
        *(LAS u32x4*)(L + L_Q + (((w >> 1) & 1) * 4 + (w & 1) * 2 + kh) * 1024 + lane * 16) = pack8(keep * (0.25f * LOG2E));
    }
    STAMP(8);
    for (int it = 0; it < SPIN; ++it) {
        u32 m = 0;
#pragma unroll
        for (int i = 0; i < 8; ++i) m = pmax(m, kq[i]);
#pragma unroll
        for (int i = 0; i < 16; ++i) m = pmax(m, vq[i]);
        if (!__any(m == POISON)) break;
        asm volatile("" ::: "memory");
#pragma unroll
        for (int i = 0; i < 8; ++i) kq[i] = LD128(koff + i * 1024, 0);
        if (r32 < 16) {
#pragma unroll
            for (int i = 0; i < 16; ++i) vq[i] = LD128(voff + i * 512, 0);
        }
    }
    BAR();
    STAMP(9);
    if (b == 0 && tid == 0) __builtin_amdgcn_raw_buffer_store_b32(tag, ws, WS_EPOCH, 0, SC1);
    const int T0 = j < 3 ? 6 * j : 5 * j + 3, NT = j < 3 ? 6 : 5;
    ST128(poison4, WS_X1 + (g * 64 + w * 8 + (lane >> 3)) * 1024 + j * 128 + (lane & 7) * 16, 0);
#pragma unroll
    for (int i = 0; i < 3; ++i) {
        const int p = w * 3 + i, hf = p >= 12, ks = p - hf * 12;
        if (ks < 2 * NT) ST128(poison4, WS_H1 + ((g * 2 + hf) * 86 + 2 * T0 + ks) * 1024 + lane * 16, 0);
    }
    {
        const int G = 512 + w * 64 + lane, off = WS_MOD + (G >> 3) * 128 + (G & 7) * 8;
        u32x2 v0, v1;
        for (int it = 0; it < SPIN; ++it) {
            asm volatile("" ::: "memory");
            v0 = __builtin_amdgcn_raw_buffer_load_b64(ws, off, 0, SC1);
            v1 = __builtin_amdgcn_raw_buffer_load_b64(ws, off + 8192, 0, SC1);
            if (__all(v0.y == tag && v1.y == tag)) break;
        }
        *(LAS u32*)(L + L_MOD + G * 4) = v0.x; *(LAS u32*)(L + L_MOD + 2048 + G * 4) = v1.x;
    }
    u32x4 wof[8], w1f[24];
    const int wo_off = WS_WO + ((2 * j + (w & 1)) * 32 + (w >> 1) * 8) * 1024 + lane * 16;
#pragma unroll
    for (int i = 0; i < 8; ++i) wof[i] = LD128(wo_off + i * 1024, 0);
    int w1_off[3];
#pragma unroll
    for (int a = 0; a < 3; ++a) {
        const int tl = w < 2 ? 3 * w + a : w - 2, ks = w < 2 ? 24 : 8 * a;
        w1_off[a] = (tl < NT ? WS_W1 + ((T0 + tl) * 32 + ks) * 1024 : OOB) + lane * 16;
    }
#pragma unroll
    for (int i = 0; i < 6; ++i) w1f[i] = LD128(w1_off[0] + i * 1024, 0);
    SB();

    u32x32 Kr, Va, Vb;
#pragma unroll
    for (int i = 0; i < 8; ++i)
#pragma unroll
        for (int e = 0; e < 4; ++e) { Kr[4 * i + e] = kq[i][e]; Va[4 * i + e] = vq[i][e]; Vb[4 * i + e] = vq[8 + i][e]; }
    const u32 idx = 0xDDDDDDDDu;
    const u32x2 z2 = {0u, 0u};
    u32x4 of[8];
    const int o_off = WS_O + (g * 64 + (w >> 1) * 8) * 1024 + lane * 16;
    const int tk = w * 4 + (lane >> 4), nq = lane & 15;
    u32x2 xres[2];
#pragma unroll 1
    for (int bt = 0; bt < 2; ++bt) {
        f32x16 S0, S1 = {}, O0 = {}, O1 = {};
        u32x4 Q0 = *(LAS u32x4*)(L + L_Q + bt * 4096 + lane * 16), Q1 = {};
        S0 = mfma32(kq[0], Q0, f32x16{});
        const u32 pa = lds0 + w * PW + lane * 16, la = lds0 + 8 * PW + r32 * 48 + w * 4, qa = lds0 + L_Q + bt * 4096 + lane * 16;
        asm volatile("s_nop 7\n\ts_nop 7\n\t" STEP(32, 48, 160, 164, "") ATT_OPS(pa, la, qa + 1024));
        if (bt) BAR();
        asm volatile(STEP(48, 32, 164, 160, WRO(32)) ATT_OPS(pa, la, qa + 2048));
        asm volatile(STEP(32, 48, 160, 164, WRO(48)) ATT_OPS(pa + PSL, la + PSL, qa + 3072));
        asm volatile(STEP(48, 32, 164, 160, WRO(32)) ATT_OPS(pa + 2 * PSL, la + 2 * PSL, qa + 3072));
        asm volatile("s_nop 1\n\t" PV(48, 156, 24) "s_nop 7\n\ts_nop 7\n\t" WRO(48) ATT_OPS(pa + 3 * PSL, la + 3 * PSL, qa));
        if (bt == 0) VM0();
        BAR();
        STAMP(10 + 2 * bt);
        {
            const int sl = w >> 1, q = lane >> 1, rg = lane & 1;
            LAS char* p = L + sl * PSL + rg * PPL + ((w & 1) * 32 + q) * 16;
            f32x4 o = *(LAS f32x4*)p;
#pragma unroll
            for (int i = 1; i < 8; ++i) o += *(LAS f32x4*)(p + i * PW);
            const f32x4 l4 = *(LAS f32x4*)(L + sl * PSL + 8 * PW + q * 48) + *(LAS f32x4*)(L + sl * PSL + 8 * PW + q * 48 + 16);
            const float l = (l4[0] + l4[1]) + (l4[2] + l4[3]);
            const float am = fmaxf(fmaxf(fabsf(o[0]), fabsf(o[1])), fmaxf(fabsf(o[2]), fabsf(o[3])));
            const float sm = (o[0] + o[1]) + (o[2] + o[3]);
            const int dst = WS_O + ((g * 2 + bt) * 32 + j * 4 + sl) * 1024;
            const bool ok = l > 0x1p-60f && l < 0x1p100f && am < 0x1p120f && sm == sm;
            if (__builtin_expect(__all(ok), 1)) {
                o *= __builtin_amdgcn_rcpf(l);
                ST64((u32x2{clean(pk2(o[0], o[1])), clean(pk2(o[2], o[3]))}), dst + (w & 1) * 512 + lane * 8, 0);
            } else {
                const u32x4 Q = *(LAS u32x4*)(L + L_Q + (bt * 4 + sl) * 1024 + lane * 16);
                f32x16 O = {}; float m = -3e38f;
#pragma unroll 1
                for (int t = 0; t < 64; ++t) {
                    const u32x4 kk = LD128(WS_K + t * 1024 + r32 * 32 + hi * 16, 0);
                    u32x4 v0 = {0x3F803F80u, 0x3F803F80u, 0x3F803F80u, 0x3F803F80u}, v1 = v0;
                    if (r32 < 16) { v0 = LD128(WS_V + t * 1024 + hi * 256 + r32 * 16, 0); v1 = LD128(WS_V + t * 1024 + 512 + hi * 256 + r32 * 16, 0); }
                    f32x16 s = mfma32(kk, Q, f32x16{});
                    float tm = m;
#pragma unroll
                    for (int i = 0; i < 16; ++i) tm = fmaxf(tm, s[i]);
                    auto sw = __builtin_amdgcn_permlane32_swap(__builtin_bit_cast(u32, tm), __builtin_bit_cast(u32, tm), false, false);
                    const u32 sw0 = sw[0], sw1 = sw[1];
                    tm = fmaxf(__builtin_bit_cast(float, sw0), __builtin_bit_cast(float, sw1));
                    O *= __builtin_amdgcn_exp2f(m - tm);
                    m = tm;
#pragma unroll
                    for (int i = 0; i < 16; ++i) s[i] = __builtin_amdgcn_exp2f(s[i] - m);
                    O = __builtin_amdgcn_smfmac_f32_32x32x32_bf16(__builtin_bit_cast(bf16x8, v0), __builtin_bit_cast(bf16x16, __builtin_shufflevector(s, s, 0, 1, 2, 3, 4, 5, 6, 7)), O, (int)idx, 0, 0);
                    O = __builtin_amdgcn_smfmac_f32_32x32x32_bf16(__builtin_bit_cast(bf16x8, v1), __builtin_bit_cast(bf16x16, __builtin_shufflevector(s, s, 8, 9, 10, 11, 12, 13, 14, 15)), O, (int)idx, 0, 0);
                }
                const float inv = __builtin_amdgcn_rcpf(O[8]);
                const u32x4 r = {clean(pk2(O[0] * inv, O[1] * inv)), clean(pk2(O[2] * inv, O[3] * inv)), clean(pk2(O[4] * inv, O[5] * inv)), clean(pk2(O[6] * inv, O[7] * inv))};
                if (hi == (w & 1)) ST128(r, dst + lane * 16, 0);
            }
        }
        STAMP(11 + 2 * bt);
    }

#define LOAD_O(hf) _Pragma("unroll") for (int i = 0; i < 8; ++i) of[i] = LD128(o_off + (hf) * 32768 + i * 1024, 0)
#define VALID(N, arr, RELOAD) _Pragma("unroll 1") for (int it = 0; it < SPIN; ++it) { u32 m = 0; _Pragma("unroll") for (int i = 0; i < N; ++i) m = pmax(m, arr[i]); if (!__any(m == POISON)) break; asm volatile("" ::: "memory"); RELOAD; }
#define WRITE_TILE(base, acc) _Pragma("unroll") for (int q = 0; q < 4; ++q) *(LAS f32x4*)((base) + q * 32) = f32x4{acc[4 * q], acc[4 * q + 1], acc[4 * q + 2], acc[4 * q + 3]}
#define WO_GEMM(hf, lp) { VALID(8, of, LOAD_O(hf)); f32x16 acc = {}; _Pragma("unroll") for (int i = 0; i < 8; ++i) acc = mfma32(wof[i], of[i], acc); \
        WRITE_TILE(L + (lp) + (w >> 1) * TP + r32 * TS + (w & 1) * 128 + hi * 16, acc); }
#define X1_FIN(hf, lp) { LAS char* p = L + (lp) + tk * TS + nq * 16; \
        const f32x4 a = (*(LAS f32x4*)p + *(LAS f32x4*)(p + TP)) + (*(LAS f32x4*)(p + 2 * TP) + *(LAS f32x4*)(p + 3 * TP)); \
        const u32x2 xv = xres[hf]; \
        xres[hf] = u32x2{clean(pk2(bf_lo(xv.x) + ga[0] * a[0], bf_hi(xv.x) + ga[1] * a[1])), clean(pk2(bf_lo(xv.y) + ga[2] * a[2], bf_hi(xv.y) + ga[3] * a[3]))}; \
        ST64(xres[hf], WS_X1 + (g * 64 + (hf) * 32 + tk) * 1024 + j * 128 + nq * 8, 0); }
#define LOAD_X1(hf, arr) _Pragma("unroll") for (int i = 0; i < 4; ++i) arr[i] = LD128(WS_X1 + (g * 64 + (hf) * 32 + w * 4 + i) * 1024 + lane * 16, 0)
#define LN2(hf, arr, lp) { VALID(4, arr, LOAD_X1(hf, arr)); float mean[4], rstd[4]; stats4(arr, mean, rstd); \
        _Pragma("unroll") for (int i = 0; i < 4; ++i) *(LAS u32x4*)(L + (lp) + (w * 4 + i) * HS + lane * 16) = pack8((unpack8(arr[i]) * rstd[i] - mean[i] * rstd[i]) * cb + sb); }
#define FFN1(lh, lp, acc) { LAS char* p = L + (lh) + r32 * HS + hi * 16; \
        if (w < 2) { u32x4 bq[8]; _Pragma("unroll") for (int s = 0; s < 8; ++s) bq[s] = *(LAS u32x4*)(p + (24 + s) * 32); \
            _Pragma("unroll") for (int a = 0; a < 3; ++a) { acc = f32x16{}; _Pragma("unroll") for (int s = 0; s < 8; ++s) acc = mfma32(w1f[8 * a + s], bq[s], acc); \
                _Pragma("unroll") for (int q = 0; q < 4; ++q) *(LAS f32x4*)(L + (lp) + (3 * w + a) * 4096 + q * 1024 + lane * 16) = f32x4{acc[4 * q], acc[4 * q + 1], acc[4 * q + 2], acc[4 * q + 3]}; } \
        } else { acc = f32x16{}; _Pragma("unroll") for (int s = 0; s < 24; ++s) acc = mfma32(w1f[s], *(LAS u32x4*)(p + s * 32), acc); } }
#define SILU(hf, lp, acc) if (w >= 2 && w - 2 < NT) { u32 pk[8]; \
        _Pragma("unroll") for (int q = 0; q < 4; ++q) { const f32x4 t = *(LAS f32x4*)(L + (lp) + (w - 2) * 4096 + q * 1024 + lane * 16); float y[4]; \
            _Pragma("unroll") for (int e = 0; e < 4; ++e) { const float z = acc[4 * q + e] + t[e]; y[e] = z * __builtin_amdgcn_rcpf(1.f + __builtin_amdgcn_exp2f(-z)); } \
            pk[2 * q] = clean(pk2(y[0], y[1])); pk[2 * q + 1] = clean(pk2(y[2], y[3])); } \
        const int dst = WS_H1 + ((g * 2 + (hf)) * 86 + 2 * (T0 + w - 2)) * 1024 + lane * 16; \
        ST128((u32x4{pk[0], pk[1], pk[2], pk[3]}), dst, 0); ST128((u32x4{pk[4], pk[5], pk[6], pk[7]}), dst + 1024, 0); }
#define LOAD_H1(hf, arr) _Pragma("unroll") for (int i = 0; i < 11; ++i) arr[i] = LD128((i >= 9 ? h1_off2 : h1_off) + (hf) * 86 * 1024 + i * 1024, 0)
#define FFN2(hf, arr, lp) { VALID(11, arr, LOAD_H1(hf, arr)); f32x16 a0 = {}, a1 = {}; \
        _Pragma("unroll") for (int i = 0; i < 11; ++i) { a0 = mfma32(w2f[i], arr[i], a0); a1 = mfma32(w2f[11 + i], arr[i], a1); } \
        WRITE_TILE(L + (lp) + w * TP + r32 * TS + hi * 16, a0); WRITE_TILE(L + (lp) + w * TP + r32 * TS + 128 + hi * 16, a1); }
#define FINAL(hf, lp) { LAS char* p = L + (lp) + tk * TS + nq * 16; f32x4 a = *(LAS f32x4*)p; \
        _Pragma("unroll") for (int i = 1; i < 8; ++i) a += *(LAS f32x4*)(p + i * TP); \
        const u32x2 xv = xres[hf]; \
        *(GAS u32x2*)(out + (g * 64 + (hf) * 32 + tk) * DM + j * 64 + nq * 4) = u32x2{pk2(bf_lo(xv.x) + gb[0] * a[0], bf_hi(xv.x) + gb[1] * a[1]), pk2(bf_lo(xv.y) + gb[2] * a[2], bf_hi(xv.y) + gb[3] * a[3])}; }

    LOAD_O(0);
#pragma unroll
    for (int hf = 0; hf < 2; ++hf) xres[hf] = *(const GAS u32x2*)(x + (g * 64 + hf * 32 + tk) * DM + j * 64 + nq * 4);
#pragma unroll
    for (int i = 6; i < 24; ++i) w1f[i] = LD128(w1_off[i >> 3] + (i & 7) * 1024, 0);
    SB();
    f32x4 ga, gb;
    { const u32x2 t = *(LAS u32x2*)(L + L_MOD + 2048 + j * 128 + nq * 8); ga = f32x4{bf_lo(t.x), bf_hi(t.x), bf_lo(t.y), bf_hi(t.y)}; }
    { const u32x2 t = *(LAS u32x2*)(L + L_MOD + 5120 + j * 128 + nq * 8); gb = f32x4{bf_lo(t.x), bf_hi(t.x), bf_lo(t.y), bf_hi(t.y)} * LN_2; }
    const f32x8 sb = unpack8(*(LAS u32x4*)(L + L_MOD + 3072 + lane * 16)) * LOG2E, cb = (unpack8(*(LAS u32x4*)(L + L_MOD + 4096 + lane * 16)) + 1.f) * LOG2E;
    WO_GEMM(0, L_WPA);
    BAR();
    STAMP(14);
    LOAD_O(1);
    SB();
    X1_FIN(0, L_WPA);
    WO_GEMM(1, L_WPB);
    VM0();
    BAR();
    STAMP(15);
    u32x4 xa[4], xb[4];
    LOAD_X1(0, xa);
    SB();
    X1_FIN(1, L_WPB);
    u32x4 w2f[22];
    const int w2_off = WS_W2 + (2 * j * 86 + 11 * w) * 1024 + lane * 16, w2_off2 = w == 7 ? OOB : w2_off;
    const int h1_off = WS_H1 + (g * 2 * 86 + 11 * w) * 1024 + lane * 16, h1_off2 = w == 7 ? OOB : h1_off;
    VM0();
    STAMP(16);
    LOAD_X1(1, xb);
#pragma unroll
    for (int i = 0; i < 11; ++i) w2f[i] = LD128((i >= 9 ? w2_off2 : w2_off) + i * 1024, 0);
    SB();
    LN2(0, xa, L_H2A);
    BAR();
    STAMP(17);
    f32x16 accA, accB;
    FFN1(L_H2A, L_F1A, accA);
    LN2(1, xb, L_H2B);
    BAR();
    STAMP(18);
    SILU(0, L_F1A, accA);
    SB();
#pragma unroll
    for (int i = 0; i < 11; ++i) w2f[11 + i] = LD128((i >= 9 ? w2_off2 : w2_off) + (86 + i) * 1024, 0);
    SB();
    FFN1(L_H2B, L_F1B, accB);
    BAR();
    STAMP(19);
    u32x4 ha[11], hb[11];
    LOAD_H1(0, ha);
    SB();
    SILU(1, L_F1B, accB);
    VM0();
    BAR();
    STAMP(20);
    LOAD_H1(1, hb);
    SB();
    FFN2(0, ha, L_F2A);
    BAR();
    STAMP(21);
    FINAL(0, L_F2A);
    FFN2(1, hb, L_F2B);
    BAR();
    STAMP(22);
    FINAL(1, L_F2B);
    STAMP(23);
}

extern "C" void kernel_launch(void* const* d_in, const int* in_sizes, int n_in, void* d_out, int out_size, void* d_ws, size_t ws_size, hipStream_t stream) {
    (void)hipFuncSetAttribute((const void*)adaln_block, hipFuncAttributeMaxDynamicSharedMemorySize, L_END);
    adaln_block<<<256, 512, L_END, stream>>>((const u16*)d_in[0], (const u16*)d_in[1], (const u16*)d_in[2], (const u16*)d_in[3], (const u16*)d_in[4],
                                              (const u16*)d_in[6], (const u16*)d_in[7], (const u16*)d_in[8], (const u16*)d_in[9], (u16*)d_out, (char*)d_ws);
}
```

```cpp
#include <hip/hip_runtime.h>
#include <hip/hip_bf16.h>

typedef unsigned u32;
typedef unsigned short u16;
typedef unsigned long long u64;
typedef u32 u32x2 __attribute__((ext_vector_type(2)));
typedef u32 u32x4 __attribute__((ext_vector_type(4)));
typedef u32 u32x32 __attribute__((ext_vector_type(32)));
typedef float f32x2 __attribute__((ext_vector_type(2)));
typedef float f32x4 __attribute__((ext_vector_type(4)));
typedef float f32x8 __attribute__((ext_vector_type(8)));
typedef float f32x16 __attribute__((ext_vector_type(16)));
typedef __bf16 bf16x2 __attribute__((ext_vector_type(2)));
typedef __bf16 bf16x8 __attribute__((ext_vector_type(8)));
typedef __bf16 bf16x16 __attribute__((ext_vector_type(16)));
typedef short s16x4 __attribute__((ext_vector_type(4)));
#define LAS __attribute__((address_space(3)))
#define GAS __attribute__((address_space(1)))
#define DEV __device__ __forceinline__
#define STAMPS 0

constexpr int SEQ = 2048, DM = 512, FF = 1376;
constexpr int WS_EPOCH = 0;
constexpr int WS_MOD = 1024;
constexpr int WS_K = 32768, WS_V = WS_K + 65536;
constexpr int WS_O = 196608;
constexpr int WS_X1 = WS_O + 2097152;
constexpr int WS_H1 = WS_X1 + 2097152;
constexpr int WS_WO = WS_H1 + 64 * 86 * 1024;
constexpr int WS_W1 = WS_WO + 512 * 1024;
constexpr int WS_W2 = WS_W1 + 43 * 32 * 1024;
constexpr int WS_END = WS_W2 + 16 * 86 * 1024;
constexpr int WS_STAMP = 16 * 1024 * 1024;
constexpr int OOB = 0x40000000;
constexpr int SC1 = 16;
constexpr u32 POISON = 0xFFFFFFFFu;
constexpr int SPIN = 1 << 20;
constexpr int HS = 1040;
constexpr int L_H = 0, L_WQ = 66560, L_PRE = 132096, L_Q = 149504, L_MOD = 157696, L_END = 163840;
constexpr int L_RED = L_Q;
constexpr int PPL = 1152, PW = 2 * PPL, PSL = 8 * PW + 32 * 48;
constexpr int TS = 272, TP = 32 * TS;
constexpr int L_WPA = 80000, L_WPB = 0;
constexpr int L_H2A = 36864, L_H2B = 80000;
constexpr int L_F1A = 116736, L_F1B = 0;
constexpr int L_F2A = 0, L_F2B = 8 * TP;
static_assert(4 * PSL <= L_WPA && L_WPA + 4 * TP <= L_F1A && L_F1A + 24576 <= L_Q && 4 * TP <= L_H2A && L_H2A + 32 * HS <= L_H2B && L_H2B + 32 * HS <= L_F1A && 16 * TP <= L_Q, "lds map");

constexpr float LOG2E = 1.4426950408889634f, LN_2 = 0.6931471805599453f;

DEV float bf_lo(u32 x) { return __builtin_bit_cast(float, x << 16); }
DEV float bf_hi(u32 x) { return __builtin_bit_cast(float, x & 0xFFFF0000u); }
DEV u32 pk2(float a, float b) { f32x2 v = {a, b}; return __builtin_bit_cast(u32, __builtin_convertvector(v, bf16x2)); }
DEV u32 clean(u32 x) { return x < 0xFFFFFFFEu ? x : 0xFFFFFFFEu; }
DEV float dot2(u32 a, u32 b, float c) { return __builtin_amdgcn_fdot2_f32_bf16(__builtin_bit_cast(bf16x2, a), __builtin_bit_cast(bf16x2, b), c, false); }
DEV u32 umax3(u32 a, u32 b, u32 c) { return max(max(a, b), c); }
DEV u32 pmax(u32 m, u32x4 v) { return umax3(umax3(m, v.x, v.y), v.z, v.w); }
DEV f32x16 mfma32(u32x4 a, u32x4 b, f32x16 c) { return __builtin_amdgcn_mfma_f32_32x32x16_bf16(__builtin_bit_cast(bf16x8, a), __builtin_bit_cast(bf16x8, b), c, 0, 0, 0); }
DEV f32x4 mfma16(u32x4 a, u32x4 b, f32x4 c) { return __builtin_amdgcn_mfma_f32_16x16x32_bf16(__builtin_bit_cast(bf16x8, a), __builtin_bit_cast(bf16x8, b), c, 0, 0, 0); }
DEV u32x2 trread(LAS char* p) { return __builtin_bit_cast(u32x2, __builtin_amdgcn_ds_read_tr16_b64_v4i16((LAS s16x4*)p)); }
DEV u32x4 cat(u32x2 a, u32x2 b) { return u32x4{a.x, a.y, b.x, b.y}; }
template <int CTRL> DEV float dpp_add(float v) { return v + __builtin_bit_cast(float, __builtin_amdgcn_update_dpp(0, __builtin_bit_cast(int, v), CTRL, 0xF, 0xF, true)); }
DEV float swap32_add(float a, float b) { auto r = __builtin_amdgcn_permlane32_swap(__builtin_bit_cast(u32, a), __builtin_bit_cast(u32, b), false, false); u32 x = r[0], y = r[1]; return __builtin_bit_cast(float, x) + __builtin_bit_cast(float, y); }
DEV float swap16_add(float a, float b) { auto r = __builtin_amdgcn_permlane16_swap(__builtin_bit_cast(u32, a), __builtin_bit_cast(u32, b), false, false); u32 x = r[0], y = r[1]; return __builtin_bit_cast(float, x) + __builtin_bit_cast(float, y); }
DEV float row_sum(float v) { v = dpp_add<0x128>(v); v = dpp_add<0x124>(v); v = dpp_add<0x122>(v); return dpp_add<0x121>(v); }
DEV float rdlane(float v, int l) { return __builtin_bit_cast(float, __builtin_amdgcn_readlane(__builtin_bit_cast(int, v), l)); }
DEV void glds16(const void* g, u32 lds_base) { u32 sv; asm volatile("s_mov_b32 %0, m0\n\ts_mov_b32 m0, %2\n\ts_nop 0\n\tglobal_load_lds_dwordx4 %1, off\n\ts_mov_b32 m0, %0" : "=&s"(sv) : "v"(g), "s"(lds_base) : "memory"); }
#define SB() __builtin_amdgcn_sched_barrier(0)
#define BAR() do { SB(); asm volatile("s_waitcnt lgkmcnt(0)\n\ts_barrier" ::: "memory"); SB(); } while (0)
#define VM0() do { SB(); asm volatile("s_waitcnt vmcnt(0)" ::: "memory"); SB(); } while (0)
#define LD128(off, so) __builtin_amdgcn_raw_buffer_load_b128(ws, (off), (so), SC1)
#define ST128(v, off, so) __builtin_amdgcn_raw_buffer_store_b128((v), ws, (off), (so), SC1)
#define ST64(v, off, so) __builtin_amdgcn_raw_buffer_store_b64((v), ws, (off), (so), SC1)
#define STAMP(i)

DEV void stats4(const u32x4* v, float* mean, float* rstd) {
    float s[4], q[4];
#pragma unroll
    for (int i = 0; i < 4; ++i) {
        const u32 one = 0x3F803F80u;
        s[i] = dot2(v[i].w, one, dot2(v[i].z, one, dot2(v[i].y, one, dot2(v[i].x, one, 0.f))));
        q[i] = dot2(v[i].w, v[i].w, dot2(v[i].z, v[i].z, dot2(v[i].y, v[i].y, dot2(v[i].x, v[i].x, 0.f))));
    }
    float ss = row_sum(swap16_add(swap32_add(s[0], s[1]), swap32_add(s[2], s[3])));
    float qq = row_sum(swap16_add(swap32_add(q[0], q[1]), swap32_add(q[2], q[3])));
    float m = ss * (1.f / DM), var = fmaxf(qq * (1.f / DM) - m * m, 0.f), rs = __builtin_amdgcn_rsqf(var + 1e-5f);
    mean[0] = rdlane(m, 0); mean[2] = rdlane(m, 16); mean[1] = rdlane(m, 32); mean[3] = rdlane(m, 48);
    rstd[0] = rdlane(rs, 0); rstd[2] = rdlane(rs, 16); rstd[1] = rdlane(rs, 32); rstd[3] = rdlane(rs, 48);
}
DEV f32x8 unpack8(u32x4 v) { return f32x8{bf_lo(v.x), bf_hi(v.x), bf_lo(v.y), bf_hi(v.y), bf_lo(v.z), bf_hi(v.z), bf_lo(v.w), bf_hi(v.w)}; }
DEV u32x4 pack8(f32x8 v) { return u32x4{pk2(v[0], v[1]), pk2(v[2], v[3]), pk2(v[4], v[5]), pk2(v[6], v[7])}; }

#define EX(b, i) "v_exp_f32 v[" #b "+" #i "], v[" #b "+" #i "]\n\t"
#define PV(o, vv, p) "v_smfmac_f32_32x32x32_bf16 v[" #o ":" #o "+15], v[" #vv ":" #vv "+3], v[" #p ":" #p "+7], %[idx]\n\t"
#define QK(s, k, q) "v_mfma_f32_32x32x16_bf16 v[" #s ":" #s "+15], v[" #k ":" #k "+3], v[" #q ":" #q "+3], 0\n\t"
#define TILE(c, n, oprev, vprev, knext, qnext, o, v0, X1, X2) \
    EX(c, 0) EX(c, 1) PV(oprev, vprev, n + 8) EX(c, 2) EX(c, 3) QK(n, knext, qnext) EX(c, 4) EX(c, 5) EX(c, 6) X1 EX(c, 7) EX(c, 8) EX(c, 9) \
    PV(o, v0, c) EX(c, 10) EX(c, 11) EX(c, 12) X2 EX(c, 13) EX(c, 14) EX(c, 15)
#define WRO(o) "ds_write_b128 %[pa], v[" #o ":" #o "+3]\n\tds_write_b128 %[pa], v[" #o "+4:" #o "+7] offset:1152\n\tds_write_b32 %[la], v[" #o "+8]\n\t"
#define ZERO(o) "v_mfma_f32_32x32x8bf16_1k v[" #o ":" #o "+15], %[z], %[z], 0\n\t"
#define STEP(o, op, q, qn, WR) \
    TILE(0, 16, op, 156, 68, q, o, 96, "", "") \
    TILE(16, 0, o, 100, 72, q, o, 104, WR, "ds_read_b128 v[" #qn ":" #qn "+3], %[qa]\n\t") \
    TILE(0, 16, o, 108, 76, q, o, 112, "", "") \
    TILE(16, 0, o, 116, 80, q, o, 120, "", "") \
    TILE(0, 16, o, 124, 84, q, o, 128, "s_waitcnt lgkmcnt(0)\n\t", ZERO(op)) \
    TILE(16, 0, o, 132, 88, q, o, 136, "", "") \
    TILE(0, 16, o, 140, 92, q, o, 144, "", "") \
    TILE(16, 0, o, 148, 64, qn, o, 152, "", "")
#define ATT_OPS(pa_, la_, qa_) \
    : "+{v[0:15]}"(S0), "+{v[16:31]}"(S1), "+{v[32:47]}"(O0), "+{v[48:63]}"(O1), "+{v[160:163]}"(Q0), "+{v[164:167]}"(Q1) \
    : "{v[64:95]}"(Kr), "{v[96:127]}"(Va), "{v[128:159]}"(Vb), [idx] "v"(idx), [z] "v"(z2), [pa] "v"(pa_), [la] "v"(la_), [qa] "v"(qa_) : "memory"

__global__ void __launch_bounds__(512, 2) adaln_block(const u16* __restrict__ x, const u16* __restrict__ wq, const u16* __restrict__ wk, const u16* __restrict__ wv,
                                                      const u16* __restrict__ wo, const u16* __restrict__ w1, const u16* __restrict__ w2, const u16* __restrict__ cvec,
                                                      const u16* __restrict__ wm, u16* __restrict__ out, char* wsp) {
    extern __shared__ __attribute__((aligned(16))) char smem[];
    LAS char* L = (LAS char*)smem;
    const u32 lds0 = (u32)(size_t)L;
    const int tid = threadIdx.x, lane = tid & 63, w = __builtin_amdgcn_readfirstlane(tid >> 6);
    const int r32 = lane & 31, hi = lane >> 5;
    const int b = blockIdx.x, xc = b & 7, rr = b >> 3, g = xc * 4 + (rr >> 3), j = rr & 7, cu = g * 8 + j;
    const __amdgpu_buffer_rsrc_t ws = __builtin_amdgcn_make_buffer_rsrc(wsp, 0, WS_END, 0x00020000);
    const u32x4 poison4 = {POISON, POISON, POISON, POISON};
    STAMP(0);

    if (b < 64 && w < 2) ST128(poison4, WS_K + b * 2048 + w * 1024 + lane * 16, 0);
    ST128(poison4, WS_O + ((g * 2 + (w >> 2)) * 32 + j * 4 + (w & 3)) * 1024 + lane * 16, 0);
    const u32 tag = __builtin_amdgcn_raw_buffer_load_b32(ws, WS_EPOCH, 0, SC1) + 1u;
    const bool modp = rr < 24;
    const int mp = ((rr >> 2) * 8 + xc) * 4 + (rr & 3);
    u32x4 wm0, wm1; u32 c0, c1;
    if (modp) {
        const int row = tid >> 1;
        wm0 = *(const GAS u32x4*)(wm + row * 3072 + mp * 16 + (tid & 1) * 8);
        wm1 = *(const GAS u32x4*)(wm + (row + 256) * 3072 + mp * 16 + (tid & 1) * 8);
        c0 = cvec[row]; c1 = cvec[row + 256];
    }
    SB();
    u32x4 xr[8];
#pragma unroll
    for (int i = 0; i < 8; ++i) xr[i] = *(const GAS u32x4*)(x + (g * 64 + w + 8 * ((i + j) & 7)) * DM + lane * 8);
    SB();
    const int l_kv = L_H + (j < 4 ? 32 * HS : 0);
#pragma unroll
    for (int i = 0; i < 2; ++i) {
        glds16(wk + (w * 2 + i) * 512 + lane * 8, lds0 + l_kv + (w * 2 + i) * 1024);
        glds16(wv + (w * 2 + i) * 512 + lane * 8, lds0 + l_kv + 16384 + (w * 2 + i) * 1024);
    }
#pragma unroll
    for (int i = 0; i < 8; ++i) glds16(wq + ((w * 8 + i) * 8 + (lane >> 3)) * DM + j * 64 + (lane & 7) * 8, lds0 + L_WQ + (w * 8 + i) * 1024);
    const int item = w * 256 + b;
    int pre_dst = 0, pre_step = 0, pre_perm = 1; bool pre_two = true;
    if (item < 1648) {
        const u16* src; int ld, m0, ks;
        if (item < 256) { src = wo; ld = DM; m0 = item & 7; ks = item >> 3; pre_step = 32 * 1024; pre_dst = WS_WO + ks * 1024; }
        else if (item < 944) { const int it = item - 256; src = w2; ld = DM; m0 = it & 7; ks = it >> 3; pre_step = 86 * 1024; pre_dst = WS_W2 + ks * 1024; }
        else { const int it = item - 944; src = w1; ld = FF; m0 = it >> 5; ks = it & 31; pre_step = 32 * 1024; pre_dst = WS_W1 + ks * 1024; pre_perm = 0; pre_two = m0 < 21; }
        pre_dst += 2 * m0 * pre_step;
        const int ch = pre_two ? (lane & 7) : (lane & 3);
#pragma unroll
        for (int i = 0; i < 2; ++i) glds16(src + (ks * 16 + i * 8 + (lane >> 3)) * ld + m0 * 64 + ch * 8, lds0 + L_PRE + w * 2048 + i * 1024);
    }
    SB();
    STAMP(1);
    if (modp) {
        const float a0 = bf_lo(c0), a1 = bf_lo(c1);
        const u32 sp = pk2(a0 * __builtin_amdgcn_rcpf(1.f + __builtin_amdgcn_exp2f(-LOG2E * a0)), a1 * __builtin_amdgcn_rcpf(1.f + __builtin_amdgcn_exp2f(-LOG2E * a1)));
        f32x8 acc = unpack8(wm0) * bf_lo(sp) + unpack8(wm1) * bf_hi(sp);
#pragma unroll
        for (int i = 0; i < 8; ++i) { float t = acc[i]; t = dpp_add<0x128>(t); t = dpp_add<0x124>(t); acc[i] = dpp_add<0x122>(t); }
        if ((lane & 14) == 0) {
            LAS f32x4* p = (LAS f32x4*)(L + L_RED + (w * 4 + (lane >> 4)) * 64 + (lane & 1) * 32);
            p[0] = f32x4{acc[0], acc[1], acc[2], acc[3]}; p[1] = f32x4{acc[4], acc[5], acc[6], acc[7]};
        }
    }
    BAR();
    if (modp && w == 0) {
        float t = 0.f;
#pragma unroll
        for (int i = 0; i < 8; ++i) t += *(LAS float*)(L + L_RED + ((lane >> 4) * 8 + i) * 64 + (lane & 15) * 4);
        t = swap32_add(t, t); t = swap16_add(t, t);
        const float nb = __builtin_bit_cast(float, __builtin_amdgcn_update_dpp(0, __builtin_bit_cast(int, t), 0xB1, 0xF, 0xF, true));
        if (lane < 16 && !(lane & 1)) ST64((u32x2{pk2(t, nb), tag}), WS_MOD + mp * 128 + lane * 4, 0);
    }
    STAMP(2);
    f32x8 n1[8];
    {
        float mean[8], rstd[8];
        stats4(xr, mean, rstd); stats4(xr + 4, mean + 4, rstd + 4);
#pragma unroll
        for (int i = 0; i < 8; ++i) n1[i] = unpack8(xr[i]) * rstd[i] - mean[i] * rstd[i];
    }
    VM0();
    STAMP(3);
    if (item < 1648) {
        const int row = (pre_perm ? 4 * hi : 8 * hi) + ((lane & 15) >> 2), rs = pre_perm ? 8 : 4;
        LAS char* p = L + L_PRE + w * 2048 + row * 128 + ((lane >> 4) & 1) * 32 + (lane & 3) * 8;
        const u32x4 f0 = cat(trread(p), trread(p + rs * 128)), f1 = cat(trread(p + 64), trread(p + 64 + rs * 128));
        ST128(f0, pre_dst + lane * 16, 0);
        if (pre_two) ST128(f1, pre_dst + pre_step + lane * 16, 0);
    }
    BAR();
    u32x4 wf[16];
    {
        LAS char* p = L + L_WQ + ((w >> 2) * 256 + 8 * hi + ((lane & 15) >> 2)) * 128 + (w & 1) * 64 + ((lane >> 4) & 1) * 32 + (lane & 3) * 8;
#pragma unroll
        for (int s = 0; s < 16; ++s) wf[s] = cat(trread(p + s * 2048), trread(p + s * 2048 + 512));
    }
    u32x4 kf[16];
    if (w < 2) {
        LAS char* p = L + l_kv + w * 16384 + (8 * (lane >> 4) + ((lane & 15) >> 2)) * 32 + (lane & 3) * 8;
#pragma unroll
        for (int s = 0; s < 16; ++s) kf[s] = cat(trread(p + s * 1024), trread(p + s * 1024 + 128));
    }
    STAMP(4);
    {
        const int G = w * 64 + lane, off = WS_MOD + (G >> 3) * 128 + (G & 7) * 8;
        u32x2 v;
        for (int it = 0; it < SPIN; ++it) {
            asm volatile("" ::: "memory");
            v = __builtin_amdgcn_raw_buffer_load_b64(ws, off, 0, SC1);
            if (__all(v.y == tag)) break;
        }
        *(LAS u32*)(L + L_MOD + G * 4) = v.x;
    }
    VM0();
    BAR();
    STAMP(5);
    const f32x8 sa = unpack8(*(LAS u32x4*)(L + L_MOD + lane * 16)), ca = unpack8(*(LAS u32x4*)(L + L_MOD + 1024 + lane * 16)) + 1.f;
    *(LAS u32x4*)(L + L_H + (w + 8 * j) * HS + lane * 16) = pack8(n1[0] * ca + sa);
    BAR();
    if (w < 2) {
        f32x4 acc = {0.f, 0.f, 0.f, 0.f};
        LAS char* p = L + L_H + (8 * j + (lane & 7)) * HS + (lane >> 4) * 16;
        const int mg = lane >> 4, nn = lane & 15;
        if (w == 0) {
#pragma unroll
            for (int s = 0; s < 16; ++s) acc = mfma16(kf[s], *(LAS u32x4*)(p + s * 64), acc);
            if (nn < 8) ST64((u32x2{clean(pk2(acc[0], acc[1])), clean(pk2(acc[2], acc[3]))}), WS_K + (cu * 8 + nn) * 32 + (mg & 1) * 16 + (mg >> 1) * 8, 0);
        } else {
#pragma unroll
            for (int s = 0; s < 16; ++s) acc = mfma16(*(LAS u32x4*)(p + s * 64), kf[s], acc);
            if (mg < 2) ST64((u32x2{clean(pk2(acc[0], acc[1])), clean(pk2(acc[2], acc[3]))}), WS_V + cu * 256 + nn * 16 + mg * 8, 0);
        }
    }
    STAMP(6);
#pragma unroll
    for (int i = 1; i < 8; ++i) *(LAS u32x4*)(L + L_H + (w + 8 * ((i + j) & 7)) * HS + lane * 16) = pack8(n1[i] * ca + sa);
    BAR();
    STAMP(7);
    u32x4 kq[8], vq[16];
    const int koff = WS_K + w * 8192 + r32 * 32 + hi * 16, voff = WS_V + w * 8192 + hi * 256 + r32 * 16;
#pragma unroll
    for (int i = 0; i < 16; ++i) vq[i] = u32x4{0x3F803F80u, 0x3F803F80u, 0x3F803F80u, 0x3F803F80u};
#pragma unroll
    for (int i = 0; i < 8; ++i) kq[i] = LD128(koff + i * 1024, 0);
    if (r32 < 16) {
#pragma unroll
        for (int i = 0; i < 16; ++i) vq[i] = LD128(voff + i * 512, 0);
    }
    SB();
    {
        f32x16 acc = {};
        LAS char* p = L + L_H + (((w >> 1) & 1) * 32 + r32) * HS + (w >> 2) * 512 + hi * 16;
#pragma unroll
        for (int s = 0; s < 16; ++s) acc = mfma32(wf[s], *(LAS u32x4*)(p + s * 32), acc);
        const int kh = w >> 2;
        f32x8 keep, send;
#pragma unroll
        for (int i = 0; i < 8; ++i) { keep[i] = kh ? acc[8 + i] : acc[i]; send[i] = kh ? acc[i] : acc[8 + i]; }
        LAS f32x4* xq = (LAS f32x4*)(L + L_PRE + w * 2048 + lane * 16);
        xq[0] = f32x4{send[0], send[1], send[2], send[3]}; xq[64] = f32x4{send[4], send[5], send[6], send[7]};
        BAR();
        LAS f32x4* yq = (LAS f32x4*)(L + L_PRE + (w ^ 4) * 2048 + lane * 16);
        const f32x4 y0 = yq[0], y1 = yq[64];
        keep += f32x8{y0[0], y0[1], y0[2], y0[3], y1[0], y1[1], y1[2], y1[3]};
        *(LAS u32x4*)(L + L_Q + (((w >> 1) & 1) * 4 + (w & 1) * 2 + kh) * 1024 + lane * 16) = pack8(keep * (0.25f * LOG2E));
    }
    STAMP(8);
    for (int it = 0; it < SPIN; ++it) {
        u32 m = 0;
#pragma unroll
        for (int i = 0; i < 8; ++i) m = pmax(m, kq[i]);
#pragma unroll
        for (int i = 0; i < 16; ++i) m = pmax(m, vq[i]);
        if (!__any(m == POISON)) break;
        asm volatile("" ::: "memory");
#pragma unroll
        for (int i = 0; i < 8; ++i) kq[i] = LD128(koff + i * 1024, 0);
        if (r32 < 16) {
#pragma unroll
            for (int i = 0; i < 16; ++i) vq[i] = LD128(voff + i * 512, 0);
        }
    }
    BAR();
    STAMP(9);
    if (b == 0 && tid == 0) __builtin_amdgcn_raw_buffer_store_b32(tag, ws, WS_EPOCH, 0, SC1);
    const int T0 = j < 3 ? 6 * j : 5 * j + 3, NT = j < 3 ? 6 : 5;
    ST128(poison4, WS_X1 + (g * 64 + w * 8 + (lane >> 3)) * 1024 + j * 128 + (lane & 7) * 16, 0);
#pragma unroll
    for (int i = 0; i < 3; ++i) {
        const int p = w * 3 + i, hf = p >= 12, ks = p - hf * 12;
        if (ks < 2 * NT) ST128(poison4, WS_H1 + ((g * 2 + hf) * 86 + 2 * T0 + ks) * 1024 + lane * 16, 0);
    }
    {
        const int G = 512 + w * 64 + lane, off = WS_MOD + (G >> 3) * 128 + (G & 7) * 8;
        u32x2 v0, v1;
        for (int it = 0; it < SPIN; ++it) {
            asm volatile("" ::: "memory");
            v0 = __builtin_amdgcn_raw_buffer_load_b64(ws, off, 0, SC1);
            v1 = __builtin_amdgcn_raw_buffer_load_b64(ws, off + 8192, 0, SC1);
            if (__all(v0.y == tag && v1.y == tag)) break;
        }
        *(LAS u32*)(L + L_MOD + G * 4) = v0.x; *(LAS u32*)(L + L_MOD + 2048 + G * 4) = v1.x;
    }
    u32x4 wof[8], w1f[24];
    const int wo_off = WS_WO + ((2 * j + (w & 1)) * 32 + (w >> 1) * 8) * 1024 + lane * 16;
#pragma unroll
    for (int i = 0; i < 8; ++i) wof[i] = LD128(wo_off + i * 1024, 0);
    int w1_off[3];
#pragma unroll
    for (int a = 0; a < 3; ++a) {
        const int tl = w < 2 ? 3 * w + a : w - 2, ks = w < 2 ? 24 : 8 * a;
        w1_off[a] = (tl < NT ? WS_W1 + ((T0 + tl) * 32 + ks) * 1024 : OOB) + lane * 16;
    }
#pragma unroll
    for (int i = 0; i < 6; ++i) w1f[i] = LD128(w1_off[0] + i * 1024, 0);
    SB();

    u32x32 Kr, Va, Vb;
#pragma unroll
    for (int i = 0; i < 8; ++i)
#pragma unroll
        for (int e = 0; e < 4; ++e) { Kr[4 * i + e] = kq[i][e]; Va[4 * i + e] = vq[i][e]; Vb[4 * i + e] = vq[8 + i][e]; }
    const u32 idx = 0xDDDDDDDDu;
    const u32x2 z2 = {0u, 0u};
    u32x4 of[8];
    const int o_off = WS_O + (g * 64 + (w >> 1) * 8) * 1024 + lane * 16;
    const int tk = w * 4 + (lane >> 4), nq = lane & 15;
    u32x2 xres[2];
#pragma unroll 1
    for (int bt = 0; bt < 2; ++bt) {
        f32x16 S0, S1 = {}, O0 = {}, O1 = {};
        u32x4 Q0 = *(LAS u32x4*)(L + L_Q + bt * 4096 + lane * 16), Q1 = {};
        S0 = mfma32(kq[0], Q0, f32x16{});
        const u32 pa = lds0 + w * PW + lane * 16, la = lds0 + 8 * PW + r32 * 48 + w * 4, qa = lds0 + L_Q + bt * 4096 + lane * 16;
        asm volatile("s_nop 7\n\ts_nop 7\n\t" STEP(32, 48, 160, 164, "") ATT_OPS(pa, la, qa + 1024));
        if (bt) BAR();
        asm volatile(STEP(48, 32, 164, 160, WRO(32)) ATT_OPS(pa, la, qa + 2048));
        asm volatile(STEP(32, 48, 160, 164, WRO(48)) ATT_OPS(pa + PSL, la + PSL, qa + 3072));
        asm volatile(STEP(48, 32, 164, 160, WRO(32)) ATT_OPS(pa + 2 * PSL, la + 2 * PSL, qa + 3072));
        asm volatile("s_nop 1\n\t" PV(48, 156, 24) "s_nop 7\n\ts_nop 7\n\t" WRO(48) ATT_OPS(pa + 3 * PSL, la + 3 * PSL, qa));
        if (bt == 0) VM0();
        BAR();
        STAMP(10 + 2 * bt);
        {
            const int sl = w >> 1, q = lane >> 1, rg = lane & 1;
            LAS char* p = L + sl * PSL + rg * PPL + ((w & 1) * 32 + q) * 16;
            f32x4 o = *(LAS f32x4*)p;
#pragma unroll
            for (int i = 1; i < 8; ++i) o += *(LAS f32x4*)(p + i * PW);
            const f32x4 l4 = *(LAS f32x4*)(L + sl * PSL + 8 * PW + q * 48) + *(LAS f32x4*)(L + sl * PSL + 8 * PW + q * 48 + 16);
            const float l = (l4[0] + l4[1]) + (l4[2] + l4[3]);
            const float am = fmaxf(fmaxf(fabsf(o[0]), fabsf(o[1])), fmaxf(fabsf(o[2]), fabsf(o[3])));
            const float sm = (o[0] + o[1]) + (o[2] + o[3]);
            const int dst = WS_O + ((g * 2 + bt) * 32 + j * 4 + sl) * 1024;
            const bool ok = l > 0x1p-60f && l < 0x1p100f && am < 0x1p120f && sm == sm;
            if (__builtin_expect(__all(ok), 1)) {
                o *= __builtin_amdgcn_rcpf(l);
                ST64((u32x2{clean(pk2(o[0], o[1])), clean(pk2(o[2], o[3]))}), dst + (w & 1) * 512 + lane * 8, 0);
            } else {
                const u32x4 Q = *(LAS u32x4*)(L + L_Q + (bt * 4 + sl) * 1024 + lane * 16);
                f32x16 O = {}; float m = -3e38f;
#pragma unroll 1
                for (int t = 0; t < 64; ++t) {
                    const u32x4 kk = LD128(WS_K + t * 1024 + r32 * 32 + hi * 16, 0);
                    u32x4 v0 = {0x3F803F80u, 0x3F803F80u, 0x3F803F80u, 0x3F803F80u}, v1 = v0;
                    if (r32 < 16) { v0 = LD128(WS_V + t * 1024 + hi * 256 + r32 * 16, 0); v1 = LD128(WS_V + t * 1024 + 512 + hi * 256 + r32 * 16, 0); }
                    f32x16 s = mfma32(kk, Q, f32x16{});
                    float tm = m;
#pragma unroll
                    for (int i = 0; i < 16; ++i) tm = fmaxf(tm, s[i]);
                    auto sw = __builtin_amdgcn_permlane32_swap(__builtin_bit_cast(u32, tm), __builtin_bit_cast(u32, tm), false, false);
                    const u32 sw0 = sw[0], sw1 = sw[1];
                    tm = fmaxf(__builtin_bit_cast(float, sw0), __builtin_bit_cast(float, sw1));
                    O *= __builtin_amdgcn_exp2f(m - tm);
                    m = tm;
#pragma unroll
                    for (int i = 0; i < 16; ++i) s[i] = __builtin_amdgcn_exp2f(s[i] - m);
                    O = __builtin_amdgcn_smfmac_f32_32x32x32_bf16(__builtin_bit_cast(bf16x8, v0), __builtin_bit_cast(bf16x16, __builtin_shufflevector(s, s, 0, 1, 2, 3, 4, 5, 6, 7)), O, (int)idx, 0, 0);
                    O = __builtin_amdgcn_smfmac_f32_32x32x32_bf16(__builtin_bit_cast(bf16x8, v1), __builtin_bit_cast(bf16x16, __builtin_shufflevector(s, s, 8, 9, 10, 11, 12, 13, 14, 15)), O, (int)idx, 0, 0);
                }
                const float inv = __builtin_amdgcn_rcpf(O[8]);
                const u32x4 r = {clean(pk2(O[0] * inv, O[1] * inv)), clean(pk2(O[2] * inv, O[3] * inv)), clean(pk2(O[4] * inv, O[5] * inv)), clean(pk2(O[6] * inv, O[7] * inv))};
                if (hi == (w & 1)) ST128(r, dst + lane * 16, 0);
            }
        }
        STAMP(11 + 2 * bt);
    }

#define LOAD_O(hf) _Pragma("unroll") for (int i = 0; i < 8; ++i) of[i] = LD128(o_off + (hf) * 32768 + i * 1024, 0)
#define VALID(N, arr, RELOAD) _Pragma("unroll 1") for (int it = 0; it < SPIN; ++it) { u32 m = 0; _Pragma("unroll") for (int i = 0; i < N; ++i) m = pmax(m, arr[i]); if (!__any(m == POISON)) break; asm volatile("" ::: "memory"); RELOAD; }
#define WRITE_TILE(base, acc) _Pragma("unroll") for (int q = 0; q < 4; ++q) *(LAS f32x4*)((base) + q * 32) = f32x4{acc[4 * q], acc[4 * q + 1], acc[4 * q + 2], acc[4 * q + 3]}
#define WO_GEMM(hf, lp) { VALID(8, of, LOAD_O(hf)); f32x16 acc = {}; _Pragma("unroll") for (int i = 0; i < 8; ++i) acc = mfma32(wof[i], of[i], acc); \
        WRITE_TILE(L + (lp) + (w >> 1) * TP + r32 * TS + (w & 1) * 128 + hi * 16, acc); }
#define X1_FIN(hf, lp) { LAS char* p = L + (lp) + tk * TS + nq * 16; \
        const f32x4 a = (*(LAS f32x4*)p + *(LAS f32x4*)(p + TP)) + (*(LAS f32x4*)(p + 2 * TP) + *(LAS f32x4*)(p + 3 * TP)); \
        const u32x2 xv = xres[hf]; \
        xres[hf] = u32x2{clean(pk2(bf_lo(xv.x) + ga[0] * a[0], bf_hi(xv.x) + ga[1] * a[1])), clean(pk2(bf_lo(xv.y) + ga[2] * a[2], bf_hi(xv.y) + ga[3] * a[3]))}; \
        ST64(xres[hf], WS_X1 + (g * 64 + (hf) * 32 + tk) * 1024 + j * 128 + nq * 8, 0); }
#define LOAD_X1(hf, arr) _Pragma("unroll") for (int i = 0; i < 4; ++i) arr[i] = LD128(WS_X1 + (g * 64 + (hf) * 32 + w * 4 + i) * 1024 + lane * 16, 0)
#define LN2(hf, arr, lp) { VALID(4, arr, LOAD_X1(hf, arr)); float mean[4], rstd[4]; stats4(arr, mean, rstd); \
        _Pragma("unroll") for (int i = 0; i < 4; ++i) *(LAS u32x4*)(L + (lp) + (w * 4 + i) * HS + lane * 16) = pack8((unpack8(arr[i]) * rstd[i] - mean[i] * rstd[i]) * cb + sb); }
#define FFN1(lh, lp, acc) { LAS char* p = L + (lh) + r32 * HS + hi * 16; \
        if (w < 2) { u32x4 bq[8]; _Pragma("unroll") for (int s = 0; s < 8; ++s) bq[s] = *(LAS u32x4*)(p + (24 + s) * 32); \
            _Pragma("unroll") for (int a = 0; a < 3; ++a) { acc = f32x16{}; _Pragma("unroll") for (int s = 0; s < 8; ++s) acc = mfma32(w1f[8 * a + s], bq[s], acc); \
                _Pragma("unroll") for (int q = 0; q < 4; ++q) *(LAS f32x4*)(L + (lp) + (3 * w + a) * 4096 + q * 1024 + lane * 16) = f32x4{acc[4 * q], acc[4 * q + 1], acc[4 * q + 2], acc[4 * q + 3]}; } \
        } else { acc = f32x16{}; _Pragma("unroll") for (int s = 0; s < 24; ++s) acc = mfma32(w1f[s], *(LAS u32x4*)(p + s * 32), acc); } }
#define SILU(hf, lp, acc) if (w >= 2 && w - 2 < NT) { u32 pk[8]; \
        _Pragma("unroll") for (int q = 0; q < 4; ++q) { const f32x4 t = *(LAS f32x4*)(L + (lp) + (w - 2) * 4096 + q * 1024 + lane * 16); float y[4]; \
            _Pragma("unroll") for (int e = 0; e < 4; ++e) { const float z = acc[4 * q + e] + t[e]; y[e] = z * __builtin_amdgcn_rcpf(1.f + __builtin_amdgcn_exp2f(-z)); } \
            pk[2 * q] = clean(pk2(y[0], y[1])); pk[2 * q + 1] = clean(pk2(y[2], y[3])); } \
        const int dst = WS_H1 + ((g * 2 + (hf)) * 86 + 2 * (T0 + w - 2)) * 1024 + lane * 16; \
        ST128((u32x4{pk[0], pk[1], pk[2], pk[3]}), dst, 0); ST128((u32x4{pk[4], pk[5], pk[6], pk[7]}), dst + 1024, 0); }
#define LOAD_H1(hf, arr) _Pragma("unroll") for (int i = 0; i < 11; ++i) arr[i] = LD128((i >= 9 ? h1_off2 : h1_off) + (hf) * 86 * 1024 + i * 1024, 0)
#define FFN2(hf, arr, lp) { VALID(11, arr, LOAD_H1(hf, arr)); f32x16 a0 = {}, a1 = {}; \
        _Pragma("unroll") for (int i = 0; i < 11; ++i) { a0 = mfma32(w2f[i], arr[i], a0); a1 = mfma32(w2f[11 + i], arr[i], a1); } \
        WRITE_TILE(L + (lp) + w * TP + r32 * TS + hi * 16, a0); WRITE_TILE(L + (lp) + w * TP + r32 * TS + 128 + hi * 16, a1); }
#define FINAL(hf, lp) { LAS char* p = L + (lp) + tk * TS + nq * 16; f32x4 a = *(LAS f32x4*)p; \
        _Pragma("unroll") for (int i = 1; i < 8; ++i) a += *(LAS f32x4*)(p + i * TP); \
        const u32x2 xv = xres[hf]; \
        *(GAS u32x2*)(out + (g * 64 + (hf) * 32 + tk) * DM + j * 64 + nq * 4) = u32x2{pk2(bf_lo(xv.x) + gb[0] * a[0], bf_hi(xv.x) + gb[1] * a[1]), pk2(bf_lo(xv.y) + gb[2] * a[2], bf_hi(xv.y) + gb[3] * a[3])}; }

    LOAD_O(0);
#pragma unroll
    for (int hf = 0; hf < 2; ++hf) xres[hf] = *(const GAS u32x2*)(x + (g * 64 + hf * 32 + tk) * DM + j * 64 + nq * 4);
#pragma unroll
    for (int i = 6; i < 24; ++i) w1f[i] = LD128(w1_off[i >> 3] + (i & 7) * 1024, 0);
    SB();
    f32x4 ga, gb;
    { const u32x2 t = *(LAS u32x2*)(L + L_MOD + 2048 + j * 128 + nq * 8); ga = f32x4{bf_lo(t.x), bf_hi(t.x), bf_lo(t.y), bf_hi(t.y)}; }
    { const u32x2 t = *(LAS u32x2*)(L + L_MOD + 5120 + j * 128 + nq * 8); gb = f32x4{bf_lo(t.x), bf_hi(t.x), bf_lo(t.y), bf_hi(t.y)} * LN_2; }
    const f32x8 sb = unpack8(*(LAS u32x4*)(L + L_MOD + 3072 + lane * 16)) * LOG2E, cb = (unpack8(*(LAS u32x4*)(L + L_MOD + 4096 + lane * 16)) + 1.f) * LOG2E;
    WO_GEMM(0, L_WPA);
    BAR();
    STAMP(14);
    LOAD_O(1);
    SB();
    X1_FIN(0, L_WPA);
    WO_GEMM(1, L_WPB);
    VM0();
    BAR();
    STAMP(15);
    u32x4 xa[4], xb[4];
    LOAD_X1(0, xa);
    SB();
    X1_FIN(1, L_WPB);
    u32x4 w2f[22];
    const int w2_off = WS_W2 + (2 * j * 86 + 11 * w) * 1024 + lane * 16, w2_off2 = w == 7 ? OOB : w2_off;
    const int h1_off = WS_H1 + (g * 2 * 86 + 11 * w) * 1024 + lane * 16, h1_off2 = w == 7 ? OOB : h1_off;
    VM0();
    STAMP(16);
    LOAD_X1(1, xb);
#pragma unroll
    for (int i = 0; i < 11; ++i) w2f[i] = LD128((i >= 9 ? w2_off2 : w2_off) + i * 1024, 0);
    SB();
    LN2(0, xa, L_H2A);
    BAR();
    STAMP(17);
    f32x16 accA, accB;
    FFN1(L_H2A, L_F1A, accA);
    LN2(1, xb, L_H2B);
    BAR();
    STAMP(18);
    SILU(0, L_F1A, accA);
    SB();
#pragma unroll
    for (int i = 0; i < 11; ++i) w2f[11 + i] = LD128((i >= 9 ? w2_off2 : w2_off) + (86 + i) * 1024, 0);
    SB();
    FFN1(L_H2B, L_F1B, accB);
    BAR();
    STAMP(19);
    u32x4 ha[11], hb[11];
    LOAD_H1(0, ha);
    SB();
    SILU(1, L_F1B, accB);
    VM0();
    BAR();
    STAMP(20);
    LOAD_H1(1, hb);
    SB();
    FFN2(0, ha, L_F2A);
    BAR();
    STAMP(21);
    FINAL(0, L_F2A);
    FFN2(1, hb, L_F2B);
    BAR();
    STAMP(22);
    FINAL(1, L_F2B);
    STAMP(23);
}

template <int BIGV> __global__ void __launch_bounds__(512, 2) node0(u64* t) {
    const u64 now = __builtin_amdgcn_s_memrealtime();
    if (BIGV) asm volatile("v_mov_b32 v255, 0" ::: "v255");
    if (threadIdx.x == 0) t[blockIdx.x] = now;
}
template <int BIGV> __global__ void __launch_bounds__(512, 2) node2(const u64* t, int n, int mode) {
    if (BIGV) asm volatile("v_mov_b32 v255, 0" ::: "v255");
    u64 t0 = t[0];
    for (int i = 1; i < n; ++i) { const u64 v = t[i]; if (mode == 1 && v < t0) t0 = v; if (mode == 2 && v > t0) t0 = v; }
    while (__builtin_amdgcn_s_memrealtime() < t0 + 20000) __builtin_amdgcn_s_sleep(1);
}
extern "C" void kernel_launch(void* const* d_in, const int* in_sizes, int n_in, void* d_out, int out_size, void* d_ws, size_t ws_size, hipStream_t stream) {
    (void)hipFuncSetAttribute((const void*)adaln_block, hipFuncAttributeMaxDynamicSharedMemorySize, L_END);
    (void)hipFuncSetAttribute((const void*)node0<1>, hipFuncAttributeMaxDynamicSharedMemorySize, L_END);
    (void)hipFuncSetAttribute((const void*)node2<0>, hipFuncAttributeMaxDynamicSharedMemorySize, L_END);
    node0<1><<<256, 512, L_END, stream>>>((u64*)((char*)d_ws + WS_STAMP));
    adaln_block<<<256, 512, L_END, stream>>>((const u16*)d_in[0], (const u16*)d_in[1], (const u16*)d_in[2], (const u16*)d_in[3], (const u16*)d_in[4],
                                              (const u16*)d_in[6], (const u16*)d_in[7], (const u16*)d_in[8], (const u16*)d_in[9], (u16*)d_out, (char*)d_ws);
    node2<0><<<1, 64, 0, stream>>>((const u64*)((char*)d_ws + WS_STAMP), 256, 1);
}
```

```cpp
#define PROBE 0
#define PROBE_B 0
#define PEXPR pt3 - pt2
#include <hip/hip_runtime.h>
#include <hip/hip_bf16.h>

typedef unsigned u32;
typedef unsigned short u16;
typedef unsigned long long u64;
typedef u32 u32x2 __attribute__((ext_vector_type(2)));
typedef u32 u32x4 __attribute__((ext_vector_type(4)));
typedef u32 u32x32 __attribute__((ext_vector_type(32)));
typedef float f32x2 __attribute__((ext_vector_type(2)));
typedef float f32x4 __attribute__((ext_vector_type(4)));
typedef float f32x8 __attribute__((ext_vector_type(8)));
typedef float f32x16 __attribute__((ext_vector_type(16)));
typedef __bf16 bf16x2 __attribute__((ext_vector_type(2)));
typedef __bf16 bf16x8 __attribute__((ext_vector_type(8)));
typedef __bf16 bf16x16 __attribute__((ext_vector_type(16)));
typedef short s16x4 __attribute__((ext_vector_type(4)));
#define LAS __attribute__((address_space(3)))
#define GAS __attribute__((address_space(1)))
#define DEV __device__ __forceinline__
#define STAMPS 0

constexpr int SEQ = 2048, DM = 512, FF = 1376;
constexpr int WS_EPOCH = 0;
constexpr int WS_MOD = 1024;
constexpr int WS_K = 32768, WS_V = WS_K + 65536;
constexpr int WS_O = 196608;
constexpr int WS_X1 = WS_O + 2097152;
constexpr int WS_H1 = WS_X1 + 2097152;
constexpr int WS_WO = WS_H1 + 64 * 86 * 1024;
constexpr int WS_W1 = WS_WO + 512 * 1024;
constexpr int WS_W2 = WS_W1 + 43 * 32 * 1024;
constexpr int WS_END = WS_W2 + 16 * 86 * 1024;
constexpr int WS_STAMP = 16 * 1024 * 1024;
constexpr int OOB = 0x40000000;
constexpr int SC1 = 16;
constexpr u32 POISON = 0xFFFFFFFFu;
constexpr int SPIN = 1 << 20;
constexpr int HS = 1040;
constexpr int L_H = 0, L_WQ = 66560, L_PRE = 132096, L_Q = 149504, L_MOD = 157696, L_END = 163840;
constexpr int L_RED = L_Q;
constexpr int PPL = 1152, PW = 2 * PPL, PSL = 8 * PW + 32 * 48;
constexpr int TS = 272, TP = 32 * TS;
constexpr int L_WPA = 80000, L_WPB = 0;
constexpr int L_H2A = 36864, L_H2B = 80000;
constexpr int L_F1A = 116736, L_F1B = 0;
constexpr int L_F2A = 0, L_F2B = 8 * TP;
static_assert(4 * PSL <= L_WPA && L_WPA + 4 * TP <= L_F1A && L_F1A + 24576 <= L_Q && 4 * TP <= L_H2A && L_H2A + 32 * HS <= L_H2B && L_H2B + 32 * HS <= L_F1A && 16 * TP <= L_Q, "lds map");

constexpr float LOG2E = 1.4426950408889634f, LN_2 = 0.6931471805599453f;

DEV float bf_lo(u32 x) { return __builtin_bit_cast(float, x << 16); }
DEV float bf_hi(u32 x) { return __builtin_bit_cast(float, x & 0xFFFF0000u); }
DEV u32 pk2(float a, float b) { f32x2 v = {a, b}; return __builtin_bit_cast(u32, __builtin_convertvector(v, bf16x2)); }
DEV u32 clean(u32 x) { return x < 0xFFFFFFFEu ? x : 0xFFFFFFFEu; }
DEV float dot2(u32 a, u32 b, float c) { return __builtin_amdgcn_fdot2_f32_bf16(__builtin_bit_cast(bf16x2, a), __builtin_bit_cast(bf16x2, b), c, false); }
DEV u32 umax3(u32 a, u32 b, u32 c) { return max(max(a, b), c); }
DEV u32 pmax(u32 m, u32x4 v) { return umax3(umax3(m, v.x, v.y), v.z, v.w); }
DEV f32x16 mfma32(u32x4 a, u32x4 b, f32x16 c) { return __builtin_amdgcn_mfma_f32_32x32x16_bf16(__builtin_bit_cast(bf16x8, a), __builtin_bit_cast(bf16x8, b), c, 0, 0, 0); }
DEV f32x4 mfma16(u32x4 a, u32x4 b, f32x4 c) { return __builtin_amdgcn_mfma_f32_16x16x32_bf16(__builtin_bit_cast(bf16x8, a), __builtin_bit_cast(bf16x8, b), c, 0, 0, 0); }
DEV u32x2 trread(LAS char* p) { return __builtin_bit_cast(u32x2, __builtin_amdgcn_ds_read_tr16_b64_v4i16((LAS s16x4*)p)); }
DEV u32x4 cat(u32x2 a, u32x2 b) { return u32x4{a.x, a.y, b.x, b.y}; }
template <int CTRL> DEV float dpp_add(float v) { return v + __builtin_bit_cast(float, __builtin_amdgcn_update_dpp(0, __builtin_bit_cast(int, v), CTRL, 0xF, 0xF, true)); }
DEV float swap32_add(float a, float b) { auto r = __builtin_amdgcn_permlane32_swap(__builtin_bit_cast(u32, a), __builtin_bit_cast(u32, b), false, false); u32 x = r[0], y = r[1]; return __builtin_bit_cast(float, x) + __builtin_bit_cast(float, y); }
DEV float swap16_add(float a, float b) { auto r = __builtin_amdgcn_permlane16_swap(__builtin_bit_cast(u32, a), __builtin_bit_cast(u32, b), false, false); u32 x = r[0], y = r[1]; return __builtin_bit_cast(float, x) + __builtin_bit_cast(float, y); }
DEV float row_sum(float v) { v = dpp_add<0x128>(v); v = dpp_add<0x124>(v); v = dpp_add<0x122>(v); return dpp_add<0x121>(v); }
DEV float rdlane(float v, int l) { return __builtin_bit_cast(float, __builtin_amdgcn_readlane(__builtin_bit_cast(int, v), l)); }
DEV void glds16(const void* g, u32 lds_base) { u32 sv; asm volatile("s_mov_b32 %0, m0\n\ts_mov_b32 m0, %2\n\ts_nop 0\n\tglobal_load_lds_dwordx4 %1, off\n\ts_mov_b32 m0, %0" : "=&s"(sv) : "v"(g), "s"(lds_base) : "memory"); }
#define SB() __builtin_amdgcn_sched_barrier(0)
#define BAR() do { SB(); asm volatile("s_waitcnt lgkmcnt(0)\n\ts_barrier" ::: "memory"); SB(); } while (0)
#define VM0() do { SB(); asm volatile("s_waitcnt vmcnt(0)" ::: "memory"); SB(); } while (0)
#define LD128(off, so) __builtin_amdgcn_raw_buffer_load_b128(ws, (off), (so), SC1)
#define ST128(v, off, so) __builtin_amdgcn_raw_buffer_store_b128((v), ws, (off), (so), SC1)
#define ST64(v, off, so) __builtin_amdgcn_raw_buffer_store_b64((v), ws, (off), (so), SC1)
#define STAMP(i) do { if ((i) == 0) { tp0 = __builtin_amdgcn_s_memrealtime(); tpx = tp0; } else if ((i) == PROBE) { SB(); tpx = __builtin_amdgcn_s_memrealtime(); SB(); } if ((i) == 23 && blockIdx.x == PROBE_B && w == 0) { while (__builtin_amdgcn_s_memrealtime() < pt0 + 10000 + 10 * (PEXPR)) __builtin_amdgcn_s_sleep(1); } } while (0)

DEV void stats4(const u32x4* v, float* mean, float* rstd) {
    float s[4], q[4];
#pragma unroll
    for (int i = 0; i < 4; ++i) {
        const u32 one = 0x3F803F80u;
        s[i] = dot2(v[i].w, one, dot2(v[i].z, one, dot2(v[i].y, one, dot2(v[i].x, one, 0.f))));
        q[i] = dot2(v[i].w, v[i].w, dot2(v[i].z, v[i].z, dot2(v[i].y, v[i].y, dot2(v[i].x, v[i].x, 0.f))));
    }
    float ss = row_sum(swap16_add(swap32_add(s[0], s[1]), swap32_add(s[2], s[3])));
    float qq = row_sum(swap16_add(swap32_add(q[0], q[1]), swap32_add(q[2], q[3])));
    float m = ss * (1.f / DM), var = fmaxf(qq * (1.f / DM) - m * m, 0.f), rs = __builtin_amdgcn_rsqf(var + 1e-5f);
    mean[0] = rdlane(m, 0); mean[2] = rdlane(m, 16); mean[1] = rdlane(m, 32); mean[3] = rdlane(m, 48);
    rstd[0] = rdlane(rs, 0); rstd[2] = rdlane(rs, 16); rstd[1] = rdlane(rs, 32); rstd[3] = rdlane(rs, 48);
}
DEV f32x8 unpack8(u32x4 v) { return f32x8{bf_lo(v.x), bf_hi(v.x), bf_lo(v.y), bf_hi(v.y), bf_lo(v.z), bf_hi(v.z), bf_lo(v.w), bf_hi(v.w)}; }
DEV u32x4 pack8(f32x8 v) { return u32x4{pk2(v[0], v[1]), pk2(v[2], v[3]), pk2(v[4], v[5]), pk2(v[6], v[7])}; }

#define EX(b, i) "v_exp_f32 v[" #b "+" #i "], v[" #b "+" #i "]\n\t"
#define PV(o, vv, p) "v_smfmac_f32_32x32x32_bf16 v[" #o ":" #o "+15], v[" #vv ":" #vv "+3], v[" #p ":" #p "+7], %[idx]\n\t"
#define QK(s, k, q) "v_mfma_f32_32x32x16_bf16 v[" #s ":" #s "+15], v[" #k ":" #k "+3], v[" #q ":" #q "+3], 0\n\t"
#define TILE(c, n, oprev, vprev, knext, qnext, o, v0, X1, X2) \
    EX(c, 0) EX(c, 1) PV(oprev, vprev, n + 8) EX(c, 2) EX(c, 3) QK(n, knext, qnext) EX(c, 4) EX(c, 5) EX(c, 6) X1 EX(c, 7) EX(c, 8) EX(c, 9) \
    PV(o, v0, c) EX(c, 10) EX(c, 11) EX(c, 12) X2 EX(c, 13) EX(c, 14) EX(c, 15)
#define WRO(o) "ds_write_b128 %[pa], v[" #o ":" #o "+3]\n\tds_write_b128 %[pa], v[" #o "+4:" #o "+7] offset:1152\n\tds_write_b32 %[la], v[" #o "+8]\n\t"
#define ZERO(o) "v_mfma_f32_32x32x8bf16_1k v[" #o ":" #o "+15], %[z], %[z], 0\n\t"
#define STEP(o, op, q, qn, WR) \
    TILE(0, 16, op, 156, 68, q, o, 96, "", "") \
    TILE(16, 0, o, 100, 72, q, o, 104, WR, "ds_read_b128 v[" #qn ":" #qn "+3], %[qa]\n\t") \
    TILE(0, 16, o, 108, 76, q, o, 112, "", "") \
    TILE(16, 0, o, 116, 80, q, o, 120, "", "") \
    TILE(0, 16, o, 124, 84, q, o, 128, "s_waitcnt lgkmcnt(0)\n\t", ZERO(op)) \
    TILE(16, 0, o, 132, 88, q, o, 136, "", "") \
    TILE(0, 16, o, 140, 92, q, o, 144, "", "") \
    TILE(16, 0, o, 148, 64, qn, o, 152, "", "")
#define ATT_OPS(pa_, la_, qa_) \
    : "+{v[0:15]}"(S0), "+{v[16:31]}"(S1), "+{v[32:47]}"(O0), "+{v[48:63]}"(O1), "+{v[160:163]}"(Q0), "+{v[164:167]}"(Q1) \
    : "{v[64:95]}"(Kr), "{v[96:127]}"(Va), "{v[128:159]}"(Vb), [idx] "v"(idx), [z] "v"(z2), [pa] "v"(pa_), [la] "v"(la_), [qa] "v"(qa_) : "memory"

__global__ void __launch_bounds__(512, 2) adaln_block(const u16* __restrict__ x, const u16* __restrict__ wq, const u16* __restrict__ wk, const u16* __restrict__ wv,
                                                      const u16* __restrict__ wo, const u16* __restrict__ w1, const u16* __restrict__ w2, const u16* __restrict__ cvec,
                                                      const u16* __restrict__ wm, u16* __restrict__ out, char* wsp) {

    u64 pt0, pt1, pt2, pt3, pt4;
    {
        const GAS char* ka = (const GAS char*)__builtin_amdgcn_kernarg_segment_ptr();
        u64 wmp, wsq, xp; u32 d;
        asm volatile("s_memrealtime %0\n\ts_waitcnt lgkmcnt(0)\n\ts_load_dwordx2 %2, %5, 0x40\n\ts_load_dwordx2 %3, %5, 0x50\n\ts_load_dwordx2 %4, %5, 0x0\n\ts_waitcnt lgkmcnt(0)\n\ts_memrealtime %1\n\ts_waitcnt lgkmcnt(0)" : "=&s"(pt0), "=&s"(pt1), "=&s"(wmp), "=&s"(wsq), "=&s"(xp) : "s"(ka) : "memory");
        u32 o1 = threadIdx.x * 6144 + (blockIdx.x & 63) * 32, o2 = 0, o3 = blockIdx.x * 8192 + threadIdx.x * 16;
        asm volatile("global_load_dword %1, %2, %3\n\ts_waitcnt vmcnt(0)\n\ts_memrealtime %0\n\ts_waitcnt lgkmcnt(0)" : "=&s"(pt2), "=&v"(d) : "v"(o1), "s"(wmp) : "memory");
        asm volatile("global_load_dword %1, %2, %3 sc1\n\ts_waitcnt vmcnt(0)\n\ts_memrealtime %0\n\ts_waitcnt lgkmcnt(0)" : "=&s"(pt3), "=&v"(d) : "v"(o2), "s"(wsq) : "memory");
        asm volatile("global_load_dword %1, %2, %3\n\ts_waitcnt vmcnt(0)\n\ts_memrealtime %0\n\ts_waitcnt lgkmcnt(0)" : "=&s"(pt4), "=&v"(d) : "v"(o3), "s"(xp) : "memory");
    }
    extern __shared__ __attribute__((aligned(16))) char smem[];
    LAS char* L = (LAS char*)smem;
    const u32 lds0 = (u32)(size_t)L;
    const int tid = threadIdx.x, lane = tid & 63, w = __builtin_amdgcn_readfirstlane(tid >> 6);
    const int r32 = lane & 31, hi = lane >> 5;
    const int b = blockIdx.x, xc = b & 7, rr = b >> 3, g = xc * 4 + (rr >> 3), j = rr & 7, cu = g * 8 + j;
    const __amdgpu_buffer_rsrc_t ws = __builtin_amdgcn_make_buffer_rsrc(wsp, 0, WS_END, 0x00020000);
    const u32x4 poison4 = {POISON, POISON, POISON, POISON};
    u64 tp0, tpx;
    STAMP(0);

    if (b < 64 && w < 2) ST128(poison4, WS_K + b * 2048 + w * 1024 + lane * 16, 0);
    ST128(poison4, WS_O + ((g * 2 + (w >> 2)) * 32 + j * 4 + (w & 3)) * 1024 + lane * 16, 0);
    const u32 tag = __builtin_amdgcn_raw_buffer_load_b32(ws, WS_EPOCH, 0, SC1) + 1u;
    const bool modp = rr < 24;
    const int mp = ((rr >> 2) * 8 + xc) * 4 + (rr & 3);
    u32x4 wm0, wm1; u32 c0, c1;
    if (modp) {
        const int row = tid >> 1;
        wm0 = *(const GAS u32x4*)(wm + row * 3072 + mp * 16 + (tid & 1) * 8);
        wm1 = *(const GAS u32x4*)(wm + (row + 256) * 3072 + mp * 16 + (tid & 1) * 8);
        c0 = cvec[row]; c1 = cvec[row + 256];
    }
    SB();
    u32x4 xr[8];
#pragma unroll
    for (int i = 0; i < 8; ++i) xr[i] = *(const GAS u32x4*)(x + (g * 64 + w + 8 * ((i + j) & 7)) * DM + lane * 8);
    SB();
    const int l_kv = L_H + (j < 4 ? 32 * HS : 0);
#pragma unroll
    for (int i = 0; i < 2; ++i) {
        glds16(wk + (w * 2 + i) * 512 + lane * 8, lds0 + l_kv + (w * 2 + i) * 1024);
        glds16(wv + (w * 2 + i) * 512 + lane * 8, lds0 + l_kv + 16384 + (w * 2 + i) * 1024);
    }
#pragma unroll
    for (int i = 0; i < 8; ++i) glds16(wq + ((w * 8 + i) * 8 + (lane >> 3)) * DM + j * 64 + (lane & 7) * 8, lds0 + L_WQ + (w * 8 + i) * 1024);
    const int item = w * 256 + b;
    int pre_dst = 0, pre_step = 0, pre_perm = 1; bool pre_two = true;
    if (item < 1648) {
        const u16* src; int ld, m0, ks;
        if (item < 256) { src = wo; ld = DM; m0 = item & 7; ks = item >> 3; pre_step = 32 * 1024; pre_dst = WS_WO + ks * 1024; }
        else if (item < 944) { const int it = item - 256; src = w2; ld = DM; m0 = it & 7; ks = it >> 3; pre_step = 86 * 1024; pre_dst = WS_W2 + ks * 1024; }
        else { const int it = item - 944; src = w1; ld = FF; m0 = it >> 5; ks = it & 31; pre_step = 32 * 1024; pre_dst = WS_W1 + ks * 1024; pre_perm = 0; pre_two = m0 < 21; }
        pre_dst += 2 * m0 * pre_step;
        const int ch = pre_two ? (lane & 7) : (lane & 3);
#pragma unroll
        for (int i = 0; i < 2; ++i) glds16(src + (ks * 16 + i * 8 + (lane >> 3)) * ld + m0 * 64 + ch * 8, lds0 + L_PRE + w * 2048 + i * 1024);
    }
    SB();
    STAMP(1);
    if (modp) {
        const float a0 = bf_lo(c0), a1 = bf_lo(c1);
        const u32 sp = pk2(a0 * __builtin_amdgcn_rcpf(1.f + __builtin_amdgcn_exp2f(-LOG2E * a0)), a1 * __builtin_amdgcn_rcpf(1.f + __builtin_amdgcn_exp2f(-LOG2E * a1)));
        f32x8 acc = unpack8(wm0) * bf_lo(sp) + unpack8(wm1) * bf_hi(sp);
#pragma unroll
        for (int i = 0; i < 8; ++i) { float t = acc[i]; t = dpp_add<0x128>(t); t = dpp_add<0x124>(t); acc[i] = dpp_add<0x122>(t); }
        if ((lane & 14) == 0) {
            LAS f32x4* p = (LAS f32x4*)(L + L_RED + (w * 4 + (lane >> 4)) * 64 + (lane & 1) * 32);
            p[0] = f32x4{acc[0], acc[1], acc[2], acc[3]}; p[1] = f32x4{acc[4], acc[5], acc[6], acc[7]};
        }
    }
    BAR();
    if (modp && w == 0) {
        float t = 0.f;
#pragma unroll
        for (int i = 0; i < 8; ++i) t += *(LAS float*)(L + L_RED + ((lane >> 4) * 8 + i) * 64 + (lane & 15) * 4);
        t = swap32_add(t, t); t = swap16_add(t, t);
        const float nb = __builtin_bit_cast(float, __builtin_amdgcn_update_dpp(0, __builtin_bit_cast(int, t), 0xB1, 0xF, 0xF, true));
        if (lane < 16 && !(lane & 1)) ST64((u32x2{pk2(t, nb), tag}), WS_MOD + mp * 128 + lane * 4, 0);
    }
    STAMP(2);
    f32x8 n1[8];
    {
        float mean[8], rstd[8];
        stats4(xr, mean, rstd); stats4(xr + 4, mean + 4, rstd + 4);
#pragma unroll
        for (int i = 0; i < 8; ++i) n1[i] = unpack8(xr[i]) * rstd[i] - mean[i] * rstd[i];
    }
    VM0();
    STAMP(3);
    if (item < 1648) {
        const int row = (pre_perm ? 4 * hi : 8 * hi) + ((lane & 15) >> 2), rs = pre_perm ? 8 : 4;
        LAS char* p = L + L_PRE + w * 2048 + row * 128 + ((lane >> 4) & 1) * 32 + (lane & 3) * 8;
        const u32x4 f0 = cat(trread(p), trread(p + rs * 128)), f1 = cat(trread(p + 64), trread(p + 64 + rs * 128));
        ST128(f0, pre_dst + lane * 16, 0);
        if (pre_two) ST128(f1, pre_dst + pre_step + lane * 16, 0);
    }
    BAR();
    u32x4 wf[16];
    {
        LAS char* p = L + L_WQ + ((w >> 2) * 256 + 8 * hi + ((lane & 15) >> 2)) * 128 + (w & 1) * 64 + ((lane >> 4) & 1) * 32 + (lane & 3) * 8;
#pragma unroll
        for (int s = 0; s < 16; ++s) wf[s] = cat(trread(p + s * 2048), trread(p + s * 2048 + 512));
    }
    u32x4 kf[16];
    if (w < 2) {
        LAS char* p = L + l_kv + w * 16384 + (8 * (lane >> 4) + ((lane & 15) >> 2)) * 32 + (lane & 3) * 8;
#pragma unroll
        for (int s = 0; s < 16; ++s) kf[s] = cat(trread(p + s * 1024), trread(p + s * 1024 + 128));
    }
    STAMP(4);
    {
        const int G = w * 64 + lane, off = WS_MOD + (G >> 3) * 128 + (G & 7) * 8;
        u32x2 v;
        for (int it = 0; it < SPIN; ++it) {
            asm volatile("" ::: "memory");
            v = __builtin_amdgcn_raw_buffer_load_b64(ws, off, 0, SC1);
            if (__all(v.y == tag)) break;
        }
        *(LAS u32*)(L + L_MOD + G * 4) = v.x;
    }
    VM0();
    BAR();
    STAMP(5);
    const f32x8 sa = unpack8(*(LAS u32x4*)(L + L_MOD + lane * 16)), ca = unpack8(*(LAS u32x4*)(L + L_MOD + 1024 + lane * 16)) + 1.f;
    *(LAS u32x4*)(L + L_H + (w + 8 * j) * HS + lane * 16) = pack8(n1[0] * ca + sa);
    BAR();
    if (w < 2) {
        f32x4 acc = {0.f, 0.f, 0.f, 0.f};
        LAS char* p = L + L_H + (8 * j + (lane & 7)) * HS + (lane >> 4) * 16;
        const int mg = lane >> 4, nn = lane & 15;
        if (w == 0) {
#pragma unroll
            for (int s = 0; s < 16; ++s) acc = mfma16(kf[s], *(LAS u32x4*)(p + s * 64), acc);
            if (nn < 8) ST64((u32x2{clean(pk2(acc[0], acc[1])), clean(pk2(acc[2], acc[3]))}), WS_K + (cu * 8 + nn) * 32 + (mg & 1) * 16 + (mg >> 1) * 8, 0);
        } else {
#pragma unroll
            for (int s = 0; s < 16; ++s) acc = mfma16(*(LAS u32x4*)(p + s * 64), kf[s], acc);
            if (mg < 2) ST64((u32x2{clean(pk2(acc[0], acc[1])), clean(pk2(acc[2], acc[3]))}), WS_V + cu * 256 + nn * 16 + mg * 8, 0);
        }
    }
    STAMP(6);
#pragma unroll
    for (int i = 1; i < 8; ++i) *(LAS u32x4*)(L + L_H + (w + 8 * ((i + j) & 7)) * HS + lane * 16) = pack8(n1[i] * ca + sa);
    BAR();
    STAMP(7);
    u32x4 kq[8], vq[16];
    const int koff = WS_K + w * 8192 + r32 * 32 + hi * 16, voff = WS_V + w * 8192 + hi * 256 + r32 * 16;
#pragma unroll
    for (int i = 0; i < 16; ++i) vq[i] = u32x4{0x3F803F80u, 0x3F803F80u, 0x3F803F80u, 0x3F803F80u};
#pragma unroll
    for (int i = 0; i < 8; ++i) kq[i] = LD128(koff + i * 1024, 0);
    if (r32 < 16) {
#pragma unroll
        for (int i = 0; i < 16; ++i) vq[i] = LD128(voff + i * 512, 0);
    }
    SB();
    {
        f32x16 acc = {};
        LAS char* p = L + L_H + (((w >> 1) & 1) * 32 + r32) * HS + (w >> 2) * 512 + hi * 16;
#pragma unroll
        for (int s = 0; s < 16; ++s) acc = mfma32(wf[s], *(LAS u32x4*)(p + s * 32), acc);
        const int kh = w >> 2;
        f32x8 keep, send;
#pragma unroll
        for (int i = 0; i < 8; ++i) { keep[i] = kh ? acc[8 + i] : acc[i]; send[i] = kh ? acc[i] : acc[8 + i]; }
        LAS f32x4* xq = (LAS f32x4*)(L + L_PRE + w * 2048 + lane * 16);
        xq[0] = f32x4{send[0], send[1], send[2], send[3]}; xq[64] = f32x4{send[4], send[5], send[6], send[7]};
        BAR();
        LAS f32x4* yq = (LAS f32x4*)(L + L_PRE + (w ^ 4) * 2048 + lane * 16);
        const f32x4 y0 = yq[0], y1 = yq[64];
        keep += f32x8{y0[0], y0[1], y0[2], y0[3], y1[0], y1[1], y1[2], y1[3]};
        *(LAS u32x4*)(L + L_Q + (((w >> 1) & 1) * 4 + (w & 1) * 2 + kh) * 1024 + lane * 16) = pack8(keep * (0.25f * LOG2E));
    }
    STAMP(8);
    for (int it = 0; it < SPIN; ++it) {
        u32 m = 0;
#pragma unroll
        for (int i = 0; i < 8; ++i) m = pmax(m, kq[i]);
#pragma unroll
        for (int i = 0; i < 16; ++i) m = pmax(m, vq[i]);
        if (!__any(m == POISON)) break;
        asm volatile("" ::: "memory");
#pragma unroll
        for (int i = 0; i < 8; ++i) kq[i] = LD128(koff + i * 1024, 0);
        if (r32 < 16) {
#pragma unroll
            for (int i = 0; i < 16; ++i) vq[i] = LD128(voff + i * 512, 0);
        }
    }
    BAR();
    STAMP(9);
    if (b == 0 && tid == 0) __builtin_amdgcn_raw_buffer_store_b32(tag, ws, WS_EPOCH, 0, SC1);
    const int T0 = j < 3 ? 6 * j : 5 * j + 3, NT = j < 3 ? 6 : 5;
    ST128(poison4, WS_X1 + (g * 64 + w * 8 + (lane >> 3)) * 1024 + j * 128 + (lane & 7) * 16, 0);
#pragma unroll
    for (int i = 0; i < 3; ++i) {
        const int p = w * 3 + i, hf = p >= 12, ks = p - hf * 12;
        if (ks < 2 * NT) ST128(poison4, WS_H1 + ((g * 2 + hf) * 86 + 2 * T0 + ks) * 1024 + lane * 16, 0);
    }
    {
        const int G = 512 + w * 64 + lane, off = WS_MOD + (G >> 3) * 128 + (G & 7) * 8;
        u32x2 v0, v1;
        for (int it = 0; it < SPIN; ++it) {
            asm volatile("" ::: "memory");
            v0 = __builtin_amdgcn_raw_buffer_load_b64(ws, off, 0, SC1);
            v1 = __builtin_amdgcn_raw_buffer_load_b64(ws, off + 8192, 0, SC1);
            if (__all(v0.y == tag && v1.y == tag)) break;
        }
        *(LAS u32*)(L + L_MOD + G * 4) = v0.x; *(LAS u32*)(L + L_MOD + 2048 + G * 4) = v1.x;
    }
    u32x4 wof[8], w1f[24];
    const int wo_off = WS_WO + ((2 * j + (w & 1)) * 32 + (w >> 1) * 8) * 1024 + lane * 16;
#pragma unroll
    for (int i = 0; i < 8; ++i) wof[i] = LD128(wo_off + i * 1024, 0);
    int w1_off[3];
#pragma unroll
    for (int a = 0; a < 3; ++a) {
        const int tl = w < 2 ? 3 * w + a : w - 2, ks = w < 2 ? 24 : 8 * a;
        w1_off[a] = (tl < NT ? WS_W1 + ((T0 + tl) * 32 + ks) * 1024 : OOB) + lane * 16;
    }
#pragma unroll
    for (int i = 0; i < 6; ++i) w1f[i] = LD128(w1_off[0] + i * 1024, 0);
    SB();

    u32x32 Kr, Va, Vb;
#pragma unroll
    for (int i = 0; i < 8; ++i)
#pragma unroll
        for (int e = 0; e < 4; ++e) { Kr[4 * i + e] = kq[i][e]; Va[4 * i + e] = vq[i][e]; Vb[4 * i + e] = vq[8 + i][e]; }
    const u32 idx = 0xDDDDDDDDu;
    const u32x2 z2 = {0u, 0u};
    u32x4 of[8];
    const int o_off = WS_O + (g * 64 + (w >> 1) * 8) * 1024 + lane * 16;
    const int tk = w * 4 + (lane >> 4), nq = lane & 15;
    u32x2 xres[2];
#pragma unroll 1
    for (int bt = 0; bt < 2; ++bt) {
        f32x16 S0, S1 = {}, O0 = {}, O1 = {};
        u32x4 Q0 = *(LAS u32x4*)(L + L_Q + bt * 4096 + lane * 16), Q1 = {};
        S0 = mfma32(kq[0], Q0, f32x16{});
        const u32 pa = lds0 + w * PW + lane * 16, la = lds0 + 8 * PW + r32 * 48 + w * 4, qa = lds0 + L_Q + bt * 4096 + lane * 16;
        asm volatile("s_nop 7\n\ts_nop 7\n\t" STEP(32, 48, 160, 164, "") ATT_OPS(pa, la, qa + 1024));
        if (bt) BAR();
        asm volatile(STEP(48, 32, 164, 160, WRO(32)) ATT_OPS(pa, la, qa + 2048));
        asm volatile(STEP(32, 48, 160, 164, WRO(48)) ATT_OPS(pa + PSL, la + PSL, qa + 3072));
        asm volatile(STEP(48, 32, 164, 160, WRO(32)) ATT_OPS(pa + 2 * PSL, la + 2 * PSL, qa + 3072));
        asm volatile("s_nop 1\n\t" PV(48, 156, 24) "s_nop 7\n\ts_nop 7\n\t" WRO(48) ATT_OPS(pa + 3 * PSL, la + 3 * PSL, qa));
        if (bt == 0) VM0();
        BAR();
        STAMP(10 + 2 * bt);
        {
            const int sl = w >> 1, q = lane >> 1, rg = lane & 1;
            LAS char* p = L + sl * PSL + rg * PPL + ((w & 1) * 32 + q) * 16;
            f32x4 o = *(LAS f32x4*)p;
#pragma unroll
            for (int i = 1; i < 8; ++i) o += *(LAS f32x4*)(p + i * PW);
            const f32x4 l4 = *(LAS f32x4*)(L + sl * PSL + 8 * PW + q * 48) + *(LAS f32x4*)(L + sl * PSL + 8 * PW + q * 48 + 16);
            const float l = (l4[0] + l4[1]) + (l4[2] + l4[3]);
            const float am = fmaxf(fmaxf(fabsf(o[0]), fabsf(o[1])), fmaxf(fabsf(o[2]), fabsf(o[3])));
            const float sm = (o[0] + o[1]) + (o[2] + o[3]);
            const int dst = WS_O + ((g * 2 + bt) * 32 + j * 4 + sl) * 1024;
            const bool ok = l > 0x1p-60f && l < 0x1p100f && am < 0x1p120f && sm == sm;
            if (__builtin_expect(__all(ok), 1)) {
                o *= __builtin_amdgcn_rcpf(l);
                ST64((u32x2{clean(pk2(o[0], o[1])), clean(pk2(o[2], o[3]))}), dst + (w & 1) * 512 + lane * 8, 0);
            } else {
                const u32x4 Q = *(LAS u32x4*)(L + L_Q + (bt * 4 + sl) * 1024 + lane * 16);
                f32x16 O = {}; float m = -3e38f;
#pragma unroll 1
                for (int t = 0; t < 64; ++t) {
                    const u32x4 kk = LD128(WS_K + t * 1024 + r32 * 32 + hi * 16, 0);
                    u32x4 v0 = {0x3F803F80u, 0x3F803F80u, 0x3F803F80u, 0x3F803F80u}, v1 = v0;
                    if (r32 < 16) { v0 = LD128(WS_V + t * 1024 + hi * 256 + r32 * 16, 0); v1 = LD128(WS_V + t * 1024 + 512 + hi * 256 + r32 * 16, 0); }
                    f32x16 s = mfma32(kk, Q, f32x16{});
                    float tm = m;
#pragma unroll
                    for (int i = 0; i < 16; ++i) tm = fmaxf(tm, s[i]);
                    auto sw = __builtin_amdgcn_permlane32_swap(__builtin_bit_cast(u32, tm), __builtin_bit_cast(u32, tm), false, false);
                    const u32 sw0 = sw[0], sw1 = sw[1];
                    tm = fmaxf(__builtin_bit_cast(float, sw0), __builtin_bit_cast(float, sw1));
                    O *= __builtin_amdgcn_exp2f(m - tm);
                    m = tm;
#pragma unroll
                    for (int i = 0; i < 16; ++i) s[i] = __builtin_amdgcn_exp2f(s[i] - m);
                    O = __builtin_amdgcn_smfmac_f32_32x32x32_bf16(__builtin_bit_cast(bf16x8, v0), __builtin_bit_cast(bf16x16, __builtin_shufflevector(s, s, 0, 1, 2, 3, 4, 5, 6, 7)), O, (int)idx, 0, 0);
                    O = __builtin_amdgcn_smfmac_f32_32x32x32_bf16(__builtin_bit_cast(bf16x8, v1), __builtin_bit_cast(bf16x16, __builtin_shufflevector(s, s, 8, 9, 10, 11, 12, 13, 14, 15)), O, (int)idx, 0, 0);
                }
                const float inv = __builtin_amdgcn_rcpf(O[8]);
                const u32x4 r = {clean(pk2(O[0] * inv, O[1] * inv)), clean(pk2(O[2] * inv, O[3] * inv)), clean(pk2(O[4] * inv, O[5] * inv)), clean(pk2(O[6] * inv, O[7] * inv))};
                if (hi == (w & 1)) ST128(r, dst + lane * 16, 0);
            }
        }
        STAMP(11 + 2 * bt);
    }

#define LOAD_O(hf) _Pragma("unroll") for (int i = 0; i < 8; ++i) of[i] = LD128(o_off + (hf) * 32768 + i * 1024, 0)
#define VALID(N, arr, RELOAD) _Pragma("unroll 1") for (int it = 0; it < SPIN; ++it) { u32 m = 0; _Pragma("unroll") for (int i = 0; i < N; ++i) m = pmax(m, arr[i]); if (!__any(m == POISON)) break; asm volatile("" ::: "memory"); RELOAD; }
#define WRITE_TILE(base, acc) _Pragma("unroll") for (int q = 0; q < 4; ++q) *(LAS f32x4*)((base) + q * 32) = f32x4{acc[4 * q], acc[4 * q + 1], acc[4 * q + 2], acc[4 * q + 3]}
#define WO_GEMM(hf, lp) { VALID(8, of, LOAD_O(hf)); f32x16 acc = {}; _Pragma("unroll") for (int i = 0; i < 8; ++i) acc = mfma32(wof[i], of[i], acc); \
        WRITE_TILE(L + (lp) + (w >> 1) * TP + r32 * TS + (w & 1) * 128 + hi * 16, acc); }
#define X1_FIN(hf, lp) { LAS char* p = L + (lp) + tk * TS + nq * 16; \
        const f32x4 a = (*(LAS f32x4*)p + *(LAS f32x4*)(p + TP)) + (*(LAS f32x4*)(p + 2 * TP) + *(LAS f32x4*)(p + 3 * TP)); \
        const u32x2 xv = xres[hf]; \
        xres[hf] = u32x2{clean(pk2(bf_lo(xv.x) + ga[0] * a[0], bf_hi(xv.x) + ga[1] * a[1])), clean(pk2(bf_lo(xv.y) + ga[2] * a[2], bf_hi(xv.y) + ga[3] * a[3]))}; \
        ST64(xres[hf], WS_X1 + (g * 64 + (hf) * 32 + tk) * 1024 + j * 128 + nq * 8, 0); }
#define LOAD_X1(hf, arr) _Pragma("unroll") for (int i = 0; i < 4; ++i) arr[i] = LD128(WS_X1 + (g * 64 + (hf) * 32 + w * 4 + i) * 1024 + lane * 16, 0)
#define LN2(hf, arr, lp) { VALID(4, arr, LOAD_X1(hf, arr)); float mean[4], rstd[4]; stats4(arr, mean, rstd); \
        _Pragma("unroll") for (int i = 0; i < 4; ++i) *(LAS u32x4*)(L + (lp) + (w * 4 + i) * HS + lane * 16) = pack8((unpack8(arr[i]) * rstd[i] - mean[i] * rstd[i]) * cb + sb); }
#define FFN1(lh, lp, acc) { LAS char* p = L + (lh) + r32 * HS + hi * 16; \
        if (w < 2) { u32x4 bq[8]; _Pragma("unroll") for (int s = 0; s < 8; ++s) bq[s] = *(LAS u32x4*)(p + (24 + s) * 32); \
            _Pragma("unroll") for (int a = 0; a < 3; ++a) { acc = f32x16{}; _Pragma("unroll") for (int s = 0; s < 8; ++s) acc = mfma32(w1f[8 * a + s], bq[s], acc); \
                _Pragma("unroll") for (int q = 0; q < 4; ++q) *(LAS f32x4*)(L + (lp) + (3 * w + a) * 4096 + q * 1024 + lane * 16) = f32x4{acc[4 * q], acc[4 * q + 1], acc[4 * q + 2], acc[4 * q + 3]}; } \
        } else { acc = f32x16{}; _Pragma("unroll") for (int s = 0; s < 24; ++s) acc = mfma32(w1f[s], *(LAS u32x4*)(p + s * 32), acc); } }
#define SILU(hf, lp, acc) if (w >= 2 && w - 2 < NT) { u32 pk[8]; \
        _Pragma("unroll") for (int q = 0; q < 4; ++q) { const f32x4 t = *(LAS f32x4*)(L + (lp) + (w - 2) * 4096 + q * 1024 + lane * 16); float y[4]; \
            _Pragma("unroll") for (int e = 0; e < 4; ++e) { const float z = acc[4 * q + e] + t[e]; y[e] = z * __builtin_amdgcn_rcpf(1.f + __builtin_amdgcn_exp2f(-z)); } \
            pk[2 * q] = clean(pk2(y[0], y[1])); pk[2 * q + 1] = clean(pk2(y[2], y[3])); } \
        const int dst = WS_H1 + ((g * 2 + (hf)) * 86 + 2 * (T0 + w - 2)) * 1024 + lane * 16; \
        ST128((u32x4{pk[0], pk[1], pk[2], pk[3]}), dst, 0); ST128((u32x4{pk[4], pk[5], pk[6], pk[7]}), dst + 1024, 0); }
#define LOAD_H1(hf, arr) _Pragma("unroll") for (int i = 0; i < 11; ++i) arr[i] = LD128((i >= 9 ? h1_off2 : h1_off) + (hf) * 86 * 1024 + i * 1024, 0)
#define FFN2(hf, arr, lp) { VALID(11, arr, LOAD_H1(hf, arr)); f32x16 a0 = {}, a1 = {}; \
        _Pragma("unroll") for (int i = 0; i < 11; ++i) { a0 = mfma32(w2f[i], arr[i], a0); a1 = mfma32(w2f[11 + i], arr[i], a1); } \
        WRITE_TILE(L + (lp) + w * TP + r32 * TS + hi * 16, a0); WRITE_TILE(L + (lp) + w * TP + r32 * TS + 128 + hi * 16, a1); }
#define FINAL(hf, lp) { LAS char* p = L + (lp) + tk * TS + nq * 16; f32x4 a = *(LAS f32x4*)p; \
        _Pragma("unroll") for (int i = 1; i < 8; ++i) a += *(LAS f32x4*)(p + i * TP); \
        const u32x2 xv = xres[hf]; \
        *(GAS u32x2*)(out + (g * 64 + (hf) * 32 + tk) * DM + j * 64 + nq * 4) = u32x2{pk2(bf_lo(xv.x) + gb[0] * a[0], bf_hi(xv.x) + gb[1] * a[1]), pk2(bf_lo(xv.y) + gb[2] * a[2], bf_hi(xv.y) + gb[3] * a[3])}; }

    LOAD_O(0);
#pragma unroll
    for (int hf = 0; hf < 2; ++hf) xres[hf] = *(const GAS u32x2*)(x + (g * 64 + hf * 32 + tk) * DM + j * 64 + nq * 4);
#pragma unroll
    for (int i = 6; i < 24; ++i) w1f[i] = LD128(w1_off[i >> 3] + (i & 7) * 1024, 0);
    SB();
    f32x4 ga, gb;
    { const u32x2 t = *(LAS u32x2*)(L + L_MOD + 2048 + j * 128 + nq * 8); ga = f32x4{bf_lo(t.x), bf_hi(t.x), bf_lo(t.y), bf_hi(t.y)}; }
    { const u32x2 t = *(LAS u32x2*)(L + L_MOD + 5120 + j * 128 + nq * 8); gb = f32x4{bf_lo(t.x), bf_hi(t.x), bf_lo(t.y), bf_hi(t.y)} * LN_2; }
    const f32x8 sb = unpack8(*(LAS u32x4*)(L + L_MOD + 3072 + lane * 16)) * LOG2E, cb = (unpack8(*(LAS u32x4*)(L + L_MOD + 4096 + lane * 16)) + 1.f) * LOG2E;
    WO_GEMM(0, L_WPA);
    BAR();
    STAMP(14);
    LOAD_O(1);
    SB();
    X1_FIN(0, L_WPA);
    WO_GEMM(1, L_WPB);
    VM0();
    BAR();
    STAMP(15);
    u32x4 xa[4], xb[4];
    LOAD_X1(0, xa);
    SB();
    X1_FIN(1, L_WPB);
    u32x4 w2f[22];
    const int w2_off = WS_W2 + (2 * j * 86 + 11 * w) * 1024 + lane * 16, w2_off2 = w == 7 ? OOB : w2_off;
    const int h1_off = WS_H1 + (g * 2 * 86 + 11 * w) * 1024 + lane * 16, h1_off2 = w == 7 ? OOB : h1_off;
    VM0();
    STAMP(16);
    LOAD_X1(1, xb);
#pragma unroll
    for (int i = 0; i < 11; ++i) w2f[i] = LD128((i >= 9 ? w2_off2 : w2_off) + i * 1024, 0);
    SB();
    LN2(0, xa, L_H2A);
    BAR();
    STAMP(17);
    f32x16 accA, accB;
    FFN1(L_H2A, L_F1A, accA);
    LN2(1, xb, L_H2B);
    BAR();
    STAMP(18);
    SILU(0, L_F1A, accA);
    SB();
#pragma unroll
    for (int i = 0; i < 11; ++i) w2f[11 + i] = LD128((i >= 9 ? w2_off2 : w2_off) + (86 + i) * 1024, 0);
    SB();
    FFN1(L_H2B, L_F1B, accB);
    BAR();
    STAMP(19);
    u32x4 ha[11], hb[11];
    LOAD_H1(0, ha);
    SB();
    SILU(1, L_F1B, accB);
    VM0();
    BAR();
    STAMP(20);
    LOAD_H1(1, hb);
    SB();
    FFN2(0, ha, L_F2A);
    BAR();
    STAMP(21);
    FINAL(0, L_F2A);
    FFN2(1, hb, L_F2B);
    BAR();
    STAMP(22);
    FINAL(1, L_F2B);
    STAMP(23);
}

extern "C" void kernel_launch(void* const* d_in, const int* in_sizes, int n_in, void* d_out, int out_size, void* d_ws, size_t ws_size, hipStream_t stream) {
    (void)hipFuncSetAttribute((const void*)adaln_block, hipFuncAttributeMaxDynamicSharedMemorySize, L_END);
    adaln_block<<<256, 512, L_END, stream>>>((const u16*)d_in[0], (const u16*)d_in[1], (const u16*)d_in[2], (const u16*)d_in[3], (const u16*)d_in[4],
                                              (const u16*)d_in[6], (const u16*)d_in[7], (const u16*)d_in[8], (const u16*)d_in[9], (u16*)d_out, (char*)d_ws);
}
```

```cpp
#include <hip/hip_runtime.h>
#include <hip/hip_bf16.h>

static __constant__ const unsigned kOffsets[64 * 128] __attribute__((aligned(512))) = {
    8192,16512,24832,33152,41472,49792,58112,66432,0,41472,1024,41472,8192,32768,144,2048,14336,8192,20480,140288,0,0,0,0,1024,0,67108864,16384,256,196608,229376,43008,0,65536,131072,147456,148480,149504,157696,2048,32768,36864,40960,45056,0,745472,749568,851968,856064,860160,864256,204800,208896,92160,0,59392,32768,49152,34816,43008,0,0,0,0,167936,180224,192512,212992,233472,159744,160768,161792,162816,163840,155648,243712,65536,98304,92160,131072,139264,98304,139264,124928,59392,160000,8192,0,286261504,319947522,353633540,387319558,421005576,454691594,488377612,522063630,323584,2129920,2130944,2135040,2139136,2220032,2221056,2225152,2229248,196608,200704,92160,32768,34816,77824,0,0,149504,851968,3934208,0,0,0,0,0,0,0,0,0,0,0,0,
    9232,17552,25872,34192,42512,50832,59152,67472,0,57856,1024,57856,8192,33792,176,3072,15360,9216,21504,140800,0,0,0,0,1024,0,67108864,256,512,2129920,2220032,45056,1024,66560,139264,150528,151552,152576,158208,2048,49152,53248,57344,61440,512,778240,782336,868352,872448,876544,880640,221184,225280,106496,4096,63488,32800,49184,43008,47104,0,0,0,0,168960,181248,193536,214016,234496,164864,165888,166912,172032,173056,156672,244736,69632,102400,96256,132096,140288,99328,139520,129024,63488,160064,16384,2048,1886280280,2627372676,1110185472,303042060,639902228,1143871490,943073832,1077820474,323584,2141184,2142208,2146304,2150400,2231296,2232320,2236416,2240512,212992,217088,106496,40960,43008,79872,16384,256,151552,851968,3934208,0,0,0,0,0,0,0,0,0,0,0,0,
    10272,18592,26912,35232,43552,51872,60192,68512,0,74752,8192,41472,8192,33792,176,4096,16384,10240,22528,142336,1065353216,0,0,0,1024,524288,67108864,256,512,2129920,2220032,47104,2048,67584,67108864,153600,154624,155648,158784,2048,65536,69632,73728,77824,1024,811008,815104,884736,888832,892928,897024,237568,241664,120832,8192,67584,32832,49216,51200,51200,0,0,0,0,169984,182272,202752,223232,174080,175104,176128,177152,178176,179200,157696,245760,73728,106496,100352,133120,141312,100352,139776,133120,67584,160128,24576,4096,1177557508,1380994636,1583109716,1650465798,1785226852,1211265644,2054714996,2189459068,323584,2152448,2153472,2157568,2161664,2242560,2243584,2247680,2251776,229376,233472,120832,49152,51200,81920,0,512,153600,884736,3934720,0,0,0,0,0,0,0,0,0,0,0,0,
    11312,19632,27952,36272,44592,52912,61232,69552,131072,91136,8192,57856,8192,33792,176,5120,17408,11264,23552,142848,1065353216,0,0,0,1024,1048576,67108864,256,512,2129920,2220032,49152,3072,68608,67108864,156672,157696,158720,159296,2048,81920,86016,90112,94208,1536,843776,847872,901120,905216,909312,913408,253952,258048,135168,12288,71680,32864,49248,59392,55296,0,0,0,0,171008,183296,203776,224256,184320,185344,186368,187392,188416,189440,158720,246784,77824,110592,104448,134144,142336,101376,140032,137216,71680,160192,32768,6144,2324176392,2458947212,1244960404,2728435352,2863179428,4294909962,4294967295,4294967295,323584,2163712,2164736,2168832,2172928,2253824,2254848,2258944,2263040,245760,249856,135168,57344,59392,83968,16384,768,155648,884736,3934720,0,0,0,0,0,0,0,0,0,0,0,0,
    12352,20672,28992,37312,45632,53952,62272,70592,262144,107520,8192,0,8192,33792,160,2048,14336,8192,20480,140288,0,0,0,0,2752,0,3571712,256,512,720896,753664,51200,32768,98304,67108864,235520,236544,237568,157696,2048,98304,102400,106496,110592,0,720896,724992,729088,733184,737280,741376,2174976,2265088,100352,16384,75776,0,16384,34816,43008,0,0,0,0,148480,190464,191488,194560,195584,196608,197632,198656,199680,200704,147456,235520,81920,114688,108544,135168,143360,65536,131072,124928,59392,160000,40960,0,100925952,235670024,370414096,505158168,639902240,774646312,909390384,1044134456,323584,2174976,2176000,2180096,2184192,2265088,2266112,2270208,2274304,196608,200704,92160,32768,34816,77824,0,1024,149504,720896,3932160,0,0,0,0,0,0,0,0,0,0,0,0,
    13392,21712,30032,38352,46672,54992,63312,71632,327680,115712,8192,2048,8192,33792,160,3072,15360,9216,21504,140800,0,0,0,0,2752,1024,3702784,256,512,1245184,1277952,53248,33792,99328,67108864,238592,239616,240640,158208,2048,114688,118784,122880,126976,512,753664,757760,761856,765952,770048,774144,2186240,2276352,114688,20480,79872,32,16416,43008,47104,0,0,0,0,150528,201728,204800,205824,206848,207872,208896,209920,210944,211968,149504,237568,86016,118784,112640,136192,144384,66560,131328,129024,63488,160064,49152,2048,100925952,235670024,370414096,505158168,639902240,774646312,909390384,1044134456,323584,2186240,2187264,2191360,2195456,2276352,2277376,2281472,2285568,212992,217088,106496,40960,43008,79872,16384,1280,151552,753664,3932672,0,0,0,0,0,0,0,0,0,0,0,0,
    14432,22752,31072,39392,47712,56032,64352,72672,393216,123904,8192,6144,41472,33792,160,4096,16384,10240,22528,142336,1065353216,0,0,0,2752,2048,3833856,256,512,1769472,1802240,55296,34816,100352,67108864,241664,242688,243712,158784,2048,131072,135168,139264,143360,1024,786432,790528,794624,798720,802816,806912,2197504,2287616,129024,24576,83968,64,16448,51200,51200,0,0,0,0,152576,215040,216064,217088,218112,219136,220160,221184,222208,225280,151552,239616,90112,122880,116736,137216,145408,67584,131584,133120,67584,160128,57344,4096,100925952,235670024,370414096,505158168,639902240,774646312,909390384,1044134456,323584,2197504,2198528,2202624,2206720,2287616,2288640,2292736,2296832,229376,233472,120832,49152,51200,81920,0,1536,153600,786432,3933184,0,0,0,0,0,0,0,0,0,0,0,0,
    15472,23792,32112,40432,48752,57072,65392,73712,458752,132096,8192,4096,41472,33792,144,5120,17408,11264,23552,142848,1065353216,0,0,0,1024,0,67108864,16384,256,196608,229376,57344,35840,101376,67108864,244736,245760,246784,159296,2048,147456,151552,155648,159744,1536,819200,823296,827392,831488,835584,839680,2208768,2298880,143360,28672,88064,96,16480,59392,55296,0,0,0,0,154624,226304,227328,228352,229376,230400,231424,232448,67108864,67108864,153600,241664,94208,126976,120832,138240,146432,68608,131840,137216,71680,160192,65536,6144,4294967295,4294967295,4294967295,4294967295,4294967295,4294967295,4294967295,4294967295,323584,2208768,2209792,2213888,67108864,2298880,2299904,2304000,67108864,245760,249856,135168,57344,59392,83968,16384,1792,155648,819200,3933696,0,0,0,0,0,0,0,0,0,0,0,0,
    16512,8192,33152,24832,49792,41472,66432,58112,0,41472,1024,41472,16512,32896,144,2048,14336,8192,20480,140288,0,0,0,0,1024,128,67108864,16384,256,262144,294912,43008,4096,69632,132096,159744,160768,161792,157696,2304,32768,36864,40960,45056,4096,942080,946176,1048576,1052672,1056768,1060864,270336,274432,92160,0,59392,32896,49280,34816,43008,0,0,0,0,180224,192512,212992,233472,157696,172032,173056,174080,175104,176128,167936,256000,65536,98304,92160,131072,139264,102400,140288,124928,59392,160000,8192,0,286261504,319947522,353633540,387319558,421005576,454691594,488377612,522063630,323712,2310144,2311168,2315264,2319360,2400256,2401280,2405376,2409472,262144,266240,92160,32768,34816,77824,0,0,149504,1048576,3937280,0,0,0,0,0,0,0,0,0,0,0,0,
    17552,9232,34192,25872,50832,42512,67472,59152,0,57856,1024,57856,16512,34048,176,3072,15360,9216,21504,140800,0,0,0,0,1024,128,67108864,256,512,2310144,2400256,45056,5120,70656,140288,162816,163840,164864,158208,2304,49152,53248,57344,61440,4608,974848,978944,1064960,1069056,1073152,1077248,286720,290816,106496,4096,63488,32928,49312,43008,47104,0,0,0,0,181248,193536,214016,234496,158720,177152,178176,179200,184320,185344,168960,257024,69632,102400,96256,132096,140288,103424,140544,129024,63488,160064,16384,2048,2728435352,2863179428,537545268,1886280280,2627372676,1110185472,303042060,639902228,323712,2321408,2322432,2326528,2330624,2411520,2412544,2416640,2420736,278528,282624,106496,40960,43008,79872,16384,256,151552,1048576,3937280,0,0,0,0,0,0,0,0,0,0,0,0,
    18592,10272,35232,26912,51872,43552,68512,60192,128,74752,8192,41472,16512,34048,176,4096,16384,10240,22528,142336,1065353216,0,0,0,1024,524416,67108864,256,512,2310144,2400256,47104,6144,71680,67108864,165888,166912,167936,158784,2304,65536,69632,73728,77824,5120,1007616,1011712,1081344,1085440,1089536,1093632,303104,307200,120832,8192,67584,32960,49344,51200,51200,0,0,0,0,182272,202752,223232,155648,186368,187392,188416,189440,190464,191488,169984,258048,73728,106496,100352,133120,141312,104448,140800,133120,67584,160128,24576,4096,1143871490,943073832,1077820474,1313610244,1448366672,1177575004,1717854816,1919707752,323712,2332672,2333696,2337792,2341888,2422784,2423808,2427904,2432000,294912,299008,120832,49152,51200,81920,0,512,153600,1081344,3937792,0,0,0,0,0,0,0,0,0,0,0,0,
    19632,11312,36272,27952,52912,44592,69552,61232,131200,91136,8192,57856,16512,34048,176,5120,17408,11264,23552,142848,1065353216,0,0,0,1024,1048704,67108864,256,512,2310144,2400256,49152,7168,72704,67108864,168960,169984,171008,159296,2304,81920,86016,90112,94208,5632,1040384,1044480,1097728,1101824,1105920,1110016,319488,323584,135168,12288,71680,32992,49376,59392,55296,0,0,0,0,183296,203776,224256,156672,194560,195584,196608,197632,198656,199680,171008,259072,77824,110592,104448,134144,142336,105472,141056,137216,71680,160192,32768,6144,1987320838,2122087032,1211269760,2391575176,2526319248,4294909448,4294967295,4294967295,323712,2343936,2344960,2349056,2353152,2434048,2435072,2439168,2443264,311296,315392,135168,57344,59392,83968,16384,768,155648,1081344,3937792,0,0,0,0,0,0,0,0,0,0,0,0,
    20672,12352,37312,28992,53952,45632,70592,62272,262272,107520,8192,0,8192,34048,160,2048,14336,8192,20480,140288,0,0,0,0,2752,128,3588096,256,512,786432,819200,51200,36864,102400,67108864,247808,248832,249856,157696,2304,98304,102400,106496,110592,4096,917504,921600,925696,929792,933888,937984,2355200,2445312,100352,16384,75776,128,16512,34816,43008,0,0,0,0,160768,200704,201728,204800,205824,206848,207872,208896,209920,210944,159744,247808,81920,114688,108544,135168,143360,69632,132096,124928,59392,160000,40960,0,100925952,235670024,370414096,505158168,639902240,774646312,909390384,1044134456,323712,2355200,2356224,2360320,2364416,2445312,2446336,2450432,2454528,262144,266240,92160,32768,34816,77824,0,1024,149504,917504,3935232,0,0,0,0,0,0,0,0,0,0,0,0,
    21712,13392,38352,30032,54992,46672,71632,63312,327808,115712,8192,2048,8192,34048,160,3072,15360,9216,21504,140800,0,0,0,0,2752,1152,3719168,256,512,1310720,1343488,53248,37888,103424,67108864,250880,251904,252928,158208,2304,114688,118784,122880,126976,4608,950272,954368,958464,962560,966656,970752,2366464,2456576,114688,20480,79872,160,16544,43008,47104,0,0,0,0,162816,211968,215040,216064,217088,218112,219136,220160,221184,222208,161792,249856,86016,118784,112640,136192,144384,70656,132352,129024,63488,160064,49152,2048,100925952,235670024,370414096,505158168,639902240,774646312,909390384,1044134456,323712,2366464,2367488,2371584,2375680,2456576,2457600,2461696,2465792,278528,282624,106496,40960,43008,79872,16384,1280,151552,950272,3935744,0,0,0,0,0,0,0,0,0,0,0,0,
    22752,14432,39392,31072,56032,47712,72672,64352,393344,123904,8192,6144,41472,34048,160,4096,16384,10240,22528,142336,1065353216,0,0,0,2752,2176,3850240,256,512,1835008,1867776,55296,38912,104448,67108864,253952,254976,256000,158784,2304,131072,135168,139264,143360,5120,983040,987136,991232,995328,999424,1003520,2377728,2467840,129024,24576,83968,192,16576,51200,51200,0,0,0,0,164864,225280,226304,227328,228352,229376,230400,231424,232448,147456,163840,251904,90112,122880,116736,137216,145408,71680,132608,133120,67584,160128,57344,4096,100925952,235670024,370414096,505158168,639902240,774646312,909390384,1044134456,323712,2377728,2378752,2382848,2386944,2467840,2468864,2472960,2477056,294912,299008,120832,49152,51200,81920,0,1536,153600,983040,3936256,0,0,0,0,0,0,0,0,0,0,0,0,
    23792,15472,40432,32112,57072,48752,73712,65392,458880,132096,8192,4096,41472,34048,144,5120,17408,11264,23552,142848,1065353216,0,0,0,1024,0,67108864,16384,256,196608,229376,57344,39936,105472,67108864,257024,258048,259072,159296,2304,147456,151552,155648,159744,5632,1015808,1019904,1024000,1028096,1032192,1036288,2388992,2479104,143360,28672,88064,224,16608,59392,55296,0,0,0,0,166912,148480,149504,150528,151552,152576,153600,154624,67108864,67108864,165888,253952,94208,126976,120832,138240,146432,72704,132864,137216,71680,160192,65536,6144,4294967295,4294967295,4294967295,4294967295,4294967295,4294967295,4294967295,4294967295,323712,2388992,2390016,2394112,67108864,2479104,2480128,2484224,67108864,311296,315392,135168,57344,59392,83968,16384,1792,155648,1015808,3936768,0,0,0,0,0,0,0,0,0,0,0,0,
    24832,33152,8192,16512,58112,66432,41472,49792,0,41472,1024,41472,24832,33024,144,2048,14336,8192,20480,140288,0,0,0,0,1024,256,67108864,16384,256,327680,360448,43008,8192,73728,133120,172032,173056,174080,157696,2560,32768,36864,40960,45056,8192,1138688,1142784,1245184,1249280,1253376,1257472,335872,339968,92160,0,59392,33024,49408,34816,43008,0,0,0,0,192512,212992,233472,157696,169984,184320,185344,186368,187392,188416,180224,268288,65536,98304,92160,131072,139264,106496,141312,124928,59392,160000,8192,0,286261504,319947522,353633540,387319558,421005576,454691594,488377612,522063630,323840,2490368,2491392,2495488,2499584,2580480,2581504,2585600,2589696,327680,331776,92160,32768,34816,77824,0,0,149504,1245184,3940352,0,0,0,0,0,0,0,0,0,0,0,0,
    25872,34192,9232,17552,59152,67472,42512,50832,0,57856,1024,57856,24832,34304,176,3072,15360,9216,21504,140800,0,0,0,0,1024,256,67108864,256,512,2490368,2580480,45056,9216,74752,141312,175104,176128,177152,158208,2560,49152,53248,57344,61440,8704,1171456,1175552,1261568,1265664,1269760,1273856,352256,356352,106496,4096,63488,33056,49440,43008,47104,0,0,0,0,193536,214016,234496,158720,171008,189440,190464,191488,194560,195584,181248,269312,69632,102400,96256,132096,140288,107520,141568,129024,63488,160064,16384,2048,2391575176,2526319248,503859250,2728435352,2863179428,537545268,1886280280,2627372676,323840,2501632,2502656,2506752,2510848,2591744,2592768,2596864,2600960,344064,348160,106496,40960,43008,79872,16384,256,151552,1245184,3940352,0,0,0,0,0,0,0,0,0,0,0,0,
    26912,35232,10272,18592,60192,68512,43552,51872,256,74752,8192,41472,24832,34304,176,4096,16384,10240,22528,142336,1065353216,0,0,0,1024,524544,67108864,256,512,2490368,2580480,47104,10240,75776,67108864,178176,179200,180224,158784,2560,65536,69632,73728,77824,9216,1204224,1208320,1277952,1282048,1286144,1290240,368640,372736,120832,8192,67584,33088,49472,51200,51200,0,0,0,0,202752,223232,155648,167936,196608,197632,198656,199680,200704,201728,182272,270336,73728,106496,100352,133120,141312,108544,141824,133120,67584,160128,24576,4096,1110185472,303042060,639902228,707270658,1010448438,1143881790,1380994636,1583109716,323840,2512896,2513920,2518016,2522112,2603008,2604032,2608128,2612224,360448,364544,120832,49152,51200,81920,0,512,153600,1277952,3940864,0,0,0,0,0,0,0,0,0,0,0,0,
    27952,36272,11312,19632,61232,69552,44592,52912,131328,91136,8192,57856,24832,34304,176,5120,17408,11264,23552,142848,1065353216,0,0,0,1024,1048832,67108864,256,512,2490368,2580480,49152,11264,76800,67108864,181248,182272,183296,159296,2560,81920,86016,90112,94208,9728,1236992,1241088,1294336,1298432,1302528,1306624,385024,389120,135168,12288,71680,33120,49504,59392,55296,0,0,0,0,203776,224256,156672,168960,204800,205824,206848,207872,208896,209920,183296,271360,77824,110592,104448,134144,142336,109568,142080,137216,71680,160192,32768,6144,1650465284,1785226852,1177580140,2054714996,2189459068,4294908934,4294967295,4294967295,323840,2524160,2525184,2529280,2533376,2614272,2615296,2619392,2623488,376832,380928,135168,57344,59392,83968,16384,768,155648,1277952,3940864,0,0,0,0,0,0,0,0,0,0,0,0,
    28992,37312,12352,20672,62272,70592,45632,53952,262400,107520,8192,0,8192,34304,160,2048,14336,8192,20480,140288,0,0,0,0,2752,256,3604480,256,512,851968,884736,51200,40960,106496,67108864,260096,261120,262144,157696,2560,98304,102400,106496,110592,8192,1114112,1118208,1122304,1126400,1130496,1134592,2535424,2625536,100352,16384,75776,256,16640,34816,43008,0,0,0,0,173056,210944,211968,215040,216064,217088,218112,219136,220160,221184,172032,260096,81920,114688,108544,135168,143360,73728,133120,124928,59392,160000,40960,0,100925952,235670024,370414096,505158168,639902240,774646312,909390384,1044134456,323840,2535424,2536448,2540544,2544640,2625536,2626560,2630656,2634752,327680,331776,92160,32768,34816,77824,0,1024,149504,1114112,3938304,0,0,0,0,0,0,0,0,0,0,0,0,
    30032,38352,13392,21712,63312,71632,46672,54992,327936,115712,8192,2048,8192,34304,160,3072,15360,9216,21504,140800,0,0,0,0,2752,1280,3735552,256,512,1376256,1409024,53248,41984,107520,67108864,263168,264192,265216,158208,2560,114688,118784,122880,126976,8704,1146880,1150976,1155072,1159168,1163264,1167360,2546688,2636800,114688,20480,79872,288,16672,43008,47104,0,0,0,0,175104,222208,225280,226304,227328,228352,229376,230400,231424,232448,174080,262144,86016,118784,112640,136192,144384,74752,133376,129024,63488,160064,49152,2048,100925952,235670024,370414096,505158168,639902240,774646312,909390384,1044134456,323840,2546688,2547712,2551808,2555904,2636800,2637824,2641920,2646016,344064,348160,106496,40960,43008,79872,16384,1280,151552,1146880,3938816,0,0,0,0,0,0,0,0,0,0,0,0,
    31072,39392,14432,22752,64352,72672,47712,56032,393472,123904,8192,6144,41472,34304,160,4096,16384,10240,22528,142336,1065353216,0,0,0,2752,2304,3866624,256,512,1900544,1933312,55296,43008,108544,67108864,266240,267264,268288,158784,2560,131072,135168,139264,143360,9216,1179648,1183744,1187840,1191936,1196032,1200128,2557952,2648064,129024,24576,83968,320,16704,51200,51200,0,0,0,0,177152,147456,148480,149504,150528,151552,152576,153600,154624,159744,176128,264192,90112,122880,116736,137216,145408,75776,133632,133120,67584,160128,57344,4096,100925952,235670024,370414096,505158168,639902240,774646312,909390384,1044134456,323840,2557952,2558976,2563072,2567168,2648064,2649088,2653184,2657280,360448,364544,120832,49152,51200,81920,0,1536,153600,1179648,3939328,0,0,0,0,0,0,0,0,0,0,0,0,
    32112,40432,15472,23792,65392,73712,48752,57072,459008,132096,8192,4096,41472,34304,144,5120,17408,11264,23552,142848,1065353216,0,0,0,1024,0,67108864,16384,256,196608,229376,57344,44032,109568,67108864,269312,270336,271360,159296,2560,147456,151552,155648,159744,9728,1212416,1216512,1220608,1224704,1228800,1232896,2569216,2659328,143360,28672,88064,352,16736,59392,55296,0,0,0,0,179200,160768,161792,162816,163840,164864,165888,166912,67108864,67108864,178176,266240,94208,126976,120832,138240,146432,76800,133888,137216,71680,160192,65536,6144,4294967295,4294967295,4294967295,4294967295,4294967295,4294967295,4294967295,4294967295,323840,2569216,2570240,2574336,67108864,2659328,2660352,2664448,67108864,376832,380928,135168,57344,59392,83968,16384,1792,155648,1212416,3939840,0,0,0,0,0,0,0,0,0,0,0,0,
    33152,24832,16512,8192,66432,58112,49792,41472,0,41472,1024,41472,33152,33152,144,2048,14336,8192,20480,140288,0,0,0,0,1024,384,67108864,16384,256,393216,425984,43008,12288,77824,134144,184320,185344,186368,157696,2816,32768,36864,40960,45056,12288,1335296,1339392,1441792,1445888,1449984,1454080,401408,405504,92160,0,59392,33152,49536,34816,43008,0,0,0,0,202752,223232,155648,167936,180224,194560,195584,196608,197632,198656,192512,280576,65536,98304,92160,131072,139264,110592,142336,124928,59392,160000,8192,0,286261504,319947522,353633540,387319558,421005576,454691594,488377612,522063630,323968,2670592,2671616,2675712,2679808,2760704,2761728,2765824,2769920,393216,397312,92160,32768,34816,77824,0,0,149504,1441792,3943424,0,0,0,0,0,0,0,0,0,0,0,0,
    34192,25872,17552,9232,67472,59152,50832,42512,0,57856,1024,57856,33152,34560,176,3072,15360,9216,21504,140800,0,0,0,0,1024,384,67108864,256,512,2670592,2760704,45056,13312,78848,142336,187392,188416,189440,158208,2816,49152,53248,57344,61440,12800,1368064,1372160,1458176,1462272,1466368,1470464,417792,421888,106496,4096,63488,33184,49568,43008,47104,0,0,0,0,203776,224256,156672,168960,181248,199680,200704,201728,204800,205824,193536,281600,69632,102400,96256,132096,140288,111616,142592,129024,63488,160064,16384,2048,2122087032,2324202112,1211243526,2458947212,2660800148,1244929544,2795807392,2930551464,323968,2681856,2682880,2686976,2691072,2771968,2772992,2777088,2781184,409600,413696,106496,40960,43008,79872,16384,256,151552,1441792,3943424,0,0,0,0,0,0,0,0,0,0,0,0,
    35232,26912,18592,10272,68512,60192,51872,43552,384,74752,8192,41472,33152,34560,176,4096,16384,10240,22528,142336,1065353216,0,0,0,1024,524672,67108864,256,512,2670592,2760704,47104,14336,79872,67108864,190464,191488,192512,158784,2816,65536,69632,73728,77824,13312,1400832,1404928,67108864,67108864,67108864,67108864,434176,438272,120832,8192,67584,33216,49600,51200,51200,0,0,0,0,212992,233472,157696,169984,182272,206848,207872,208896,209920,210944,67108864,67108864,73728,106496,100352,133120,141312,112640,142848,133120,67584,160128,24576,4096,1278615562,1886280280,2627372676,235673088,571740688,402794020,976759336,1312833084,323968,67108864,2694144,2698240,2702336,67108864,2784256,2788352,2792448,425984,430080,120832,49152,51200,81920,0,512,153600,67108864,3943424,0,0,0,0,0,0,0,0,0,0,0,0,
    36272,27952,19632,11312,69552,61232,52912,44592,131456,91136,8192,57856,33152,34560,176,5120,17408,11264,23552,142848,1065353216,0,0,0,1024,1048960,67108864,256,512,2670592,2760704,49152,15360,80896,67108864,193536,67108864,67108864,159296,2816,81920,86016,90112,94208,13824,1433600,1437696,67108864,67108864,67108864,67108864,450560,454656,135168,12288,71680,33248,49632,59392,55296,0,0,0,0,214016,234496,158720,171008,183296,211968,215040,216064,217088,218112,67108864,67108864,77824,110592,104448,134144,142336,113664,143104,137216,71680,160192,32768,6144,1380992046,1583109716,436494944,1785226852,1987342956,4294919728,4294967295,4294967295,323968,67108864,2705408,2709504,2713600,67108864,2795520,2799616,2803712,442368,446464,135168,57344,59392,83968,16384,768,155648,67108864,3943424,0,0,0,0,0,0,0,0,0,0,0,0,
    37312,28992,20672,12352,70592,62272,53952,45632,262528,107520,8192,0,8192,34560,160,2048,14336,8192,20480,140288,0,0,0,0,2752,384,3620864,256,512,917504,950272,51200,45056,110592,67108864,272384,273408,274432,157696,2816,98304,102400,106496,110592,12288,1310720,1314816,1318912,1323008,1327104,1331200,2715648,2805760,100352,16384,75776,384,16768,34816,43008,0,0,0,0,185344,219136,220160,221184,222208,225280,226304,227328,228352,229376,184320,272384,81920,114688,108544,135168,143360,77824,134144,124928,59392,160000,40960,0,100925952,235670024,370414096,505158168,639902240,774646312,909390384,1044134456,323968,2715648,2716672,2720768,2724864,2805760,2806784,2810880,2814976,393216,397312,92160,32768,34816,77824,0,1024,149504,1310720,3941376,0,0,0,0,0,0,0,0,0,0,0,0,
    38352,30032,21712,13392,71632,63312,54992,46672,328064,115712,8192,2048,8192,34560,160,3072,15360,9216,21504,140800,0,0,0,0,2752,1408,3751936,256,512,1441792,1474560,53248,46080,111616,67108864,275456,276480,277504,158208,2816,114688,118784,122880,126976,12800,1343488,1347584,1351680,1355776,1359872,1363968,2726912,2817024,114688,20480,79872,416,16800,43008,47104,0,0,0,0,187392,230400,231424,232448,147456,148480,149504,150528,151552,152576,186368,274432,86016,118784,112640,136192,144384,78848,134400,129024,63488,160064,49152,2048,100925952,235670024,370414096,505158168,639902240,774646312,909390384,1044134456,323968,2726912,2727936,2732032,2736128,2817024,2818048,2822144,2826240,409600,413696,106496,40960,43008,79872,16384,1280,151552,1343488,3941888,0,0,0,0,0,0,0,0,0,0,0,0,
    39392,31072,22752,14432,72672,64352,56032,47712,393600,123904,8192,6144,41472,34560,160,4096,16384,10240,22528,142336,1065353216,0,0,0,2752,2432,3883008,256,512,1966080,1998848,55296,47104,112640,67108864,278528,279552,280576,158784,2816,131072,135168,139264,143360,13312,1376256,1380352,1384448,1388544,1392640,1396736,2738176,2828288,129024,24576,83968,448,16832,51200,51200,0,0,0,0,189440,153600,154624,159744,160768,161792,162816,163840,164864,165888,188416,276480,90112,122880,116736,137216,145408,79872,134656,133120,67584,160128,57344,4096,100925952,235670024,370414096,505158168,639902240,774646312,909390384,1044134456,323968,2738176,2739200,2743296,2747392,2828288,2829312,2833408,2837504,425984,430080,120832,49152,51200,81920,0,1536,153600,1376256,3942400,0,0,0,0,0,0,0,0,0,0,0,0,
    40432,32112,23792,15472,73712,65392,57072,48752,459136,132096,8192,4096,41472,34560,144,5120,17408,11264,23552,142848,1065353216,0,0,0,1024,0,67108864,16384,256,196608,229376,57344,48128,113664,67108864,281600,67108864,67108864,159296,2816,147456,151552,155648,159744,13824,1409024,1413120,1417216,1421312,1425408,1429504,2749440,2839552,143360,28672,88064,480,16864,59392,55296,0,0,0,0,191488,166912,172032,173056,174080,175104,176128,177152,178176,179200,190464,278528,94208,126976,120832,138240,146432,80896,134912,137216,71680,160192,65536,6144,4294967295,4294967295,4294967295,4294967295,4294967295,4294967295,4294967295,4294967295,323968,2749440,2750464,2754560,2758656,2839552,2840576,2844672,2848768,442368,446464,135168,57344,59392,83968,16384,1792,155648,1409024,3942912,0,0,0,0,0,0,0,0,0,0,0,0,
    41472,49792,58112,66432,8192,16512,24832,33152,0,8192,1024,8192,41472,34816,144,2048,14336,8192,20480,140288,0,0,0,0,1024,512,67108864,16384,256,458752,491520,43008,16384,81920,135168,194560,195584,196608,157696,3072,32768,36864,40960,45056,16384,1499136,1503232,1605632,1609728,1613824,1617920,466944,471040,92160,0,59392,33280,49664,34816,43008,0,0,0,0,212992,233472,157696,169984,182272,204800,205824,206848,207872,208896,202752,290816,65536,98304,92160,131072,139264,114688,143360,124928,59392,160000,8192,0,286261504,319947522,353633540,387319558,421005576,454691594,488377612,522063630,324096,2850816,2851840,2855936,2860032,2940928,2941952,2946048,2950144,458752,462848,92160,32768,34816,77824,0,0,149504,1605632,3945984,0,0,0,0,0,0,0,0,0,0,0,0,
    42512,50832,59152,67472,9232,17552,25872,34192,0,24576,1024,24576,41472,35840,176,3072,15360,9216,21504,140800,0,0,0,0,1024,512,67108864,256,512,2850816,2940928,45056,17408,82944,143360,197632,198656,199680,158208,3072,49152,53248,57344,61440,16896,1531904,1536000,1622016,1626112,1630208,1634304,483328,487424,106496,4096,63488,33312,49696,43008,47104,0,0,0,0,214016,234496,158720,171008,183296,209920,210944,211968,215040,216064,203776,291840,69632,102400,96256,132096,140288,115712,143616,129024,63488,160064,16384,2048,1785226852,1987342956,470173232,2122087032,2324202112,503859250,2458947212,2660800148,324096,2862080,2863104,2867200,2871296,2952192,2953216,2957312,2961408,475136,479232,106496,40960,43008,79872,16384,256,151552,1605632,3945984,0,0,0,0,0,0,0,0,0,0,0,0,
    43552,51872,60192,68512,10272,18592,26912,35232,512,74752,8192,8192,41472,35840,176,4096,16384,10240,22528,142336,1065353216,0,0,0,1024,524800,67108864,256,512,2850816,2940928,47104,18432,83968,67108864,200704,201728,202752,158784,3072,65536,69632,73728,77824,17408,1564672,1568768,67108864,67108864,67108864,67108864,499712,503808,120832,8192,67584,33344,49728,51200,51200,0,0,0,0,223232,155648,167936,180224,192512,217088,218112,219136,220160,221184,67108864,67108864,73728,106496,100352,133120,141312,116736,143872,133120,67584,160128,24576,4096,537545268,2795807392,2930551464,1515736118,2256826478,369138842,303042060,639902228,324096,67108864,2874368,2878464,2882560,67108864,2964480,2968576,2972672,491520,495616,120832,49152,51200,81920,0,512,153600,67108864,3945984,0,0,0,0,0,0,0,0,0,0,0,0,
    44592,52912,61232,69552,11312,19632,27952,36272,131584,91136,8192,24576,41472,35840,176,5120,17408,11264,23552,142848,1065353216,0,0,0,1024,1049088,67108864,256,512,2850816,2940928,49152,19456,84992,67108864,203776,67108864,67108864,159296,3072,81920,86016,90112,94208,17920,1597440,1601536,67108864,67108864,67108864,67108864,516096,520192,135168,12288,71680,33376,49760,59392,55296,0,0,0,0,224256,156672,168960,181248,193536,222208,225280,226304,227328,228352,67108864,67108864,77824,110592,104448,134144,142336,117760,144128,137216,71680,160192,32768,6144,707270658,1044134456,1143885376,1448366672,1650482780,4294908420,4294967295,4294967295,324096,67108864,2885632,2889728,2893824,67108864,2975744,2979840,2983936,507904,512000,135168,57344,59392,83968,16384,768,155648,67108864,3945984,0,0,0,0,0,0,0,0,0,0,0,0,
    45632,53952,62272,70592,12352,20672,28992,37312,262656,107520,8192,0,8192,35840,160,2048,14336,8192,20480,140288,0,0,0,0,2752,512,3637248,256,512,983040,1015808,51200,49152,114688,67108864,282624,283648,284672,157696,3072,98304,102400,106496,110592,16384,1474560,1478656,1482752,1486848,1490944,1495040,2895872,2985984,100352,16384,75776,512,16896,34816,43008,0,0,0,0,195584,229376,230400,231424,232448,147456,148480,149504,150528,151552,194560,282624,81920,114688,108544,135168,143360,81920,135168,124928,59392,160000,40960,0,100925952,235670024,370414096,505158168,639902240,774646312,909390384,1044134456,324096,2895872,2896896,2900992,2905088,2985984,2987008,2991104,2995200,458752,462848,92160,32768,34816,77824,0,1024,149504,1474560,3943936,0,0,0,0,0,0,0,0,0,0,0,0,
    46672,54992,63312,71632,13392,21712,30032,38352,328192,115712,8192,2048,8192,35840,160,3072,15360,9216,21504,140800,0,0,0,0,2752,1536,3768320,256,512,1507328,1540096,53248,50176,115712,67108864,285696,286720,287744,158208,3072,114688,118784,122880,126976,16896,1507328,1511424,1515520,1519616,1523712,1527808,2907136,2997248,114688,20480,79872,544,16928,43008,47104,0,0,0,0,197632,152576,153600,154624,159744,160768,161792,162816,163840,164864,196608,284672,86016,118784,112640,136192,144384,82944,135424,129024,63488,160064,49152,2048,100925952,235670024,370414096,505158168,639902240,774646312,909390384,1044134456,324096,2907136,2908160,2912256,2916352,2997248,2998272,3002368,3006464,475136,479232,106496,40960,43008,79872,16384,1280,151552,1507328,3944448,0,0,0,0,0,0,0,0,0,0,0,0,
    47712,56032,64352,72672,14432,22752,31072,39392,393728,123904,8192,6144,41472,35840,160,4096,16384,10240,22528,142336,1065353216,0,0,0,2752,2560,3899392,256,512,2031616,2064384,55296,51200,116736,67108864,288768,289792,290816,158784,3072,131072,135168,139264,143360,17408,1540096,1544192,1548288,1552384,1556480,1560576,2918400,3008512,129024,24576,83968,576,16960,51200,51200,0,0,0,0,199680,165888,166912,172032,173056,174080,175104,176128,177152,178176,198656,286720,90112,122880,116736,137216,145408,83968,135680,133120,67584,160128,57344,4096,100925952,235670024,370414096,505158168,639902240,774646312,909390384,1044134456,324096,2918400,2919424,2923520,2927616,3008512,3009536,3013632,3017728,491520,495616,120832,49152,51200,81920,0,1536,153600,1540096,3944960,0,0,0,0,0,0,0,0,0,0,0,0,
    48752,57072,65392,73712,15472,23792,32112,40432,459264,132096,8192,4096,41472,35840,144,5120,17408,11264,23552,142848,1065353216,0,0,0,1024,0,67108864,16384,256,196608,229376,57344,52224,117760,67108864,291840,67108864,67108864,159296,3072,147456,151552,155648,159744,17920,1572864,1576960,1581056,1585152,1589248,1593344,2929664,3019776,143360,28672,88064,608,16992,59392,55296,0,0,0,0,201728,179200,184320,185344,186368,187392,188416,189440,190464,191488,200704,288768,94208,126976,120832,138240,146432,84992,135936,137216,71680,160192,65536,6144,4294967295,4294967295,4294967295,4294967295,4294967295,4294967295,4294967295,4294967295,324096,2929664,2930688,2934784,2938880,3019776,3020800,3024896,3028992,507904,512000,135168,57344,59392,83968,16384,1792,155648,1572864,3945472,0,0,0,0,0,0,0,0,0,0,0,0,
    49792,41472,66432,58112,16512,8192,33152,24832,0,8192,1024,8192,49792,34944,144,2048,14336,8192,20480,140288,0,0,0,0,1024,640,67108864,16384,256,524288,557056,43008,20480,86016,136192,204800,205824,206848,157696,3328,32768,36864,40960,45056,20480,1662976,1667072,1769472,1773568,1777664,1781760,532480,536576,92160,0,59392,33408,49792,34816,43008,0,0,0,0,223232,155648,167936,180224,192512,215040,216064,217088,218112,219136,212992,301056,65536,98304,92160,131072,139264,118784,144384,124928,59392,160000,8192,0,286261504,319947522,353633540,387319558,421005576,454691594,488377612,522063630,324224,3031040,3032064,3036160,3040256,3121152,3122176,3126272,3130368,524288,528384,92160,32768,34816,77824,0,0,149504,1769472,3948544,0,0,0,0,0,0,0,0,0,0,0,0,
    50832,42512,67472,59152,17552,9232,34192,25872,0,24576,1024,24576,49792,36096,176,3072,15360,9216,21504,140800,0,0,0,0,1024,640,67108864,256,512,3031040,3121152,45056,21504,87040,144384,207872,208896,209920,158208,3328,49152,53248,57344,61440,20992,1695744,1699840,1785856,1789952,1794048,1798144,548864,552960,106496,4096,63488,33440,49824,43008,47104,0,0,0,0,224256,156672,168960,181248,193536,220160,221184,222208,225280,226304,214016,302080,69632,102400,96256,132096,140288,119808,144640,129024,63488,160064,16384,2048,1448366672,1650482780,1177557508,1785226852,1987342956,1211243526,2122087032,2324202112,324224,3042304,3043328,3047424,3051520,3132416,3133440,3137536,3141632,540672,544768,106496,40960,43008,79872,16384,256,151552,1769472,3948544,0,0,0,0,0,0,0,0,0,0,0,0,
    51872,43552,68512,60192,18592,10272,35232,26912,640,74752,8192,8192,49792,36096,176,4096,16384,10240,22528,142336,1065353216,0,0,0,1024,524928,67108864,256,512,3031040,3121152,47104,22528,88064,67108864,210944,211968,212992,158784,3328,65536,69632,73728,77824,21504,1728512,1732608,67108864,67108864,67108864,67108864,565248,569344,120832,8192,67584,33472,49856,51200,51200,0,0,0,0,233472,157696,169984,182272,202752,227328,228352,229376,230400,231424,67108864,67108864,73728,106496,100352,133120,141312,120832,144896,133120,67584,160128,24576,4096,1244929544,2458947212,2660800148,2728402954,2863179428,1278652076,1886280280,2627372676,324224,67108864,3054592,3058688,3062784,67108864,3144704,3148800,3152896,557056,561152,120832,49152,51200,81920,0,512,153600,67108864,3948544,0,0,0,0,0,0,0,0,0,0,0,0,
    52912,44592,69552,61232,19632,11312,36272,27952,131712,91136,8192,24576,49792,36096,176,5120,17408,11264,23552,142848,1065353216,0,0,0,1024,1049216,67108864,256,512,3031040,3121152,49152,23552,89088,67108864,214016,67108864,67108864,159296,3328,81920,86016,90112,94208,22016,1761280,1765376,67108864,67108864,67108864,67108864,581632,585728,135168,12288,71680,33504,49888,59392,55296,0,0,0,0,234496,158720,171008,183296,203776,232448,147456,148480,149504,150528,67108864,67108864,77824,110592,104448,134144,142336,121856,145152,137216,71680,160192,32768,6144,235673088,571740688,402794020,976759336,1312833084,4294919214,4294967295,4294967295,324224,67108864,3065856,3069952,3074048,67108864,3155968,3160064,3164160,573440,577536,135168,57344,59392,83968,16384,768,155648,67108864,3948544,0,0,0,0,0,0,0,0,0,0,0,0,
    53952,45632,70592,62272,20672,12352,37312,28992,262784,107520,8192,0,8192,36096,160,2048,14336,8192,20480,140288,0,0,0,0,2752,640,3653632,256,512,1048576,1081344,51200,53248,118784,67108864,292864,293888,294912,157696,3328,98304,102400,106496,110592,20480,1638400,1642496,1646592,1650688,1654784,1658880,3076096,3166208,100352,16384,75776,640,17024,34816,43008,0,0,0,0,205824,151552,152576,153600,154624,159744,160768,161792,162816,163840,204800,292864,81920,114688,108544,135168,143360,86016,136192,124928,59392,160000,40960,0,100925952,235670024,370414096,505158168,639902240,774646312,909390384,1044134456,324224,3076096,3077120,3081216,3085312,3166208,3167232,3171328,3175424,524288,528384,92160,32768,34816,77824,0,1024,149504,1638400,3946496,0,0,0,0,0,0,0,0,0,0,0,0,
    54992,46672,71632,63312,21712,13392,38352,30032,328320,115712,8192,2048,8192,36096,160,3072,15360,9216,21504,140800,0,0,0,0,2752,1664,3784704,256,512,1572864,1605632,53248,54272,119808,67108864,295936,296960,297984,158208,3328,114688,118784,122880,126976,20992,1671168,1675264,1679360,1683456,1687552,1691648,3087360,3177472,114688,20480,79872,672,17056,43008,47104,0,0,0,0,207872,164864,165888,166912,172032,173056,174080,175104,176128,177152,206848,294912,86016,118784,112640,136192,144384,87040,136448,129024,63488,160064,49152,2048,100925952,235670024,370414096,505158168,639902240,774646312,909390384,1044134456,324224,3087360,3088384,3092480,3096576,3177472,3178496,3182592,3186688,540672,544768,106496,40960,43008,79872,16384,1280,151552,1671168,3947008,0,0,0,0,0,0,0,0,0,0,0,0,
    56032,47712,72672,64352,22752,14432,39392,31072,393856,123904,8192,6144,41472,36096,160,4096,16384,10240,22528,142336,1065353216,0,0,0,2752,2688,3915776,256,512,2097152,67108864,55296,55296,120832,67108864,299008,300032,301056,158784,3328,131072,135168,139264,143360,21504,1703936,1708032,1712128,1716224,1720320,1724416,3098624,3188736,129024,24576,83968,704,17088,51200,51200,0,0,0,0,209920,178176,179200,184320,185344,186368,187392,188416,189440,190464,208896,296960,90112,122880,116736,137216,145408,88064,136704,133120,67584,160128,57344,4096,100925952,235670024,370414096,505158168,639902240,774646312,909390384,1044134456,324224,3098624,3099648,3103744,3107840,3188736,3189760,3193856,3197952,557056,561152,120832,49152,51200,81920,0,1536,153600,1703936,3947520,0,0,0,0,0,0,0,0,0,0,0,0,
    57072,48752,73712,65392,23792,15472,40432,32112,459392,132096,8192,4096,41472,36096,144,5120,17408,11264,23552,142848,1065353216,0,0,0,1024,0,67108864,16384,256,196608,229376,57344,56320,121856,67108864,302080,67108864,67108864,159296,3328,147456,151552,155648,159744,22016,1736704,1740800,1744896,1748992,1753088,1757184,3109888,3200000,143360,28672,88064,736,17120,59392,55296,0,0,0,0,211968,191488,194560,195584,196608,197632,198656,199680,200704,201728,210944,299008,94208,126976,120832,138240,146432,89088,136960,137216,71680,160192,65536,6144,4294967295,4294967295,4294967295,4294967295,4294967295,4294967295,4294967295,4294967295,324224,3109888,3110912,3115008,3119104,3200000,3201024,3205120,3209216,573440,577536,135168,57344,59392,83968,16384,1792,155648,1736704,3948032,0,0,0,0,0,0,0,0,0,0,0,0,
    58112,66432,41472,49792,24832,33152,8192,16512,0,8192,1024,8192,58112,35072,144,2048,14336,8192,20480,140288,0,0,0,0,1024,768,67108864,16384,256,589824,622592,43008,24576,90112,137216,215040,216064,217088,157696,3584,32768,36864,40960,45056,24576,1826816,1830912,1933312,1937408,1941504,1945600,598016,602112,92160,0,59392,33536,49920,34816,43008,0,0,0,0,233472,157696,169984,182272,202752,225280,226304,227328,228352,229376,223232,311296,65536,98304,92160,131072,139264,122880,145408,124928,59392,160000,8192,0,286261504,319947522,353633540,387319558,421005576,454691594,488377612,522063630,324352,3211264,3212288,3216384,3220480,3301376,3302400,3306496,3310592,589824,593920,92160,32768,34816,77824,0,0,149504,1933312,3951104,0,0,0,0,0,0,0,0,0,0,0,0,
    59152,67472,42512,50832,25872,34192,9232,17552,0,24576,1024,24576,58112,36352,176,3072,15360,9216,21504,140800,0,0,0,0,1024,768,67108864,256,512,3211264,3301376,45056,25600,91136,145408,218112,219136,220160,158208,3584,49152,53248,57344,61440,25088,1859584,1863680,1949696,1953792,1957888,1961984,614400,618496,106496,4096,63488,33568,49952,43008,47104,0,0,0,0,234496,158720,171008,183296,203776,230400,231424,232448,147456,148480,224256,312320,69632,102400,96256,132096,140288,123904,145664,129024,63488,160064,16384,2048,976759336,1312833084,436487214,1448366672,1650482780,470173232,1785226852,1987342956,324352,3222528,3223552,3227648,3231744,3312640,3313664,3317760,3321856,606208,610304,106496,40960,43008,79872,16384,256,151552,1933312,3951104,0,0,0,0,0,0,0,0,0,0,0,0,
    60192,68512,43552,51872,26912,35232,10272,18592,768,74752,8192,8192,58112,36352,176,4096,16384,10240,22528,142336,1065353216,0,0,0,1024,525056,67108864,256,512,3211264,3301376,47104,26624,92160,67108864,221184,222208,223232,158784,3584,65536,69632,73728,77824,25600,1892352,1896448,67108864,67108864,67108864,67108864,630784,634880,120832,8192,67584,33600,49984,51200,51200,0,0,0,0,155648,167936,180224,192512,212992,149504,150528,151552,152576,153600,67108864,67108864,73728,106496,100352,133120,141312,124928,145920,133120,67584,160128,24576,4096,503859250,2122087032,2324202112,2391558708,2526319248,537566872,2795807392,2930551464,324352,67108864,3234816,3238912,3243008,67108864,3324928,3329024,3333120,622592,626688,120832,49152,51200,81920,0,512,153600,67108864,3951104,0,0,0,0,0,0,0,0,0,0,0,0,
    61232,69552,44592,52912,27952,36272,11312,19632,131840,91136,8192,24576,58112,36352,176,5120,17408,11264,23552,142848,1065353216,0,0,0,1024,1049344,67108864,256,512,3211264,3301376,49152,27648,93184,67108864,224256,67108864,67108864,159296,3584,81920,86016,90112,94208,26112,1925120,1929216,67108864,67108864,67108864,67108864,647168,651264,135168,12288,71680,33632,50016,59392,55296,0,0,0,0,156672,168960,181248,193536,214016,154624,159744,160768,161792,162816,67108864,67108864,77824,110592,104448,134144,142336,125952,146176,137216,71680,160192,32768,6144,1515736118,2256826478,369138842,303042060,639902228,4294907906,4294967295,4294967295,324352,67108864,3246080,3250176,3254272,67108864,3336192,3340288,3344384,638976,643072,135168,57344,59392,83968,16384,768,155648,67108864,3951104,0,0,0,0,0,0,0,0,0,0,0,0,
    62272,70592,45632,53952,28992,37312,12352,20672,262912,107520,8192,0,8192,36352,160,2048,14336,8192,20480,140288,0,0,0,0,2752,768,3670016,256,512,1114112,1146880,51200,57344,122880,67108864,303104,304128,305152,157696,3584,98304,102400,106496,110592,24576,1802240,1806336,1810432,1814528,1818624,1822720,3256320,3346432,100352,16384,75776,768,17152,34816,43008,0,0,0,0,216064,163840,164864,165888,166912,172032,173056,174080,175104,176128,215040,303104,81920,114688,108544,135168,143360,90112,137216,124928,59392,160000,40960,0,100925952,235670024,370414096,505158168,639902240,774646312,909390384,1044134456,324352,3256320,3257344,3261440,3265536,3346432,3347456,3351552,3355648,589824,593920,92160,32768,34816,77824,0,1024,149504,1802240,3949056,0,0,0,0,0,0,0,0,0,0,0,0,
    63312,71632,46672,54992,30032,38352,13392,21712,328448,115712,8192,2048,8192,36352,160,3072,15360,9216,21504,140800,0,0,0,0,2752,1792,3801088,256,512,1638400,1671168,53248,58368,123904,67108864,306176,307200,308224,158208,3584,114688,118784,122880,126976,25088,1835008,1839104,1843200,1847296,1851392,1855488,3267584,3357696,114688,20480,79872,800,17184,43008,47104,0,0,0,0,218112,177152,178176,179200,184320,185344,186368,187392,188416,189440,217088,305152,86016,118784,112640,136192,144384,91136,137472,129024,63488,160064,49152,2048,100925952,235670024,370414096,505158168,639902240,774646312,909390384,1044134456,324352,3267584,3268608,3272704,3276800,3357696,3358720,3362816,3366912,606208,610304,106496,40960,43008,79872,16384,1280,151552,1835008,3949568,0,0,0,0,0,0,0,0,0,0,0,0,
    64352,72672,47712,56032,31072,39392,14432,22752,393984,123904,8192,6144,41472,36352,144,4096,16384,10240,22528,142336,1065353216,0,0,0,1024,0,67108864,16384,256,196608,229376,55296,59392,124928,67108864,309248,310272,311296,158784,3584,131072,135168,139264,143360,25600,1867776,1871872,1875968,1880064,1884160,1888256,3278848,3368960,129024,24576,83968,832,17216,51200,51200,0,0,0,0,220160,190464,191488,194560,195584,196608,197632,198656,199680,200704,219136,307200,90112,122880,116736,137216,145408,92160,137728,133120,67584,160128,57344,4096,4294967295,4294967295,4294967295,4294967295,4294967295,4294967295,4294967295,4294967295,324352,3278848,3279872,3283968,3288064,3368960,3369984,3374080,3378176,622592,626688,120832,49152,51200,81920,0,1536,153600,1867776,3950080,0,0,0,0,0,0,0,0,0,0,0,0,
    65392,73712,48752,57072,32112,40432,15472,23792,459520,132096,8192,4096,41472,36352,144,5120,17408,11264,23552,142848,1065353216,0,0,0,1024,0,67108864,16384,256,196608,229376,57344,60416,125952,67108864,312320,67108864,67108864,159296,3584,147456,151552,155648,159744,26112,1900544,1904640,1908736,1912832,1916928,1921024,3290112,3380224,143360,28672,88064,864,17248,59392,55296,0,0,0,0,222208,201728,204800,205824,206848,207872,208896,209920,210944,211968,221184,309248,94208,126976,120832,138240,146432,93184,137984,137216,71680,160192,65536,6144,4294967295,4294967295,4294967295,4294967295,4294967295,4294967295,4294967295,4294967295,324352,3290112,3291136,3295232,3299328,3380224,3381248,3385344,3389440,638976,643072,135168,57344,59392,83968,16384,1792,155648,1900544,3950592,0,0,0,0,0,0,0,0,0,0,0,0,
    66432,58112,49792,41472,33152,24832,16512,8192,0,8192,1024,8192,66432,35200,144,2048,14336,8192,20480,140288,0,0,0,0,1024,896,67108864,16384,256,655360,688128,43008,28672,94208,138240,225280,226304,227328,157696,3840,32768,36864,40960,45056,28672,1990656,1994752,2097152,2101248,2105344,2109440,663552,667648,92160,0,59392,33664,50048,34816,43008,0,0,0,0,155648,167936,180224,192512,212992,147456,148480,149504,150528,151552,233472,321536,65536,98304,92160,131072,139264,126976,146432,124928,59392,160000,8192,0,286261504,319947522,353633540,387319558,421005576,454691594,488377612,522063630,324480,3391488,3392512,3396608,3400704,3481600,3482624,3486720,3490816,655360,659456,92160,32768,34816,77824,0,0,149504,2097152,3953664,0,0,0,0,0,0,0,0,0,0,0,0,
    67472,59152,50832,42512,34192,25872,17552,9232,0,24576,1024,24576,66432,36608,176,3072,15360,9216,21504,140800,0,0,0,0,1024,896,67108864,256,512,3391488,3481600,45056,29696,95232,146432,228352,229376,230400,158208,3840,49152,53248,57344,61440,29184,2023424,2027520,2113536,2117632,2121728,2125824,679936,684032,106496,4096,63488,33696,50080,43008,47104,0,0,0,0,156672,168960,181248,193536,214016,152576,153600,154624,159744,160768,234496,322560,69632,102400,96256,132096,140288,128000,146688,129024,63488,160064,16384,2048,303042060,639902228,1143871490,976759336,1312833084,1177557508,1448366672,1650482780,324480,3402752,3403776,3407872,3411968,3492864,3493888,3497984,3502080,671744,675840,106496,40960,43008,79872,16384,256,151552,2097152,3953664,0,0,0,0,0,0,0,0,0,0,0,0,
    68512,60192,51872,43552,35232,26912,18592,10272,896,74752,8192,8192,66432,36608,176,4096,16384,10240,22528,142336,1065353216,0,0,0,1024,525184,67108864,256,512,3391488,3481600,47104,30720,96256,67108864,231424,232448,233472,158784,3840,65536,69632,73728,77824,29696,2056192,2060288,67108864,67108864,67108864,67108864,696320,700416,120832,8192,67584,33728,50112,51200,51200,0,0,0,0,157696,169984,182272,202752,223232,161792,162816,163840,164864,165888,67108864,67108864,73728,106496,100352,133120,141312,129024,146944,133120,67584,160128,24576,4096,1211243526,1785226852,1987342956,2054692360,2189459068,1244957320,2458947212,2660800148,324480,67108864,3415040,3419136,3423232,67108864,3505152,3509248,3513344,688128,692224,120832,49152,51200,81920,0,512,153600,67108864,3953664,0,0,0,0,0,0,0,0,0,0,0,0,
    69552,61232,52912,44592,36272,27952,19632,11312,131968,91136,8192,24576,66432,36608,176,5120,17408,11264,23552,142848,1065353216,0,0,0,1024,1049472,67108864,256,512,3391488,3481600,49152,31744,97280,67108864,234496,67108864,67108864,159296,3840,81920,86016,90112,94208,30208,2088960,2093056,67108864,67108864,67108864,67108864,712704,716800,135168,12288,71680,33760,50144,59392,55296,0,0,0,0,158720,171008,183296,203776,224256,166912,172032,173056,174080,175104,67108864,67108864,77824,110592,104448,134144,142336,130048,147200,137216,71680,160192,32768,6144,2728402954,2863179428,1278652076,1886280280,2627372676,4294907392,4294967295,4294967295,324480,67108864,3426304,3430400,3434496,67108864,3516416,3520512,3524608,704512,708608,135168,57344,59392,83968,16384,768,155648,67108864,3953664,0,0,0,0,0,0,0,0,0,0,0,0,
    70592,62272,53952,45632,37312,28992,20672,12352,263040,107520,8192,0,8192,36608,160,2048,14336,8192,20480,140288,0,0,0,0,2752,896,3686400,256,512,1179648,1212416,51200,61440,126976,67108864,313344,314368,315392,157696,3840,98304,102400,106496,110592,28672,1966080,1970176,1974272,1978368,1982464,1986560,3436544,3526656,100352,16384,75776,896,17280,34816,43008,0,0,0,0,226304,176128,177152,178176,179200,184320,185344,186368,187392,188416,225280,313344,81920,114688,108544,135168,143360,94208,138240,124928,59392,160000,40960,0,100925952,235670024,370414096,505158168,639902240,774646312,909390384,1044134456,324480,3436544,3437568,3441664,3445760,3526656,3527680,3531776,3535872,655360,659456,92160,32768,34816,77824,0,1024,149504,1966080,3951616,0,0,0,0,0,0,0,0,0,0,0,0,
    71632,63312,54992,46672,38352,30032,21712,13392,328576,115712,8192,2048,8192,36608,160,3072,15360,9216,21504,140800,0,0,0,0,2752,1920,3817472,256,512,1703936,1736704,53248,62464,128000,67108864,316416,317440,318464,158208,3840,114688,118784,122880,126976,29184,1998848,2002944,2007040,2011136,2015232,2019328,3447808,3537920,114688,20480,79872,928,17312,43008,47104,0,0,0,0,228352,189440,190464,191488,194560,195584,196608,197632,198656,199680,227328,315392,86016,118784,112640,136192,144384,95232,138496,129024,63488,160064,49152,2048,100925952,235670024,370414096,505158168,639902240,774646312,909390384,1044134456,324480,3447808,3448832,3452928,3457024,3537920,3538944,3543040,3547136,671744,675840,106496,40960,43008,79872,16384,1280,151552,1998848,3952128,0,0,0,0,0,0,0,0,0,0,0,0,
    72672,64352,56032,47712,39392,31072,22752,14432,394112,123904,8192,6144,41472,36608,144,4096,16384,10240,22528,142336,1065353216,0,0,0,1024,0,67108864,16384,256,196608,229376,55296,63488,129024,67108864,319488,320512,321536,158784,3840,131072,135168,139264,143360,29696,2031616,2035712,2039808,2043904,2048000,2052096,3459072,3549184,129024,24576,83968,960,17344,51200,51200,0,0,0,0,230400,200704,201728,204800,205824,206848,207872,208896,209920,210944,229376,317440,90112,122880,116736,137216,145408,96256,138752,133120,67584,160128,57344,4096,4294967295,4294967295,4294967295,4294967295,4294967295,4294967295,4294967295,4294967295,324480,3459072,3460096,3464192,3468288,3549184,3550208,3554304,3558400,688128,692224,120832,49152,51200,81920,0,1536,153600,2031616,3952640,0,0,0,0,0,0,0,0,0,0,0,0,
    73712,65392,57072,48752,40432,32112,23792,15472,459648,132096,8192,4096,41472,36608,144,5120,17408,11264,23552,142848,1065353216,0,0,0,1024,0,67108864,16384,256,196608,229376,57344,64512,130048,67108864,322560,67108864,67108864,159296,3840,147456,151552,155648,159744,30208,2064384,2068480,2072576,2076672,2080768,2084864,3470336,3560448,143360,28672,88064,992,17376,59392,55296,0,0,0,0,232448,211968,215040,216064,217088,218112,219136,220160,221184,222208,231424,319488,94208,126976,120832,138240,146432,97280,139008,137216,71680,160192,65536,6144,4294967295,4294967295,4294967295,4294967295,4294967295,4294967295,4294967295,4294967295,324480,3470336,3471360,3475456,3479552,3560448,3561472,3565568,3569664,704512,708608,135168,57344,59392,83968,16384,1792,155648,2064384,3953152,0,0,0,0,0,0,0,0,0,0,0,0};

#define BUF(n) const void* p##n, unsigned s##n, unsigned f##n
__global__ void __launch_bounds__(512, 2) __attribute__((aligned(4096))) adaln_block(BUF(0), BUF(1), BUF(2), BUF(3), BUF(4), BUF(5), BUF(6), BUF(7), BUF(8), BUF(9), BUF(10), BUF(11)) {
#if defined(__HIP_DEVICE_COMPILE__)
    auto ka = __builtin_amdgcn_kernarg_segment_ptr();
    unsigned bid = __builtin_amdgcn_workgroup_id_x(), tid = __builtin_amdgcn_workitem_id_x();
    const unsigned* tab = kOffsets;
    asm volatile(
        "s_load_dwordx16 s[20:35], s[0:1], 0x40\n"
        "s_load_dwordx16 s[4:19], s[0:1], 0x0\n"
        "v_lshrrev_b32 v1, 6, v0\n"
        "v_and_b32 v2, 63, v0\n"
        "v_readfirstlane_b32 s3, v1\n"
        "s_load_dwordx4 s[36:39], s[0:1], 0x80\n"
        "v_lshlrev_b32 v53, 4, v2\n"
        "s_and_b32 s89, s2, 0x38\n"
        "s_add_u32 s89, s89, s3\n"
        "s_lshl_b32 s72, s89, 10\n"
        "s_lshl_b32 s89, s89, 9\n"
        "s_add_u32 s100, s100, s89\n"
        "s_addc_u32 s101, s101, 0\n"
        "s_and_b32 s88, s2, 7\n"
        "s_lshr_b32 s89, s2, 6\n"
        "s_lshl_b32 s44, s88, 2\n"
        "s_add_u32 s44, s44, s89\n"
        "s_lshl_b32 s90, s44, 16\n"
        "s_xor_b32 s73, s72, 8192\n"
        "s_xor_b32 s74, s72, 16384\n"
        "s_xor_b32 s75, s72, 24576\n"
        "s_xor_b32 s76, s72, 32768\n"
        "s_xor_b32 s77, s72, 40960\n"
        "s_xor_b32 s78, s72, 49152\n"
        "s_xor_b32 s79, s72, 57344\n"
        "s_cmp_ge_u32 s3, 4\n"
        "s_cbranch_scc1 skip_3\n"
        "v_lshrrev_b32 v3, 2, v0\n"
        "v_and_b32 v4, 3, v0\n"
        "v_mul_u32_u24 v5, 0x6000, v3\n"
        "v_lshl_or_b32 v5, v4, 4, v5\n"
        "v_lshlrev_b32 v186, 3, v3\n"
        "s_lshr_b32 s89, s2, 5\n"
        "s_lshl_b32 s89, s89, 3\n"
        "s_add_u32 s89, s89, s88\n"
        "s_lshl_b32 s89, s89, 1\n"
        "s_bfe_u32 s92, s2, 0x10003\n"
        "s_add_u32 s89, s89, s92\n"
        "s_bfe_u32 s92, s2, 0x10004\n"
        "s_mul_i32 s80, s92, 1572864\n"
        "s_lshl_b32 s81, s89, 6\n"
        "s_add_u32 s80, s80, s81\n"
        "s_add_u32 s81, s80, 12288\n"
        "s_bitcmp1_b32 s2, 3\n"
        "s_cselect_b32 s82, s80, s81\n"
        "s_cselect_b32 s80, s81, s80\n"
        "s_add_u32 s81, s80, 6144\n"
        "s_add_u32 s83, s82, 6144\n"
        "s_mul_i32 s87, s92, 96\n"
        "s_add_u32 s87, s87, s89\n"
        "s_lshl_b32 s87, s87, 7\n"
        "s_lshl_b32 s84, s92, 9\n"
        "v_mov_b64 v[28:29], -1\n"
        "v_mov_b64 v[30:31], -1\n"
        "v_lshlrev_b32 v10, 3, v2\n"
        "v_add_u32 v10, 144384, v10\n"
        "ds_write_b64 v10, v[28:29]\n"
        "s_lshl_b32 s86, s2, 1\n"
        "s_add_u32 s86, s86, s3\n"
        "s_lshl_b32 s86, s86, 10\n"
        "s_add_u32 s86, s86, 32768\n"
        "s_lshr_b32 s89, s2, 5\n"
        "s_or_b32 s89, s89, s3\n"
        "s_cmp_ge_u32 s89, 2\n"
        "s_cselect_b32 s86, 67108864, s86\n"
        "s_cmp_ge_u32 s2, 192\n"
        "s_waitcnt lgkmcnt(0)\n"
        "s_cbranch_scc1 skip_4\n"
        "buffer_load_dwordx2 v[176:177], v186, s[20:23], s84 offen\n"
        "buffer_load_dwordx4 v[160:163], v5, s[24:27], s80 offen\n"
        "buffer_load_dwordx4 v[164:167], v5, s[24:27], s81 offen\n"
        "buffer_load_dwordx4 v[168:171], v5, s[24:27], s82 offen\n"
        "buffer_load_dwordx4 v[172:175], v5, s[24:27], s83 offen\n"
        "buffer_store_dwordx4 v[28:31], v53, s[32:35], s86 offen sc1\n"
        "skip_4:\n"
        "skip_3:\n"
        "s_barrier\n"
        "s_waitcnt lgkmcnt(0)\n"
        "buffer_load_dword v26, off, s[32:35], 0 sc1\n"
        "s_add_u32 s4, s4, s90\n"
        "s_addc_u32 s5, s5, 0\n"
        "buffer_load_dwordx4 v[64:67], v53, s[4:7], s72 offen\n"
        "s_cmp_lt_u32 s3, 4\n"
        "s_cbranch_scc1 skip_7\n"
        "s_mov_b64 exec, 0\n"
        "s_cmp_lg_u32 s3, 4\n"
        "s_cbranch_scc1 warm_9\n"
        "s_call_b64 s[88:89], page1\n"
        "s_branch warmed_8\n"
        "warm_9:\n"
        "s_cmp_lg_u32 s3, 5\n"
        "s_cbranch_scc1 warm_10\n"
        "s_call_b64 s[88:89], young_1\n"
        "s_branch warmed_8\n"
        "warm_10:\n"
        "s_cmp_lg_u32 s3, 6\n"
        "s_cbranch_scc1 warm_11\n"
        "s_call_b64 s[88:89], page2\n"
        "s_branch warmed_8\n"
        "warm_11:\n"
        "s_call_b64 s[88:89], qgemm_5\n"
        "warmed_8:\n"
        "s_mov_b64 exec, -1\n"
        "skip_7:\n"
        "s_cmp_lt_u32 s3, 7\n"
        "s_cbranch_scc1 skip_13\n"
        "s_lshl_b32 s40, s2, 11\n"
        "v_and_b32 v10, 15, v2\n"
        "v_lshlrev_b32 v10, 7, v10\n"
        "buffer_load_dword v11, v10, s[8:11], s40 offen\n"
        "s_lshl_b32 s40, s2, 6\n"
        "buffer_load_dword v8, off, s[12:15], s40\n"
        "buffer_load_dword v9, off, s[16:19], s40\n"
        "skip_13:\n"
        "s_mov_b32 s94, 0x3f803f80\n"
        "v_add_u32 v27, 140288, v53\n"
        "v_lshl_or_b32 v54, v1, 11, v53\n"
        "v_add_u32 v54, 8192, v54\n"
        "v_xor_b32 v55, 0x480, v54\n"
        "v_and_b32 v48, 31, v2\n"
        "v_lshlrev_b32 v48, 5, v48\n"
        "v_lshl_or_b32 v48, v1, 2, v48\n"
        "v_add_u32 v56, 24576, v48\n"
        "buffer_load_dwordx4 v[68:71], v53, s[4:7], s73 offen\n"
        "v_mov_b32 v52, 0xdddddddd\n"
        "v_and_b32 v48, 11, v2\n"
        "v_cmp_eq_u32 vcc, 0, v48\n"
        "v_mov_b32 v48, s94\n"
        "v_lshrrev_b32 v49, 5, v2\n"
        "v_cndmask_b32 v51, 0, v48, vcc\n"
        "v_and_b32 v48, 0xf0, v53\n"
        "v_lshl_or_b32 v61, v49, 8, v48\n"
        "v_mov_b32 v50, v51\n"
        "buffer_load_dwordx4 v[72:75], v53, s[4:7], s74 offen\n"
        "v_and_b32 v4, 3, v2\n"
        "v_lshrrev_b32 v3, 4, v2\n"
        "v_bfe_u32 v21, v2, 2, 2\n"
        "v_lshl_or_b32 v60, v3, 3, v21\n"
        "v_lshlrev_b32 v60, 5, v60\n"
        "v_lshl_or_b32 v60, v4, 3, v60\n"
        "v_lshl_or_b32 v16, v4, 3, v3\n"
        "v_lshlrev_b32 v16, 4, v16\n"
        "buffer_load_dwordx4 v[76:79], v53, s[4:7], s75 offen\n"
        "v_lshl_or_b32 v16, v1, 2, v16\n"
        "v_add_u32 v187, 144384, v16\n"
        "v_and_b32 v17, 7, v2\n"
        "v_mul_u32_u24 v18, 1040, v17\n"
        "v_lshl_add_u32 v58, v3, 4, v18\n"
        "v_and_b32 v22, 1, v3\n"
        "v_lshlrev_b32 v19, 9, v22\n"
        "v_lshl_or_b32 v19, v17, 4, v19\n"
        "buffer_load_dwordx4 v[80:83], v53, s[4:7], s76 offen\n"
        "v_lshl_or_b32 v19, v49, 3, v19\n"
        "v_lshl_or_b32 v18, v22, 3, v48\n"
        "v_cmp_eq_u32 vcc, 0, v1\n"
        "v_lshrrev_b32 v23, 1, v2\n"
        "v_and_b32 v20, 1, v2\n"
        "v_cndmask_b32 v59, v18, v19, vcc\n"
        "v_lshlrev_b32 v6, 4, v23\n"
        "v_and_b32 v18, 1, v1\n"
        "v_lshl_or_b32 v24, v18, 9, v6\n"
        "buffer_load_dwordx4 v[84:87], v53, s[4:7], s77 offen\n"
        "v_lshl_or_b32 v6, v20, 10, v6\n"
        "v_mul_u32_u24 v19, 0x480, v20\n"
        "v_xor_b32 v24, v24, v19\n"
        "v_lshrrev_b32 v19, 1, v1\n"
        "v_mul_u32_u24 v19, 17408, v19\n"
        "v_add_u32 v19, 8192, v19\n"
        "v_add_u32 v57, v24, v19\n"
        "v_lshl_add_u32 v62, v23, 5, v19\n"
        "buffer_load_dwordx4 v[88:91], v53, s[4:7], s78 offen\n"
        "v_add_u32 v62, 0x4000, v62\n"
        "v_lshlrev_b32 v19, 2, v22\n"
        "v_xor_b32 v19, v19, v17\n"
        "v_lshlrev_b32 v19, 4, v19\n"
        "v_lshrrev_b32 v7, 3, v2\n"
        "v_cmp_gt_u32 vcc, 2, v1\n"
        "v_lshl_or_b32 v7, v7, 10, v19\n"
        "v_lshl_or_b32 v19, v49, 3, v21\n"
        "v_cndmask_b32 v7, v7, v53, vcc\n"
        "buffer_load_dwordx4 v[92:95], v53, s[4:7], s79 offen\n"
        "v_lshlrev_b32 v19, 7, v19\n"
        "v_lshrrev_b32 v21, 1, v21\n"
        "v_xor_b32 v21, v18, v21\n"
        "v_lshl_or_b32 v19, v21, 6, v19\n"
        "v_lshl_or_b32 v19, v22, 5, v19\n"
        "v_lshl_or_b32 v19, v4, 3, v19\n"
        "v_cmp_gt_u32 vcc, 4, v1\n"
        "v_and_b32 v21, 31, v2\n"
        "v_mul_u32_u24 v21, 1040, v21\n"
        "v_lshl_add_u32 v21, v49, 4, v21\n"
        "v_cndmask_b32 v60, v19, v60, vcc\n"
        "v_cndmask_b32 v58, v21, v58, vcc\n"
        "v_cndmask_b32 v59, v53, v59, vcc\n"
        "v_lshl_or_b32 v12, v3, 9, v48\n"
        "v_and_b32 v13, 0x2f0, v12\n"
        "v_lshl_or_b32 v13, v49, 3, v13\n"
        "v_lshl_or_b32 v15, v3, 2, v48\n"
        "v_bfe_u32 v19, v2, 1, 3\n"
        "v_lshlrev_b32 v22, 1, v22\n"
        "v_cmp_eq_u32 vcc, v19, v22\n"
        "v_mul_u32_u24 v19, 1088, v20\n"
        "v_sub_u32 v19, 158784, v19\n"
        "v_lshl_add_u32 v19, v49, 4, v19\n"
        "v_mov_b32 v14, 161920\n"
        "v_cndmask_b32 v14, v14, v19, vcc\n"
        "v_mov_b32 v188, v14\n"
        "v_add_u32 v19, 160768, v53\n"
        "ds_write_b128 v19, v[12:15]\n"
        "v_mov_b32 v18, 0\n"
        "v_and_b32 v19, 0xff, v0\n"
        "v_lshlrev_b32 v19, 2, v19\n"
        "v_add_u32 v19, 161920, v19\n"
        "ds_write_b32 v19, v18\n"
        "s_mul_i32 s91, s44, 324608\n"
        "s_add_u32 s28, s28, s90\n"
        "s_addc_u32 s29, s29, 0\n"
        "s_add_u32 s36, s36, s91\n"
        "s_addc_u32 s37, s37, 0\n"
        "s_mov_b32 s46, 65536\n"
        "s_mov_b32 s47, 0\n"
        "s_mov_b32 s93, 0xffff0000\n"
        "s_cmp_lt_u32 s3, 4\n"
        "s_cbranch_scc1 skip_14\n"
        "s_getpc_b64 s[40:41]\n"
        "code_here:\n"
        "s_lshr_b32 s42, s2, 3\n"
        "s_lshl_b32 s42, s42, 2\n"
        "s_add_u32 s42, s42, s3\n"
        "s_sub_u32 s42, s42, 4\n"
        "s_lshl_b32 s42, s42, 6\n"
        "s_load_dword s85, s[100:101], -0x800\n"
        "s_load_dword s85, s[100:101], -0x7c0\n"
        "s_branch young_1\n"
        "warm_back:\n"
        "s_setpc_b64 s[88:89]\n"
        "skip_14:\n"
        "s_cmp_ge_u32 s2, 192\n"
        "s_cbranch_scc1 skip_15\n"
        "s_mov_b32 s88, 0x05040100\n"
        "s_mov_b32 s89, 0x07060302\n"
        "s_waitcnt vmcnt(14)\n"
        "v_lshlrev_b32 v8, 16, v176\n"
        "v_and_b32 v9, 0xffff0000, v176\n"
        "v_lshlrev_b32 v10, 16, v177\n"
        "v_and_b32 v11, 0xffff0000, v177\n"
        "v_mul_f32 v178, 0xbfb8aa3b, v8\n"
        "v_mul_f32 v179, 0xbfb8aa3b, v9\n"
        "v_mul_f32 v180, 0xbfb8aa3b, v10\n"
        "v_mul_f32 v181, 0xbfb8aa3b, v11\n"
        "v_exp_f32 v178, v178\n"
        "v_exp_f32 v179, v179\n"
        "v_exp_f32 v180, v180\n"
        "v_exp_f32 v181, v181\n"
        "v_add_f32 v178, 1.0, v178\n"
        "v_add_f32 v179, 1.0, v179\n"
        "v_add_f32 v180, 1.0, v180\n"
        "v_add_f32 v181, 1.0, v181\n"
        "v_rcp_f32 v178, v178\n"
        "v_rcp_f32 v179, v179\n"
        "v_rcp_f32 v180, v180\n"
        "v_rcp_f32 v181, v181\n"
        "v_mul_f32 v8, v8, v178\n"
        "v_mul_f32 v9, v9, v179\n"
        "v_mul_f32 v10, v10, v180\n"
        "v_mul_f32 v11, v11, v181\n"
        "v_cvt_pk_bf16_f32 v178, v8, v9\n"
        "v_cvt_pk_bf16_f32 v9, v10, v11\n"
        "s_bitcmp1_b32 s2, 3\n"
        "s_cselect_b64 vcc, -1, 0\n"
        "v_cndmask_b32 v8, v178, v9, vcc\n"
        "v_cndmask_b32 v9, v9, v178, vcc\n"
        "s_waitcnt vmcnt(12)\n"
        "v_perm_b32 v178, v164, v160, s88\n"
        "v_perm_b32 v179, v164, v160, s89\n"
        "v_perm_b32 v180, v165, v161, s88\n"
        "v_perm_b32 v181, v165, v161, s89\n"
        "v_perm_b32 v182, v166, v162, s88\n"
        "v_perm_b32 v183, v166, v162, s89\n"
        "v_perm_b32 v184, v167, v163, s88\n"
        "v_perm_b32 v185, v167, v163, s89\n"
        "v_dot2_f32_bf16 v160, v178, v8, 0\n"
        "v_dot2_f32_bf16 v161, v179, v8, 0\n"
        "v_dot2_f32_bf16 v162, v180, v8, 0\n"
        "v_dot2_f32_bf16 v163, v181, v8, 0\n"
        "v_dot2_f32_bf16 v164, v182, v8, 0\n"
        "v_dot2_f32_bf16 v165, v183, v8, 0\n"
        "v_dot2_f32_bf16 v166, v184, v8, 0\n"
        "v_dot2_f32_bf16 v167, v185, v8, 0\n"
        "s_waitcnt vmcnt(10)\n"
        "v_perm_b32 v178, v172, v168, s88\n"
        "v_perm_b32 v179, v172, v168, s89\n"
        "v_perm_b32 v180, v173, v169, s88\n"
        "v_perm_b32 v181, v173, v169, s89\n"
        "v_perm_b32 v182, v174, v170, s88\n"
        "v_perm_b32 v183, v174, v170, s89\n"
        "v_perm_b32 v184, v175, v171, s88\n"
        "v_perm_b32 v185, v175, v171, s89\n"
        "v_dot2c_f32_bf16 v160, v178, v9\n"
        "v_dot2c_f32_bf16 v161, v179, v9\n"
        "v_dot2c_f32_bf16 v162, v180, v9\n"
        "v_dot2c_f32_bf16 v163, v181, v9\n"
        "v_dot2c_f32_bf16 v164, v182, v9\n"
        "v_dot2c_f32_bf16 v165, v183, v9\n"
        "v_dot2c_f32_bf16 v166, v184, v9\n"
        "v_dot2c_f32_bf16 v167, v185, v9\n"
        "v_permlane32_swap_b32 v160, v162\n"
        "v_permlane32_swap_b32 v161, v163\n"
        "v_permlane32_swap_b32 v164, v166\n"
        "v_permlane32_swap_b32 v165, v167\n"
        "v_add_f32 v160, v160, v162\n"
        "v_add_f32 v161, v161, v163\n"
        "v_add_f32 v164, v164, v166\n"
        "v_add_f32 v165, v165, v167\n"
        "v_and_b32 v10, 0x1f0, v53\n"
        "v_permlane16_swap_b32 v160, v161\n"
        "v_permlane16_swap_b32 v164, v165\n"
        "v_add_f32 v160, v160, v161\n"
        "v_add_f32 v164, v164, v165\n"
        "v_add_u32 v11, 144384, v10\n"
        "v_add_f32_dpp v160, v160, v160 row_ror:8 row_mask:0xf bank_mask:0xf bound_ctrl:1\n"
        "v_add_f32_dpp v164, v164, v164 row_ror:8 row_mask:0xf bank_mask:0xf bound_ctrl:1\n"
        "v_lshrrev_b32 v10, 2, v10\n"
        "v_add_f32_dpp v160, v160, v160 row_ror:4 row_mask:0xf bank_mask:0xf bound_ctrl:1\n"
        "v_add_f32_dpp v164, v164, v164 row_ror:4 row_mask:0xf bank_mask:0xf bound_ctrl:1\n"
        "v_min_u32 v160, -2, v160\n"
        "v_min_u32 v164, -2, v164\n"
        "s_waitcnt vmcnt(9)\n"
        "ds_write2_b32 v187, v160, v164 offset0:0 offset1:16\n"
        "s_cmp_lg_u32 s3, 3\n"
        "s_cbranch_scc1 skip_15\n"
        "s_waitcnt vmcnt(8)\n"
        "v_add_u32 v9, 1, v26\n"
        "s_movk_i32 s88, 0xff\n"
        "sums_16:\n"
        "ds_read_b128 v[160:163], v11\n"
        "s_waitcnt lgkmcnt(0)\n"
        "v_max3_u32 v8, v160, v161, v162\n"
        "v_max_u32 v8, v8, v163\n"
        "v_cmp_eq_u32 vcc, -1, v8\n"
        "s_cbranch_vccnz sums_16\n"
        "v_add_f32 v160, v160, v161\n"
        "v_add_f32 v162, v162, v163\n"
        "v_add_f32 v160, v160, v162\n"
        "v_bfi_b32 v160, s88, v9, v160\n"
        "buffer_store_dword v160, v10, s[32:35], s87 offen offset:2048 sc1\n"
        "skip_15:\n"
        "s_load_dwordx16 s[48:63], s[100:101], 0x0\n"
        "s_load_dwordx8 s[64:71], s[100:101], 0x40\n"
        "s_cmp_eq_u32 s3, 0\n"
        "s_cselect_b64 s[16:17], s[12:13], s[16:17]\n"
        "s_cmp_lt_u32 s3, 2\n"
        "s_cselect_b64 s[8:9], s[16:17], s[8:9]\n"
        "s_cselect_b32 s10, s14, s10\n"
        "s_waitcnt vmcnt(7)\n"
        "v_dot2_f32_bf16 v40, v64, s94, 0\n"
        "v_dot2_f32_bf16 v44, v64, v64, 0\n"
        "s_waitcnt vmcnt(6)\n"
        "v_dot2_f32_bf16 v41, v68, s94, 0\n"
        "v_dot2_f32_bf16 v45, v68, v68, 0\n"
        "s_waitcnt vmcnt(5)\n"
        "v_dot2_f32_bf16 v42, v72, s94, 0\n"
        "v_dot2_f32_bf16 v46, v72, v72, 0\n"
        "s_waitcnt vmcnt(4)\n"
        "v_dot2_f32_bf16 v43, v76, s94, 0\n"
        "v_dot2_f32_bf16 v47, v76, v76, 0\n"
        "v_dot2c_f32_bf16 v40, s94, v65\n"
        "v_dot2c_f32_bf16 v44, v65, v65\n"
        "v_dot2c_f32_bf16 v41, s94, v69\n"
        "v_dot2c_f32_bf16 v45, v69, v69\n"
        "v_dot2c_f32_bf16 v42, s94, v73\n"
        "v_dot2c_f32_bf16 v46, v73, v73\n"
        "v_dot2c_f32_bf16 v43, s94, v77\n"
        "v_dot2c_f32_bf16 v47, v77, v77\n"
        "s_waitcnt lgkmcnt(0)\n"
        "s_mov_b32 m0, s57\n"
        "s_add_u32 s57, s57, 1024\n"
        "buffer_load_dwordx4 v7, s[8:11], s56 offen lds\n"
        "s_add_u32 s56, s56, s58\n"
        "v_dot2c_f32_bf16 v40, s94, v66\n"
        "v_dot2c_f32_bf16 v44, v66, v66\n"
        "v_dot2c_f32_bf16 v41, s94, v70\n"
        "v_dot2c_f32_bf16 v45, v70, v70\n"
        "v_dot2c_f32_bf16 v42, s94, v74\n"
        "v_dot2c_f32_bf16 v46, v74, v74\n"
        "s_mov_b32 m0, s57\n"
        "s_add_u32 s57, s57, 1024\n"
        "buffer_load_dwordx4 v7, s[8:11], s56 offen lds\n"
        "s_add_u32 s56, s56, s58\n"
        "v_dot2c_f32_bf16 v43, s94, v78\n"
        "v_dot2c_f32_bf16 v47, v78, v78\n"
        "v_dot2c_f32_bf16 v40, s94, v67\n"
        "v_dot2c_f32_bf16 v44, v67, v67\n"
        "v_dot2c_f32_bf16 v41, s94, v71\n"
        "v_dot2c_f32_bf16 v45, v71, v71\n"
        "s_mov_b32 m0, s57\n"
        "s_add_u32 s57, s57, 1024\n"
        "buffer_load_dwordx4 v7, s[8:11], s56 offen lds\n"
        "s_add_u32 s56, s56, s58\n"
        "v_dot2c_f32_bf16 v42, s94, v75\n"
        "v_dot2c_f32_bf16 v46, v75, v75\n"
        "v_dot2c_f32_bf16 v43, s94, v79\n"
        "v_dot2c_f32_bf16 v47, v79, v79\n"
        "v_lshlrev_b32 v96, 16, v64\n"
        "v_and_b32 v97, s93, v64\n"
        "s_mov_b32 m0, s57\n"
        "s_add_u32 s57, s57, 1024\n"
        "buffer_load_dwordx4 v7, s[8:11], s56 offen lds\n"
        "s_add_u32 s56, s56, s58\n"
        "v_lshlrev_b32 v104, 16, v68\n"
        "v_and_b32 v105, s93, v68\n"
        "v_lshlrev_b32 v112, 16, v72\n"
        "v_and_b32 v113, s93, v72\n"
        "v_lshlrev_b32 v120, 16, v76\n"
        "v_and_b32 v121, s93, v76\n"
        "s_mov_b32 m0, s57\n"
        "s_add_u32 s57, s57, 1024\n"
        "buffer_load_dwordx4 v7, s[8:11], s56 offen lds\n"
        "s_add_u32 s56, s56, s58\n"
        "v_permlane32_swap_b32 v40, v41\n"
        "v_permlane32_swap_b32 v42, v43\n"
        "v_permlane32_swap_b32 v44, v45\n"
        "v_permlane32_swap_b32 v46, v47\n"
        "v_add_f32 v40, v40, v41\n"
        "v_add_f32 v42, v42, v43\n"
        "s_mov_b32 m0, s57\n"
        "s_add_u32 s57, s57, 1024\n"
        "buffer_load_dwordx4 v7, s[8:11], s56 offen lds\n"
        "s_add_u32 s56, s56, s58\n"
        "v_add_f32 v44, v44, v45\n"
        "v_add_f32 v46, v46, v47\n"
        "v_lshlrev_b32 v98, 16, v65\n"
        "v_and_b32 v99, s93, v65\n"
        "v_permlane16_swap_b32 v40, v42\n"
        "v_permlane16_swap_b32 v44, v46\n"
        "s_mov_b32 m0, s57\n"
        "s_add_u32 s57, s57, 1024\n"
        "buffer_load_dwordx4 v7, s[8:11], s56 offen lds\n"
        "s_add_u32 s56, s56, s58\n"
        "v_add_f32 v40, v40, v42\n"
        "v_add_f32 v44, v44, v46\n"
        "v_lshlrev_b32 v106, 16, v69\n"
        "v_and_b32 v107, s93, v69\n"
        "v_lshlrev_b32 v114, 16, v73\n"
        "v_and_b32 v115, s93, v73\n"
        "s_mov_b32 m0, s57\n"
        "s_add_u32 s57, s57, 1024\n"
        "buffer_load_dwordx4 v7, s[8:11], s56 offen lds\n"
        "s_add_u32 s56, s56, s58\n"
        "v_lshlrev_b32 v122, 16, v77\n"
        "v_and_b32 v123, s93, v77\n"
        "v_add_f32_dpp v40, v40, v40 row_ror:8 row_mask:0xf bank_mask:0xf bound_ctrl:1\n"
        "v_add_f32_dpp v44, v44, v44 row_ror:8 row_mask:0xf bank_mask:0xf bound_ctrl:1\n"
        "v_lshlrev_b32 v100, 16, v66\n"
        "v_and_b32 v101, s93, v66\n"
        "s_mov_b32 m0, s57\n"
        "s_add_u32 s57, s57, 1024\n"
        "buffer_load_dwordx4 v7, s[8:11], s56 offen lds\n"
        "s_add_u32 s56, s56, s58\n"
        "v_add_f32_dpp v40, v40, v40 row_ror:4 row_mask:0xf bank_mask:0xf bound_ctrl:1\n"
        "v_add_f32_dpp v44, v44, v44 row_ror:4 row_mask:0xf bank_mask:0xf bound_ctrl:1\n"
        "v_lshlrev_b32 v108, 16, v70\n"
        "v_and_b32 v109, s93, v70\n"
        "v_add_f32_dpp v40, v40, v40 row_ror:2 row_mask:0xf bank_mask:0xf bound_ctrl:1\n"
        "v_add_f32_dpp v44, v44, v44 row_ror:2 row_mask:0xf bank_mask:0xf bound_ctrl:1\n"
        "s_mov_b32 m0, s57\n"
        "s_add_u32 s57, s57, 1024\n"
        "buffer_load_dwordx4 v7, s[8:11], s56 offen lds\n"
        "s_add_u32 s56, s56, s58\n"
        "v_lshlrev_b32 v116, 16, v74\n"
        "v_and_b32 v117, s93, v74\n"
        "v_add_f32_dpp v40, v40, v40 row_ror:1 row_mask:0xf bank_mask:0xf bound_ctrl:1\n"
        "v_add_f32_dpp v44, v44, v44 row_ror:1 row_mask:0xf bank_mask:0xf bound_ctrl:1\n"
        "v_lshlrev_b32 v124, 16, v78\n"
        "v_and_b32 v125, s93, v78\n"
        "s_mov_b32 m0, s57\n"
        "s_add_u32 s57, s57, 1024\n"
        "buffer_load_dwordx4 v7, s[8:11], s56 offen lds\n"
        "s_add_u32 s56, s56, s58\n"
        "v_mul_f32 v40, 0xbb000000, v40\n"
        "v_mul_f32 v41, v40, v40\n"
        "v_mov_b32 v42, 0x3b000000\n"
        "v_fma_f32 v44, v44, v42, -v41\n"
        "v_max_f32 v44, 0, v44\n"
        "v_add_f32 v44, 0x3727c5ac, v44\n"
        "s_mov_b32 m0, s57\n"
        "s_add_u32 s57, s57, 1024\n"
        "buffer_load_dwordx4 v7, s[8:11], s56 offen lds\n"
        "s_add_u32 s56, s56, s58\n"
        "v_rsq_f32 v44, v44\n"
        "v_lshlrev_b32 v102, 16, v67\n"
        "v_and_b32 v103, s93, v67\n"
        "v_mul_f32 v40, v40, v44\n"
        "v_lshlrev_b32 v110, 16, v71\n"
        "v_and_b32 v111, s93, v71\n"
        "s_mov_b32 m0, s57\n"
        "s_add_u32 s57, s57, 1024\n"
        "buffer_load_dwordx4 v7, s[8:11], s56 offen lds\n"
        "s_add_u32 s56, s56, s58\n"
        "v_lshlrev_b32 v118, 16, v75\n"
        "v_and_b32 v119, s93, v75\n"
        "v_lshlrev_b32 v126, 16, v79\n"
        "v_and_b32 v127, s93, v79\n"
        "v_readlane_b32 s72, v44, 0\n"
        "v_readlane_b32 s73, v40, 0\n"
        "s_mov_b32 m0, s57\n"
        "s_add_u32 s57, s57, 1024\n"
        "buffer_load_dwordx4 v7, s[8:11], s56 offen lds\n"
        "s_add_u32 s56, s56, s58\n"
        "v_readlane_b32 s74, v44, 32\n"
        "v_readlane_b32 s75, v40, 32\n"
        "v_readlane_b32 s76, v44, 16\n"
        "v_readlane_b32 s77, v40, 16\n"
        "v_readlane_b32 s78, v44, 48\n"
        "v_readlane_b32 s79, v40, 48\n"
        "s_mov_b32 m0, s57\n"
        "s_add_u32 s57, s57, 1024\n"
        "buffer_load_dwordx4 v7, s[8:11], s56 offen lds\n"
        "s_add_u32 s56, s56, s58\n"
        "v_pk_fma_f32 v[96:97], v[96:97], s[72:73], s[72:73] op_sel:[0,0,1] op_sel_hi:[1,0,1]\n"
        "v_pk_fma_f32 v[98:99], v[98:99], s[72:73], s[72:73] op_sel:[0,0,1] op_sel_hi:[1,0,1]\n"
        "v_pk_fma_f32 v[100:101], v[100:101], s[72:73], s[72:73] op_sel:[0,0,1] op_sel_hi:[1,0,1]\n"
        "v_pk_fma_f32 v[102:103], v[102:103], s[72:73], s[72:73] op_sel:[0,0,1] op_sel_hi:[1,0,1]\n"
        "v_pk_fma_f32 v[104:105], v[104:105], s[74:75], s[74:75] op_sel:[0,0,1] op_sel_hi:[1,0,1]\n"
        "v_pk_fma_f32 v[106:107], v[106:107], s[74:75], s[74:75] op_sel:[0,0,1] op_sel_hi:[1,0,1]\n"
        "s_mov_b32 m0, s57\n"
        "s_add_u32 s57, s57, 1024\n"
        "buffer_load_dwordx4 v7, s[8:11], s56 offen lds\n"
        "s_add_u32 s56, s56, s58\n"
        "v_pk_fma_f32 v[108:109], v[108:109], s[74:75], s[74:75] op_sel:[0,0,1] op_sel_hi:[1,0,1]\n"
        "v_pk_fma_f32 v[110:111], v[110:111], s[74:75], s[74:75] op_sel:[0,0,1] op_sel_hi:[1,0,1]\n"
        "v_pk_fma_f32 v[112:113], v[112:113], s[76:77], s[76:77] op_sel:[0,0,1] op_sel_hi:[1,0,1]\n"
        "v_pk_fma_f32 v[114:115], v[114:115], s[76:77], s[76:77] op_sel:[0,0,1] op_sel_hi:[1,0,1]\n"
        "v_pk_fma_f32 v[116:117], v[116:117], s[76:77], s[76:77] op_sel:[0,0,1] op_sel_hi:[1,0,1]\n"
        "v_pk_fma_f32 v[118:119], v[118:119], s[76:77], s[76:77] op_sel:[0,0,1] op_sel_hi:[1,0,1]\n"
        "v_pk_fma_f32 v[120:121], v[120:121], s[78:79], s[78:79] op_sel:[0,0,1] op_sel_hi:[1,0,1]\n"
        "v_pk_fma_f32 v[122:123], v[122:123], s[78:79], s[78:79] op_sel:[0,0,1] op_sel_hi:[1,0,1]\n"
        "v_pk_fma_f32 v[124:125], v[124:125], s[78:79], s[78:79] op_sel:[0,0,1] op_sel_hi:[1,0,1]\n"
        "v_pk_fma_f32 v[126:127], v[126:127], s[78:79], s[78:79] op_sel:[0,0,1] op_sel_hi:[1,0,1]\n"
        "v_readfirstlane_b32 s45, v26\n"
        "v_mov_b32 v14, v27\n"
        "v_add_u32 v58, s60, v58\n"
        "v_add_u32 v13, s67, v6\n"
        "v_add_u32 v60, s59, v60\n"
        "s_add_u32 s45, s45, 1\n"
        "s_and_b32 s92, s45, 0xff\n"
        "s_waitcnt vmcnt(19)\n"
        "v_dot2_f32_bf16 v40, v80, s94, 0\n"
        "v_dot2_f32_bf16 v44, v80, v80, 0\n"
        "s_waitcnt vmcnt(18)\n"
        "v_dot2_f32_bf16 v41, v84, s94, 0\n"
        "v_dot2_f32_bf16 v45, v84, v84, 0\n"
        "s_waitcnt vmcnt(17)\n"
        "v_dot2_f32_bf16 v42, v88, s94, 0\n"
        "v_dot2_f32_bf16 v46, v88, v88, 0\n"
        "s_waitcnt vmcnt(16)\n"
        "v_dot2_f32_bf16 v43, v92, s94, 0\n"
        "v_dot2_f32_bf16 v47, v92, v92, 0\n"
        "v_dot2c_f32_bf16 v40, s94, v81\n"
        "v_dot2c_f32_bf16 v44, v81, v81\n"
        "v_dot2c_f32_bf16 v41, s94, v85\n"
        "buffer_load_dwordx4 v[16:19], v53, s[32:35], s63 offen sc1\n"
        "buffer_load_dwordx4 v[20:23], v53, s[32:35], s64 offen sc1\n"
        "v_dot2c_f32_bf16 v45, v85, v85\n"
        "v_dot2c_f32_bf16 v42, s94, v89\n"
        "v_dot2c_f32_bf16 v46, v89, v89\n"
        "v_dot2c_f32_bf16 v43, s94, v93\n"
        "v_dot2c_f32_bf16 v47, v93, v93\n"
        "v_dot2c_f32_bf16 v40, s94, v82\n"
        "v_dot2c_f32_bf16 v44, v82, v82\n"
        "v_dot2c_f32_bf16 v41, s94, v86\n"
        "v_dot2c_f32_bf16 v45, v86, v86\n"
        "v_dot2c_f32_bf16 v42, s94, v90\n"
        "v_dot2c_f32_bf16 v46, v90, v90\n"
        "v_dot2c_f32_bf16 v43, s94, v94\n"
        "v_dot2c_f32_bf16 v47, v94, v94\n"
        "v_dot2c_f32_bf16 v40, s94, v83\n"
        "v_dot2c_f32_bf16 v44, v83, v83\n"
        "v_dot2c_f32_bf16 v41, s94, v87\n"
        "v_dot2c_f32_bf16 v45, v87, v87\n"
        "v_dot2c_f32_bf16 v42, s94, v91\n"
        "v_dot2c_f32_bf16 v46, v91, v91\n"
        "v_dot2c_f32_bf16 v43, s94, v95\n"
        "v_dot2c_f32_bf16 v47, v95, v95\n"
        "v_lshlrev_b32 v128, 16, v80\n"
        "v_and_b32 v129, s93, v80\n"
        "v_lshlrev_b32 v136, 16, v84\n"
        "v_and_b32 v137, s93, v84\n"
        "v_lshlrev_b32 v144, 16, v88\n"
        "v_and_b32 v145, s93, v88\n"
        "v_lshlrev_b32 v152, 16, v92\n"
        "v_and_b32 v153, s93, v92\n"
        "v_permlane32_swap_b32 v40, v41\n"
        "v_permlane32_swap_b32 v42, v43\n"
        "v_permlane32_swap_b32 v44, v45\n"
        "v_permlane32_swap_b32 v46, v47\n"
        "v_add_f32 v40, v40, v41\n"
        "v_add_f32 v42, v42, v43\n"
        "v_add_f32 v44, v44, v45\n"
        "v_add_f32 v46, v46, v47\n"
        "v_lshlrev_b32 v130, 16, v81\n"
        "v_and_b32 v131, s93, v81\n"
        "v_permlane16_swap_b32 v40, v42\n"
        "v_permlane16_swap_b32 v44, v46\n"
        "v_add_f32 v40, v40, v42\n"
        "v_add_f32 v44, v44, v46\n"
        "v_lshlrev_b32 v138, 16, v85\n"
        "v_and_b32 v139, s93, v85\n"
        "v_lshlrev_b32 v146, 16, v89\n"
        "v_and_b32 v147, s93, v89\n"
        "v_lshlrev_b32 v154, 16, v93\n"
        "v_and_b32 v155, s93, v93\n"
        "v_add_f32_dpp v40, v40, v40 row_ror:8 row_mask:0xf bank_mask:0xf bound_ctrl:1\n"
        "v_add_f32_dpp v44, v44, v44 row_ror:8 row_mask:0xf bank_mask:0xf bound_ctrl:1\n"
        "v_lshlrev_b32 v132, 16, v82\n"
        "v_and_b32 v133, s93, v82\n"
        "v_add_f32_dpp v40, v40, v40 row_ror:4 row_mask:0xf bank_mask:0xf bound_ctrl:1\n"
        "v_add_f32_dpp v44, v44, v44 row_ror:4 row_mask:0xf bank_mask:0xf bound_ctrl:1\n"
        "v_lshlrev_b32 v140, 16, v86\n"
        "v_and_b32 v141, s93, v86\n"
        "v_add_f32_dpp v40, v40, v40 row_ror:2 row_mask:0xf bank_mask:0xf bound_ctrl:1\n"
        "v_add_f32_dpp v44, v44, v44 row_ror:2 row_mask:0xf bank_mask:0xf bound_ctrl:1\n"
        "v_lshlrev_b32 v148, 16, v90\n"
        "v_and_b32 v149, s93, v90\n"
        "v_add_f32_dpp v40, v40, v40 row_ror:1 row_mask:0xf bank_mask:0xf bound_ctrl:1\n"
        "v_add_f32_dpp v44, v44, v44 row_ror:1 row_mask:0xf bank_mask:0xf bound_ctrl:1\n"
        "v_lshlrev_b32 v156, 16, v94\n"
        "v_and_b32 v157, s93, v94\n"
        "v_mul_f32 v40, 0xbb000000, v40\n"
        "v_mul_f32 v41, v40, v40\n"
        "v_mov_b32 v42, 0x3b000000\n"
        "v_fma_f32 v44, v44, v42, -v41\n"
        "v_max_f32 v44, 0, v44\n"
        "v_add_f32 v44, 0x3727c5ac, v44\n"
        "v_rsq_f32 v44, v44\n"
        "v_lshlrev_b32 v134, 16, v83\n"
        "v_and_b32 v135, s93, v83\n"
        "v_mul_f32 v40, v40, v44\n"
        "v_lshlrev_b32 v142, 16, v87\n"
        "v_and_b32 v143, s93, v87\n"
        "v_lshlrev_b32 v150, 16, v91\n"
        "v_and_b32 v151, s93, v91\n"
        "v_lshlrev_b32 v158, 16, v95\n"
        "v_and_b32 v159, s93, v95\n"
        "v_readlane_b32 s72, v44, 0\n"
        "v_readlane_b32 s73, v40, 0\n"
        "v_readlane_b32 s74, v44, 32\n"
        "v_readlane_b32 s75, v40, 32\n"
        "v_readlane_b32 s76, v44, 16\n"
        "v_readlane_b32 s77, v40, 16\n"
        "v_readlane_b32 s78, v44, 48\n"
        "v_readlane_b32 s79, v40, 48\n"
        "v_pk_fma_f32 v[128:129], v[128:129], s[72:73], s[72:73] op_sel:[0,0,1] op_sel_hi:[1,0,1]\n"
        "v_pk_fma_f32 v[130:131], v[130:131], s[72:73], s[72:73] op_sel:[0,0,1] op_sel_hi:[1,0,1]\n"
        "v_pk_fma_f32 v[132:133], v[132:133], s[72:73], s[72:73] op_sel:[0,0,1] op_sel_hi:[1,0,1]\n"
        "v_pk_fma_f32 v[134:135], v[134:135], s[72:73], s[72:73] op_sel:[0,0,1] op_sel_hi:[1,0,1]\n"
        "v_pk_fma_f32 v[136:137], v[136:137], s[74:75], s[74:75] op_sel:[0,0,1] op_sel_hi:[1,0,1]\n"
        "v_pk_fma_f32 v[138:139], v[138:139], s[74:75], s[74:75] op_sel:[0,0,1] op_sel_hi:[1,0,1]\n"
        "v_pk_fma_f32 v[140:141], v[140:141], s[74:75], s[74:75] op_sel:[0,0,1] op_sel_hi:[1,0,1]\n"
        "v_pk_fma_f32 v[142:143], v[142:143], s[74:75], s[74:75] op_sel:[0,0,1] op_sel_hi:[1,0,1]\n"
        "v_pk_fma_f32 v[144:145], v[144:145], s[76:77], s[76:77] op_sel:[0,0,1] op_sel_hi:[1,0,1]\n"
        "v_pk_fma_f32 v[146:147], v[146:147], s[76:77], s[76:77] op_sel:[0,0,1] op_sel_hi:[1,0,1]\n"
        "v_pk_fma_f32 v[148:149], v[148:149], s[76:77], s[76:77] op_sel:[0,0,1] op_sel_hi:[1,0,1]\n"
        "v_pk_fma_f32 v[150:151], v[150:151], s[76:77], s[76:77] op_sel:[0,0,1] op_sel_hi:[1,0,1]\n"
        "v_pk_fma_f32 v[152:153], v[152:153], s[78:79], s[78:79] op_sel:[0,0,1] op_sel_hi:[1,0,1]\n"
        "v_pk_fma_f32 v[154:155], v[154:155], s[78:79], s[78:79] op_sel:[0,0,1] op_sel_hi:[1,0,1]\n"
        "v_pk_fma_f32 v[156:157], v[156:157], s[78:79], s[78:79] op_sel:[0,0,1] op_sel_hi:[1,0,1]\n"
        "v_pk_fma_f32 v[158:159], v[158:159], s[78:79], s[78:79] op_sel:[0,0,1] op_sel_hi:[1,0,1]\n"
        "s_load_dwordx4 s[40:43], s[0:1], s62\n"
        "s_load_dwordx16 s[8:23], s[100:101], 0x60\n"
        "s_load_dwordx4 s[24:27], s[100:101], 0xa0\n"
        "s_load_dwordx16 s[72:87], s[100:101], 0xb0\n"
        "s_and_b32 s89, s44, 0x1c\n"
        "s_load_dword s89, s[100:101], s89 offset:0x160\n"
        "s_cmp_ge_u32 s3, 2\n"
        "s_cbranch_scc1 skip_17\n"
        "s_waitcnt vmcnt(2)\n"
        "ds_read_b64_tr_b16 v[192:193], v60 offset:0\n"
        "ds_read_b64_tr_b16 v[194:195], v60 offset:128\n"
        "ds_read_b64_tr_b16 v[196:197], v60 offset:1024\n"
        "ds_read_b64_tr_b16 v[198:199], v60 offset:1152\n"
        "ds_read_b64_tr_b16 v[200:201], v60 offset:2048\n"
        "ds_read_b64_tr_b16 v[202:203], v60 offset:2176\n"
        "ds_read_b64_tr_b16 v[204:205], v60 offset:3072\n"
        "ds_read_b64_tr_b16 v[206:207], v60 offset:3200\n"
        "ds_read_b64_tr_b16 v[208:209], v60 offset:4096\n"
        "ds_read_b64_tr_b16 v[210:211], v60 offset:4224\n"
        "ds_read_b64_tr_b16 v[212:213], v60 offset:5120\n"
        "ds_read_b64_tr_b16 v[214:215], v60 offset:5248\n"
        "ds_read_b64_tr_b16 v[216:217], v60 offset:6144\n"
        "ds_read_b64_tr_b16 v[218:219], v60 offset:6272\n"
        "ds_read_b64_tr_b16 v[220:221], v60 offset:7168\n"
        "ds_read_b64_tr_b16 v[222:223], v60 offset:7296\n"
        "ds_read_b64_tr_b16 v[224:225], v60 offset:8192\n"
        "ds_read_b64_tr_b16 v[226:227], v60 offset:8320\n"
        "ds_read_b64_tr_b16 v[228:229], v60 offset:9216\n"
        "ds_read_b64_tr_b16 v[230:231], v60 offset:9344\n"
        "ds_read_b64_tr_b16 v[232:233], v60 offset:10240\n"
        "ds_read_b64_tr_b16 v[234:235], v60 offset:10368\n"
        "ds_read_b64_tr_b16 v[236:237], v60 offset:11264\n"
        "ds_read_b64_tr_b16 v[238:239], v60 offset:11392\n"
        "ds_read_b64_tr_b16 v[240:241], v60 offset:12288\n"
        "ds_read_b64_tr_b16 v[242:243], v60 offset:12416\n"
        "ds_read_b64_tr_b16 v[244:245], v60 offset:13312\n"
        "ds_read_b64_tr_b16 v[246:247], v60 offset:13440\n"
        "ds_read_b64_tr_b16 v[248:249], v60 offset:14336\n"
        "ds_read_b64_tr_b16 v[250:251], v60 offset:14464\n"
        "ds_read_b64_tr_b16 v[252:253], v60 offset:15360\n"
        "ds_read_b64_tr_b16 v[254:255], v60 offset:15488\n"
        "skip_17:\n"
        "s_waitcnt lgkmcnt(0)\n"
        "s_mul_i32 s88, s44, s8\n"
        "s_lshl_b32 s88, s88, 4\n"
        "s_add_u32 s9, s9, s88\n"
        "s_lshl_b32 s88, s44, 3\n"
        "s_lshr_b32 s89, s89, s88\n"
        "s_and_b32 s89, s89, 0xff\n"
        "s_lshl_b32 s88, s89, 9\n"
        "s_cmp_eq_u32 s89, 0xff\n"
        "s_cselect_b32 s88, 67108864, s88\n"
        "s_cselect_b32 s42, 0, s42\n"
        "s_add_u32 s13, s13, s88\n"
        "s_add_u32 s14, s14, s88\n"
        "s_lshl_b32 s88, s44, 9\n"
        "s_add_u32 s10, s10, s88\n"
        "v_lshrrev_b32 v164, 7, v53\n"
        "page1:\n"
        "s_cbranch_execz warm_back\n"
        "v_and_b32 v165, 0x70, v53\n"
        "v_mad_u32_u24 v164, v164, s8, v165\n"
        "v_bfe_u32 v165, v53, 6, 2\n"
        "v_lshrrev_b32 v166, 9, v53\n"
        "v_lshl_or_b32 v165, v166, 2, v165\n"
        "v_bfe_u32 v167, v53, 8, 1\n"
        "v_lshl_or_b32 v165, v165, 2, v167\n"
        "v_bfe_u32 v168, v53, 4, 2\n"
        "v_lshl_or_b32 v165, v165, 2, v168\n"
        "v_lshl_add_u32 v165, v165, 3, s15\n"
        "v_and_b32 v168, 0xf0, v53\n"
        "v_lshlrev_b32 v169, 6, v168\n"
        "v_mad_u32_u24 v168, v167, s11, v168\n"
        "v_mad_u32_u24 v166, v166, s12, v168\n"
        "v_bfe_u32 v168, v53, 8, 2\n"
        "v_lshl_or_b32 v169, v168, 3, v169\n"
        "s_lshl_b32 s88, s44, 5\n"
        "v_add_u32 v170, s88, v188\n"
        "s_lshl_b32 s11, s8, 3\n"
        "s_add_u32 s11, s11, s9\n"
        "s_setprio 3\n"
        "chk_18:\n"
        "s_waitcnt vmcnt(1)\n"
        "v_xor_b32 v16, s92, v16\n"
        "v_xor_b32 v17, s92, v17\n"
        "v_xor_b32 v18, s92, v18\n"
        "v_xor_b32 v19, s92, v19\n"
        "s_waitcnt vmcnt(0)\n"
        "v_xor_b32 v20, s92, v20\n"
        "v_xor_b32 v21, s92, v21\n"
        "v_xor_b32 v22, s92, v22\n"
        "v_xor_b32 v23, s92, v23\n"
        "v_or3_b32 v24, v16, v17, v18\n"
        "v_or3_b32 v25, v19, v20, v21\n"
        "v_or3_b32 v24, v24, v22, v23\n"
        "v_or_b32 v24, v24, v25\n"
        "v_and_b32 v24, 0xff, v24\n"
        "v_cmp_ne_u32 vcc, 0, v24\n"
        "s_cbranch_vccnz rmod_20\n"
        "go_19:\n"
        "v_add_f32 v16, v16, v20\n"
        "v_add_f32 v17, v17, v21\n"
        "v_add_f32 v18, v18, v22\n"
        "v_add_f32 v19, v19, v23\n"
        "v_cvt_pk_bf16_f32 v16, v16, v17\n"
        "v_cvt_pk_bf16_f32 v17, v18, v19\n"
        "v_lshlrev_b32 v20, 16, v16\n"
        "v_and_b32 v21, 0xffff0000, v16\n"
        "v_lshlrev_b32 v22, 16, v17\n"
        "v_and_b32 v23, 0xffff0000, v17\n"
        "v_add_f32 v20, s68, v20\n"
        "v_add_f32 v21, s68, v21\n"
        "v_add_f32 v22, s68, v22\n"
        "v_add_f32 v23, s68, v23\n"
        "v_cvt_pk_bf16_f32 v16, v20, v21\n"
        "v_cvt_pk_bf16_f32 v17, v22, v23\n"
        "v_lshlrev_b32 v20, 16, v16\n"
        "v_and_b32 v21, 0xffff0000, v16\n"
        "v_lshlrev_b32 v22, 16, v17\n"
        "v_and_b32 v23, 0xffff0000, v17\n"
        "ds_write_b128 v13, v[20:23]\n"
        "s_setprio 0\n"
        "s_branch red_2\n"
        "young_1:\n"
        "s_cbranch_execz warm_back\n"
        "s_load_dwordx16 s[48:63], s[100:101], 0x0\n"
        "s_load_dwordx8 s[64:71], s[100:101], 0x40\n"
        "s_waitcnt vmcnt(7)\n"
        "v_dot2_f32_bf16 v40, v64, s94, 0\n"
        "v_dot2_f32_bf16 v44, v64, v64, 0\n"
        "s_waitcnt vmcnt(6)\n"
        "v_dot2_f32_bf16 v41, v68, s94, 0\n"
        "v_dot2_f32_bf16 v45, v68, v68, 0\n"
        "s_waitcnt vmcnt(5)\n"
        "v_dot2_f32_bf16 v42, v72, s94, 0\n"
        "v_dot2_f32_bf16 v46, v72, v72, 0\n"
        "s_waitcnt vmcnt(4)\n"
        "v_dot2_f32_bf16 v43, v76, s94, 0\n"
        "v_dot2_f32_bf16 v47, v76, v76, 0\n"
        "v_dot2c_f32_bf16 v40, s94, v65\n"
        "v_dot2c_f32_bf16 v44, v65, v65\n"
        "v_dot2c_f32_bf16 v41, s94, v69\n"
        "v_dot2c_f32_bf16 v45, v69, v69\n"
        "v_dot2c_f32_bf16 v42, s94, v73\n"
        "v_dot2c_f32_bf16 v46, v73, v73\n"
        "v_dot2c_f32_bf16 v43, s94, v77\n"
        "v_dot2c_f32_bf16 v47, v77, v77\n"
        "v_dot2c_f32_bf16 v40, s94, v66\n"
        "v_dot2c_f32_bf16 v44, v66, v66\n"
        "v_dot2c_f32_bf16 v41, s94, v70\n"
        "v_dot2c_f32_bf16 v45, v70, v70\n"
        "v_dot2c_f32_bf16 v42, s94, v74\n"
        "v_dot2c_f32_bf16 v46, v74, v74\n"
        "v_dot2c_f32_bf16 v43, s94, v78\n"
        "v_dot2c_f32_bf16 v47, v78, v78\n"
        "v_dot2c_f32_bf16 v40, s94, v67\n"
        "v_dot2c_f32_bf16 v44, v67, v67\n"
        "v_dot2c_f32_bf16 v41, s94, v71\n"
        "v_dot2c_f32_bf16 v45, v71, v71\n"
        "v_dot2c_f32_bf16 v42, s94, v75\n"
        "v_dot2c_f32_bf16 v46, v75, v75\n"
        "v_dot2c_f32_bf16 v43, s94, v79\n"
        "v_dot2c_f32_bf16 v47, v79, v79\n"
        "v_lshlrev_b32 v96, 16, v64\n"
        "v_and_b32 v97, s93, v64\n"
        "v_lshlrev_b32 v104, 16, v68\n"
        "v_and_b32 v105, s93, v68\n"
        "v_lshlrev_b32 v112, 16, v72\n"
        "v_and_b32 v113, s93, v72\n"
        "v_lshlrev_b32 v120, 16, v76\n"
        "v_and_b32 v121, s93, v76\n"
        "v_permlane32_swap_b32 v40, v41\n"
        "v_permlane32_swap_b32 v42, v43\n"
        "v_permlane32_swap_b32 v44, v45\n"
        "v_permlane32_swap_b32 v46, v47\n"
        "v_add_f32 v40, v40, v41\n"
        "v_add_f32 v42, v42, v43\n"
        "v_add_f32 v44, v44, v45\n"
        "v_add_f32 v46, v46, v47\n"
        "v_lshlrev_b32 v98, 16, v65\n"
        "v_and_b32 v99, s93, v65\n"
        "v_permlane16_swap_b32 v40, v42\n"
        "v_permlane16_swap_b32 v44, v46\n"
        "v_add_f32 v40, v40, v42\n"
        "v_add_f32 v44, v44, v46\n"
        "v_lshlrev_b32 v106, 16, v69\n"
        "v_and_b32 v107, s93, v69\n"
        "v_lshlrev_b32 v114, 16, v73\n"
        "v_and_b32 v115, s93, v73\n"
        "v_lshlrev_b32 v122, 16, v77\n"
        "v_and_b32 v123, s93, v77\n"
        "v_add_f32_dpp v40, v40, v40 row_ror:8 row_mask:0xf bank_mask:0xf bound_ctrl:1\n"
        "v_add_f32_dpp v44, v44, v44 row_ror:8 row_mask:0xf bank_mask:0xf bound_ctrl:1\n"
        "v_lshlrev_b32 v100, 16, v66\n"
        "v_and_b32 v101, s93, v66\n"
        "v_add_f32_dpp v40, v40, v40 row_ror:4 row_mask:0xf bank_mask:0xf bound_ctrl:1\n"
        "v_add_f32_dpp v44, v44, v44 row_ror:4 row_mask:0xf bank_mask:0xf bound_ctrl:1\n"
        "v_lshlrev_b32 v108, 16, v70\n"
        "v_and_b32 v109, s93, v70\n"
        "v_add_f32_dpp v40, v40, v40 row_ror:2 row_mask:0xf bank_mask:0xf bound_ctrl:1\n"
        "v_add_f32_dpp v44, v44, v44 row_ror:2 row_mask:0xf bank_mask:0xf bound_ctrl:1\n"
        "v_lshlrev_b32 v116, 16, v74\n"
        "v_and_b32 v117, s93, v74\n"
        "v_add_f32_dpp v40, v40, v40 row_ror:1 row_mask:0xf bank_mask:0xf bound_ctrl:1\n"
        "v_add_f32_dpp v44, v44, v44 row_ror:1 row_mask:0xf bank_mask:0xf bound_ctrl:1\n"
        "v_lshlrev_b32 v124, 16, v78\n"
        "v_and_b32 v125, s93, v78\n"
        "v_mul_f32 v40, 0xbb000000, v40\n"
        "v_mul_f32 v41, v40, v40\n"
        "v_mov_b32 v42, 0x3b000000\n"
        "v_fma_f32 v44, v44, v42, -v41\n"
        "v_max_f32 v44, 0, v44\n"
        "v_add_f32 v44, 0x3727c5ac, v44\n"
        "v_rsq_f32 v44, v44\n"
        "v_lshlrev_b32 v102, 16, v67\n"
        "v_and_b32 v103, s93, v67\n"
        "v_mul_f32 v40, v40, v44\n"
        "v_lshlrev_b32 v110, 16, v71\n"
        "v_and_b32 v111, s93, v71\n"
        "v_lshlrev_b32 v118, 16, v75\n"
        "v_and_b32 v119, s93, v75\n"
        "v_lshlrev_b32 v126, 16, v79\n"
        "v_and_b32 v127, s93, v79\n"
        "v_readlane_b32 s72, v44, 0\n"
        "v_readlane_b32 s73, v40, 0\n"
        "v_readlane_b32 s74, v44, 32\n"
        "v_readlane_b32 s75, v40, 32\n"
        "v_readlane_b32 s76, v44, 16\n"
        "v_readlane_b32 s77, v40, 16\n"
        "v_readlane_b32 s78, v44, 48\n"
        "v_readlane_b32 s79, v40, 48\n"
        "v_pk_fma_f32 v[96:97], v[96:97], s[72:73], s[72:73] op_sel:[0,0,1] op_sel_hi:[1,0,1]\n"
        "v_pk_fma_f32 v[98:99], v[98:99], s[72:73], s[72:73] op_sel:[0,0,1] op_sel_hi:[1,0,1]\n"
        "v_pk_fma_f32 v[100:101], v[100:101], s[72:73], s[72:73] op_sel:[0,0,1] op_sel_hi:[1,0,1]\n"
        "v_pk_fma_f32 v[102:103], v[102:103], s[72:73], s[72:73] op_sel:[0,0,1] op_sel_hi:[1,0,1]\n"
        "v_pk_fma_f32 v[104:105], v[104:105], s[74:75], s[74:75] op_sel:[0,0,1] op_sel_hi:[1,0,1]\n"
        "v_pk_fma_f32 v[106:107], v[106:107], s[74:75], s[74:75] op_sel:[0,0,1] op_sel_hi:[1,0,1]\n"
        "v_pk_fma_f32 v[108:109], v[108:109], s[74:75], s[74:75] op_sel:[0,0,1] op_sel_hi:[1,0,1]\n"
        "v_pk_fma_f32 v[110:111], v[110:111], s[74:75], s[74:75] op_sel:[0,0,1] op_sel_hi:[1,0,1]\n"
        "v_pk_fma_f32 v[112:113], v[112:113], s[76:77], s[76:77] op_sel:[0,0,1] op_sel_hi:[1,0,1]\n"
        "v_pk_fma_f32 v[114:115], v[114:115], s[76:77], s[76:77] op_sel:[0,0,1] op_sel_hi:[1,0,1]\n"
        "v_pk_fma_f32 v[116:117], v[116:117], s[76:77], s[76:77] op_sel:[0,0,1] op_sel_hi:[1,0,1]\n"
        "v_pk_fma_f32 v[118:119], v[118:119], s[76:77], s[76:77] op_sel:[0,0,1] op_sel_hi:[1,0,1]\n"
        "v_pk_fma_f32 v[120:121], v[120:121], s[78:79], s[78:79] op_sel:[0,0,1] op_sel_hi:[1,0,1]\n"
        "v_pk_fma_f32 v[122:123], v[122:123], s[78:79], s[78:79] op_sel:[0,0,1] op_sel_hi:[1,0,1]\n"
        "v_pk_fma_f32 v[124:125], v[124:125], s[78:79], s[78:79] op_sel:[0,0,1] op_sel_hi:[1,0,1]\n"
        "v_pk_fma_f32 v[126:127], v[126:127], s[78:79], s[78:79] op_sel:[0,0,1] op_sel_hi:[1,0,1]\n"
        "s_waitcnt lgkmcnt(0)\n"
        "s_and_b32 s43, s44, 1\n"
        "s_mul_i32 s43, s43, 192\n"
        "s_bitcmp1_b32 s44, 1\n"
        "s_cselect_b32 s84, 0x800, 0\n"
        "s_add_u32 s43, s43, s84\n"
        "s_load_dword s85, s[100:101], s43 offset:-0x780\n"
        "s_load_dword s85, s[100:101], s43 offset:-0x740\n"
        "s_load_dword s85, s[100:101], s43 offset:-0x700\n"
        "s_min_u32 s43, s42, code_end-code_here-4\n"
        "s_load_dword s85, s[40:41], s43\n"
        "s_add_u32 s42, s42, 8192\n"
        "s_min_u32 s43, s42, code_end-code_here-4\n"
        "s_load_dword s85, s[40:41], s43\n"
        "s_add_u32 s42, s42, 8192\n"
        "s_min_u32 s43, s42, code_end-code_here-4\n"
        "s_load_dword s85, s[40:41], s43\n"
        "s_add_u32 s42, s42, 8192\n"
        "s_min_u32 s43, s42, code_end-code_here-4\n"
        "s_load_dword s85, s[40:41], s43\n"
        "s_add_u32 s42, s42, 8192\n"
        "s_min_u32 s43, s42, code_end-code_here-4\n"
        "s_load_dword s85, s[40:41], s43\n"
        "s_add_u32 s42, s42, 8192\n"
        "s_waitcnt vmcnt(3)\n"
        "v_dot2_f32_bf16 v40, v80, s94, 0\n"
        "v_dot2_f32_bf16 v44, v80, v80, 0\n"
        "s_waitcnt vmcnt(2)\n"
        "v_dot2_f32_bf16 v41, v84, s94, 0\n"
        "v_dot2_f32_bf16 v45, v84, v84, 0\n"
        "s_waitcnt vmcnt(1)\n"
        "v_dot2_f32_bf16 v42, v88, s94, 0\n"
        "v_dot2_f32_bf16 v46, v88, v88, 0\n"
        "s_waitcnt vmcnt(0)\n"
        "v_dot2_f32_bf16 v43, v92, s94, 0\n"
        "v_dot2_f32_bf16 v47, v92, v92, 0\n"
        "v_dot2c_f32_bf16 v40, s94, v81\n"
        "v_dot2c_f32_bf16 v44, v81, v81\n"
        "v_dot2c_f32_bf16 v41, s94, v85\n"
        "v_dot2c_f32_bf16 v45, v85, v85\n"
        "s_mov_b32 m0, s57\n"
        "s_add_u32 s57, s57, 1024\n"
        "buffer_load_dwordx4 v7, s[8:11], s56 offen lds\n"
        "s_add_u32 s56, s56, s58\n"
        "v_dot2c_f32_bf16 v42, s94, v89\n"
        "v_dot2c_f32_bf16 v46, v89, v89\n"
        "v_dot2c_f32_bf16 v43, s94, v93\n"
        "v_dot2c_f32_bf16 v47, v93, v93\n"
        "v_dot2c_f32_bf16 v40, s94, v82\n"
        "v_dot2c_f32_bf16 v44, v82, v82\n"
        "v_dot2c_f32_bf16 v41, s94, v86\n"
        "v_dot2c_f32_bf16 v45, v86, v86\n"
        "v_dot2c_f32_bf16 v42, s94, v90\n"
        "v_dot2c_f32_bf16 v46, v90, v90\n"
        "v_dot2c_f32_bf16 v43, s94, v94\n"
        "v_dot2c_f32_bf16 v47, v94, v94\n"
        "s_mov_b32 m0, s57\n"
        "s_add_u32 s57, s57, 1024\n"
        "buffer_load_dwordx4 v7, s[8:11], s56 offen lds\n"
        "s_add_u32 s56, s56, s58\n"
        "v_dot2c_f32_bf16 v40, s94, v83\n"
        "v_dot2c_f32_bf16 v44, v83, v83\n"
        "v_dot2c_f32_bf16 v41, s94, v87\n"
        "v_dot2c_f32_bf16 v45, v87, v87\n"
        "v_dot2c_f32_bf16 v42, s94, v91\n"
        "v_dot2c_f32_bf16 v46, v91, v91\n"
        "v_dot2c_f32_bf16 v43, s94, v95\n"
        "v_dot2c_f32_bf16 v47, v95, v95\n"
        "v_lshlrev_b32 v128, 16, v80\n"
        "v_and_b32 v129, s93, v80\n"
        "v_lshlrev_b32 v136, 16, v84\n"
        "v_and_b32 v137, s93, v84\n"
        "s_mov_b32 m0, s57\n"
        "s_add_u32 s57, s57, 1024\n"
        "buffer_load_dwordx4 v7, s[8:11], s56 offen lds\n"
        "s_add_u32 s56, s56, s58\n"
        "v_lshlrev_b32 v144, 16, v88\n"
        "v_and_b32 v145, s93, v88\n"
        "v_lshlrev_b32 v152, 16, v92\n"
        "v_and_b32 v153, s93, v92\n"
        "v_permlane32_swap_b32 v40, v41\n"
        "v_permlane32_swap_b32 v42, v43\n"
        "v_permlane32_swap_b32 v44, v45\n"
        "v_permlane32_swap_b32 v46, v47\n"
        "v_add_f32 v40, v40, v41\n"
        "v_add_f32 v42, v42, v43\n"
        "v_add_f32 v44, v44, v45\n"
        "v_add_f32 v46, v46, v47\n"
        "s_mov_b32 m0, s57\n"
        "s_add_u32 s57, s57, 1024\n"
        "buffer_load_dwordx4 v7, s[8:11], s56 offen lds\n"
        "s_add_u32 s56, s56, s58\n"
        "v_lshlrev_b32 v130, 16, v81\n"
        "v_and_b32 v131, s93, v81\n"
        "v_permlane16_swap_b32 v40, v42\n"
        "v_permlane16_swap_b32 v44, v46\n"
        "v_add_f32 v40, v40, v42\n"
        "v_add_f32 v44, v44, v46\n"
        "v_lshlrev_b32 v138, 16, v85\n"
        "v_and_b32 v139, s93, v85\n"
        "v_lshlrev_b32 v146, 16, v89\n"
        "v_and_b32 v147, s93, v89\n"
        "v_lshlrev_b32 v154, 16, v93\n"
        "v_and_b32 v155, s93, v93\n"
        "s_mov_b32 m0, s57\n"
        "s_add_u32 s57, s57, 1024\n"
        "buffer_load_dwordx4 v7, s[8:11], s56 offen lds\n"
        "s_add_u32 s56, s56, s58\n"
        "v_add_f32_dpp v40, v40, v40 row_ror:8 row_mask:0xf bank_mask:0xf bound_ctrl:1\n"
        "v_add_f32_dpp v44, v44, v44 row_ror:8 row_mask:0xf bank_mask:0xf bound_ctrl:1\n"
        "v_lshlrev_b32 v132, 16, v82\n"
        "v_and_b32 v133, s93, v82\n"
        "v_add_f32_dpp v40, v40, v40 row_ror:4 row_mask:0xf bank_mask:0xf bound_ctrl:1\n"
        "v_add_f32_dpp v44, v44, v44 row_ror:4 row_mask:0xf bank_mask:0xf bound_ctrl:1\n"
        "v_lshlrev_b32 v140, 16, v86\n"
        "v_and_b32 v141, s93, v86\n"
        "v_add_f32_dpp v40, v40, v40 row_ror:2 row_mask:0xf bank_mask:0xf bound_ctrl:1\n"
        "v_add_f32_dpp v44, v44, v44 row_ror:2 row_mask:0xf bank_mask:0xf bound_ctrl:1\n"
        "v_lshlrev_b32 v148, 16, v90\n"
        "v_and_b32 v149, s93, v90\n"
        "s_mov_b32 m0, s57\n"
        "s_add_u32 s57, s57, 1024\n"
        "buffer_load_dwordx4 v7, s[8:11], s56 offen lds\n"
        "s_add_u32 s56, s56, s58\n"
        "v_add_f32_dpp v40, v40, v40 row_ror:1 row_mask:0xf bank_mask:0xf bound_ctrl:1\n"
        "v_add_f32_dpp v44, v44, v44 row_ror:1 row_mask:0xf bank_mask:0xf bound_ctrl:1\n"
        "v_lshlrev_b32 v156, 16, v94\n"
        "v_and_b32 v157, s93, v94\n"
        "v_mul_f32 v40, 0xbb000000, v40\n"
        "v_mul_f32 v41, v40, v40\n"
        "v_mov_b32 v42, 0x3b000000\n"
        "v_fma_f32 v44, v44, v42, -v41\n"
        "v_max_f32 v44, 0, v44\n"
        "v_add_f32 v44, 0x3727c5ac, v44\n"
        "v_rsq_f32 v44, v44\n"
        "v_lshlrev_b32 v134, 16, v83\n"
        "s_mov_b32 m0, s57\n"
        "s_add_u32 s57, s57, 1024\n"
        "buffer_load_dwordx4 v7, s[8:11], s56 offen lds\n"
        "s_add_u32 s56, s56, s58\n"
        "v_and_b32 v135, s93, v83\n"
        "v_mul_f32 v40, v40, v44\n"
        "v_lshlrev_b32 v142, 16, v87\n"
        "v_and_b32 v143, s93, v87\n"
        "v_lshlrev_b32 v150, 16, v91\n"
        "v_and_b32 v151, s93, v91\n"
        "v_lshlrev_b32 v158, 16, v95\n"
        "v_and_b32 v159, s93, v95\n"
        "v_readlane_b32 s72, v44, 0\n"
        "v_readlane_b32 s73, v40, 0\n"
        "v_readlane_b32 s74, v44, 32\n"
        "v_readlane_b32 s75, v40, 32\n"
        "s_mov_b32 m0, s57\n"
        "s_add_u32 s57, s57, 1024\n"
        "buffer_load_dwordx4 v7, s[8:11], s56 offen lds\n"
        "s_add_u32 s56, s56, s58\n"
        "v_readlane_b32 s76, v44, 16\n"
        "v_readlane_b32 s77, v40, 16\n"
        "v_readlane_b32 s78, v44, 48\n"
        "v_readlane_b32 s79, v40, 48\n"
        "v_pk_fma_f32 v[128:129], v[128:129], s[72:73], s[72:73] op_sel:[0,0,1] op_sel_hi:[1,0,1]\n"
        "v_pk_fma_f32 v[130:131], v[130:131], s[72:73], s[72:73] op_sel:[0,0,1] op_sel_hi:[1,0,1]\n"
        "v_pk_fma_f32 v[132:133], v[132:133], s[72:73], s[72:73] op_sel:[0,0,1] op_sel_hi:[1,0,1]\n"
        "v_pk_fma_f32 v[134:135], v[134:135], s[72:73], s[72:73] op_sel:[0,0,1] op_sel_hi:[1,0,1]\n"
        "v_pk_fma_f32 v[136:137], v[136:137], s[74:75], s[74:75] op_sel:[0,0,1] op_sel_hi:[1,0,1]\n"
        "v_pk_fma_f32 v[138:139], v[138:139], s[74:75], s[74:75] op_sel:[0,0,1] op_sel_hi:[1,0,1]\n"
        "v_pk_fma_f32 v[140:141], v[140:141], s[74:75], s[74:75] op_sel:[0,0,1] op_sel_hi:[1,0,1]\n"
        "v_pk_fma_f32 v[142:143], v[142:143], s[74:75], s[74:75] op_sel:[0,0,1] op_sel_hi:[1,0,1]\n"
        "v_pk_fma_f32 v[144:145], v[144:145], s[76:77], s[76:77] op_sel:[0,0,1] op_sel_hi:[1,0,1]\n"
        "v_pk_fma_f32 v[146:147], v[146:147], s[76:77], s[76:77] op_sel:[0,0,1] op_sel_hi:[1,0,1]\n"
        "v_pk_fma_f32 v[148:149], v[148:149], s[76:77], s[76:77] op_sel:[0,0,1] op_sel_hi:[1,0,1]\n"
        "v_pk_fma_f32 v[150:151], v[150:151], s[76:77], s[76:77] op_sel:[0,0,1] op_sel_hi:[1,0,1]\n"
        "v_pk_fma_f32 v[152:153], v[152:153], s[78:79], s[78:79] op_sel:[0,0,1] op_sel_hi:[1,0,1]\n"
        "v_pk_fma_f32 v[154:155], v[154:155], s[78:79], s[78:79] op_sel:[0,0,1] op_sel_hi:[1,0,1]\n"
        "v_pk_fma_f32 v[156:157], v[156:157], s[78:79], s[78:79] op_sel:[0,0,1] op_sel_hi:[1,0,1]\n"
        "v_pk_fma_f32 v[158:159], v[158:159], s[78:79], s[78:79] op_sel:[0,0,1] op_sel_hi:[1,0,1]\n"
        "s_waitcnt lgkmcnt(0)\n"
        "s_load_dwordx4 s[40:43], s[0:1], s62\n"
        "v_readfirstlane_b32 s45, v26\n"
        "v_mov_b32 v14, v27\n"
        "v_add_u32 v58, s60, v58\n"
        "v_add_u32 v59, s59, v59\n"
        "v_add_u32 v60, 74752, v60\n"
        "s_load_dwordx16 s[8:23], s[100:101], 0x60\n"
        "s_load_dwordx4 s[24:27], s[100:101], 0xa0\n"
        "s_load_dwordx16 s[72:87], s[100:101], 0xb0\n"
        "s_and_b32 s89, s44, 0x1c\n"
        "s_load_dword s89, s[100:101], s89 offset:0x160\n"
        "s_add_u32 s45, s45, 1\n"
        "s_waitcnt lgkmcnt(0)\n"
        "s_mul_i32 s88, s44, s8\n"
        "s_lshl_b32 s88, s88, 4\n"
        "s_add_u32 s9, s9, s88\n"
        "s_lshl_b32 s88, s44, 3\n"
        "s_lshr_b32 s89, s89, s88\n"
        "s_and_b32 s89, s89, 0xff\n"
        "s_lshl_b32 s88, s89, 9\n"
        "s_cmp_eq_u32 s89, 0xff\n"
        "s_cselect_b32 s88, 67108864, s88\n"
        "s_cselect_b32 s42, 0, s42\n"
        "s_add_u32 s13, s13, s88\n"
        "s_add_u32 s14, s14, s88\n"
        "s_lshl_b32 s88, s44, 9\n"
        "s_add_u32 s10, s10, s88\n"
        "v_lshrrev_b32 v164, 7, v53\n"
        "v_and_b32 v165, 0x70, v53\n"
        "v_mad_u32_u24 v164, v164, s8, v165\n"
        "v_bfe_u32 v165, v53, 6, 2\n"
        "v_lshrrev_b32 v166, 9, v53\n"
        "v_lshl_or_b32 v165, v166, 2, v165\n"
        "v_bfe_u32 v167, v53, 8, 1\n"
        "v_lshl_or_b32 v165, v165, 2, v167\n"
        "v_bfe_u32 v168, v53, 4, 2\n"
        "v_lshl_or_b32 v165, v165, 2, v168\n"
        "v_lshl_add_u32 v165, v165, 3, s15\n"
        "v_and_b32 v168, 0xf0, v53\n"
        "v_lshlrev_b32 v169, 6, v168\n"
        "v_mad_u32_u24 v168, v167, s11, v168\n"
        "v_mad_u32_u24 v166, v166, s12, v168\n"
        "v_bfe_u32 v168, v53, 8, 2\n"
        "v_lshl_or_b32 v169, v168, 3, v169\n"
        "s_lshl_b32 s88, s44, 5\n"
        "v_add_u32 v170, s88, v188\n"
        "s_lshl_b32 s11, s8, 3\n"
        "s_add_u32 s11, s11, s9\n"
        "s_mov_b64 exec, 0\n"
        "s_cmp_lg_u32 s3, 4\n"
        "s_cbranch_scc1 warm_22\n"
        "s_call_b64 s[88:89], page3\n"
        "s_branch warmed_21\n"
        "warm_22:\n"
        "s_cmp_lg_u32 s3, 5\n"
        "s_cbranch_scc1 warm_23\n"
        "s_call_b64 s[88:89], page4\n"
        "s_branch warmed_21\n"
        "warm_23:\n"
        "s_cmp_lg_u32 s3, 6\n"
        "s_cbranch_scc1 warm_24\n"
        "s_call_b64 s[88:89], page5\n"
        "s_branch warmed_21\n"
        "warm_24:\n"
        "s_call_b64 s[88:89], page6\n"
        "warmed_21:\n"
        "s_mov_b64 exec, -1\n"
        "red_2:\n"
        "s_waitcnt lgkmcnt(0)\n"
        "s_barrier\n"
        "ds_read_b128 v[16:19], v14 offset:0\n"
        "ds_read_b128 v[20:23], v14 offset:1024\n"
        "ds_read_b128 v[24:27], v14 offset:2048\n"
        "ds_read_b128 v[28:31], v14 offset:3072\n"
        "v_add_u32 v36, s48, v53\n"
        "s_waitcnt lgkmcnt(1)\n"
        "v_pk_fma_f32 v[96:97], v[96:97], v[24:25], v[16:17]\n"
        "v_pk_fma_f32 v[98:99], v[98:99], v[26:27], v[18:19]\n"
        "s_waitcnt lgkmcnt(0)\n"
        "v_pk_fma_f32 v[100:101], v[100:101], v[28:29], v[20:21]\n"
        "v_pk_fma_f32 v[102:103], v[102:103], v[30:31], v[22:23]\n"
        "v_cvt_pk_bf16_f32 v32, v96, v97\n"
        "v_cvt_pk_bf16_f32 v33, v98, v99\n"
        "v_cvt_pk_bf16_f32 v34, v100, v101\n"
        "v_cvt_pk_bf16_f32 v35, v102, v103\n"
        "ds_write_b128 v36, v[32:35]\n"
        "s_cmp_ge_u32 s3, 4\n"
        "s_cbranch_scc1 qgemm_5\n"
        "s_lshl_b32 s88, s44, 12\n"
        "s_add_u32 s88, s88, s61\n"
        "s_cmp_ge_u32 s3, 2\n"
        "s_waitcnt lgkmcnt(0)\n"
        "s_barrier\n"
        "s_cbranch_scc1 other_27\n"
        "s_setprio 3\n"
        "ds_read_b128 v[64:67], v58 offset:0\n"
        "ds_read_b128 v[68:71], v58 offset:64\n"
        "ds_read_b128 v[72:75], v58 offset:128\n"
        "ds_read_b128 v[76:79], v58 offset:192\n"
        "ds_read_b128 v[80:83], v58 offset:256\n"
        "ds_read_b128 v[84:87], v58 offset:320\n"
        "ds_read_b128 v[88:91], v58 offset:384\n"
        "ds_read_b128 v[92:95], v58 offset:448\n"
        "s_cmp_eq_u32 s3, 1\n"
        "s_cbranch_scc1 vwave_29\n"
        "v_add_u32 v42, s49, v53\n"
        "v_fma_f32 v104, v104, v24, v16\n"
        "v_fma_f32 v105, v105, v25, v17\n"
        "s_waitcnt lgkmcnt(7)\n"
        "v_mfma_f32_16x16x32_bf16 v[4:7], v[192:195], v[64:67], 0\n"
        "ds_read_b128 v[64:67], v58 offset:512\n"
        "v_fma_f32 v106, v106, v26, v18\n"
        "s_waitcnt lgkmcnt(7)\n"
        "v_mfma_f32_16x16x32_bf16 v[4:7], v[196:199], v[68:71], v[4:7]\n"
        "ds_read_b128 v[68:71], v58 offset:576\n"
        "v_fma_f32 v107, v107, v27, v19\n"
        "s_waitcnt lgkmcnt(7)\n"
        "v_mfma_f32_16x16x32_bf16 v[4:7], v[200:203], v[72:75], v[4:7]\n"
        "ds_read_b128 v[72:75], v58 offset:640\n"
        "v_fma_f32 v108, v108, v28, v20\n"
        "s_waitcnt lgkmcnt(7)\n"
        "v_mfma_f32_16x16x32_bf16 v[4:7], v[204:207], v[76:79], v[4:7]\n"
        "ds_read_b128 v[76:79], v58 offset:704\n"
        "v_fma_f32 v109, v109, v29, v21\n"
        "s_waitcnt lgkmcnt(7)\n"
        "v_mfma_f32_16x16x32_bf16 v[4:7], v[208:211], v[80:83], v[4:7]\n"
        "ds_read_b128 v[80:83], v58 offset:768\n"
        "v_fma_f32 v110, v110, v30, v22\n"
        "s_waitcnt lgkmcnt(7)\n"
        "v_mfma_f32_16x16x32_bf16 v[4:7], v[212:215], v[84:87], v[4:7]\n"
        "ds_read_b128 v[84:87], v58 offset:832\n"
        "v_fma_f32 v111, v111, v31, v23\n"
        "s_waitcnt lgkmcnt(7)\n"
        "v_mfma_f32_16x16x32_bf16 v[4:7], v[216:219], v[88:91], v[4:7]\n"
        "ds_read_b128 v[88:91], v58 offset:896\n"
        "v_cvt_pk_bf16_f32 v38, v104, v105\n"
        "s_waitcnt lgkmcnt(7)\n"
        "v_mfma_f32_16x16x32_bf16 v[4:7], v[220:223], v[92:95], v[4:7]\n"
        "ds_read_b128 v[92:95], v58 offset:960\n"
        "v_cvt_pk_bf16_f32 v39, v106, v107\n"
        "s_waitcnt lgkmcnt(7)\n"
        "v_mfma_f32_16x16x32_bf16 v[4:7], v[224:227], v[64:67], v[4:7]\n"
        "v_cvt_pk_bf16_f32 v40, v108, v109\n"
        "s_waitcnt lgkmcnt(6)\n"
        "v_mfma_f32_16x16x32_bf16 v[4:7], v[228:231], v[68:71], v[4:7]\n"
        "v_cvt_pk_bf16_f32 v41, v110, v111\n"
        "s_waitcnt lgkmcnt(5)\n"
        "v_mfma_f32_16x16x32_bf16 v[4:7], v[232:235], v[72:75], v[4:7]\n"
        "ds_write_b128 v42, v[38:41]\n"
        "v_add_u32 v36, s50, v53\n"
        "s_waitcnt lgkmcnt(5)\n"
        "v_mfma_f32_16x16x32_bf16 v[4:7], v[236:239], v[76:79], v[4:7]\n"
        "v_fma_f32 v112, v112, v24, v16\n"
        "s_waitcnt lgkmcnt(4)\n"
        "v_mfma_f32_16x16x32_bf16 v[4:7], v[240:243], v[80:83], v[4:7]\n"
        "v_fma_f32 v113, v113, v25, v17\n"
        "s_waitcnt lgkmcnt(3)\n"
        "v_mfma_f32_16x16x32_bf16 v[4:7], v[244:247], v[84:87], v[4:7]\n"
        "v_fma_f32 v114, v114, v26, v18\n"
        "s_waitcnt lgkmcnt(2)\n"
        "v_mfma_f32_16x16x32_bf16 v[4:7], v[248:251], v[88:91], v[4:7]\n"
        "v_fma_f32 v115, v115, v27, v19\n"
        "s_waitcnt lgkmcnt(1)\n"
        "v_mfma_f32_16x16x32_bf16 v[4:7], v[252:255], v[92:95], v[4:7]\n"
        "v_fma_f32 v116, v116, v28, v20\n"
        "v_fma_f32 v117, v117, v29, v21\n"
        "v_fma_f32 v118, v118, v30, v22\n"
        "v_fma_f32 v119, v119, v31, v23\n"
        "v_cvt_pk_bf16_f32 v32, v112, v113\n"
        "v_cvt_pk_bf16_f32 v33, v114, v115\n"
        "v_cvt_pk_bf16_f32 v34, v116, v117\n"
        "v_cvt_pk_bf16_f32 v35, v118, v119\n"
        "ds_write_b128 v36, v[32:35]\n"
        "s_branch kvst_30\n"
        "vwave_29:\n"
        "v_add_u32 v42, s49, v53\n"
        "v_fma_f32 v104, v104, v24, v16\n"
        "v_fma_f32 v105, v105, v25, v17\n"
        "s_waitcnt lgkmcnt(7)\n"
        "v_mfma_f32_16x16x32_bf16 v[4:7], v[64:67], v[192:195], 0\n"
        "ds_read_b128 v[64:67], v58 offset:512\n"
        "v_fma_f32 v106, v106, v26, v18\n"
        "s_waitcnt lgkmcnt(7)\n"
        "v_mfma_f32_16x16x32_bf16 v[4:7], v[68:71], v[196:199], v[4:7]\n"
        "ds_read_b128 v[68:71], v58 offset:576\n"
        "v_fma_f32 v107, v107, v27, v19\n"
        "s_waitcnt lgkmcnt(7)\n"
        "v_mfma_f32_16x16x32_bf16 v[4:7], v[72:75], v[200:203], v[4:7]\n"
        "ds_read_b128 v[72:75], v58 offset:640\n"
        "v_fma_f32 v108, v108, v28, v20\n"
        "s_waitcnt lgkmcnt(7)\n"
        "v_mfma_f32_16x16x32_bf16 v[4:7], v[76:79], v[204:207], v[4:7]\n"
        "ds_read_b128 v[76:79], v58 offset:704\n"
        "v_fma_f32 v109, v109, v29, v21\n"
        "s_waitcnt lgkmcnt(7)\n"
        "v_mfma_f32_16x16x32_bf16 v[4:7], v[80:83], v[208:211], v[4:7]\n"
        "ds_read_b128 v[80:83], v58 offset:768\n"
        "v_fma_f32 v110, v110, v30, v22\n"
        "s_waitcnt lgkmcnt(7)\n"
        "v_mfma_f32_16x16x32_bf16 v[4:7], v[84:87], v[212:215], v[4:7]\n"
        "ds_read_b128 v[84:87], v58 offset:832\n"
        "v_fma_f32 v111, v111, v31, v23\n"
        "s_waitcnt lgkmcnt(7)\n"
        "v_mfma_f32_16x16x32_bf16 v[4:7], v[88:91], v[216:219], v[4:7]\n"
        "ds_read_b128 v[88:91], v58 offset:896\n"
        "v_cvt_pk_bf16_f32 v38, v104, v105\n"
        "s_waitcnt lgkmcnt(7)\n"
        "v_mfma_f32_16x16x32_bf16 v[4:7], v[92:95], v[220:223], v[4:7]\n"
        "ds_read_b128 v[92:95], v58 offset:960\n"
        "v_cvt_pk_bf16_f32 v39, v106, v107\n"
        "s_waitcnt lgkmcnt(7)\n"
        "v_mfma_f32_16x16x32_bf16 v[4:7], v[64:67], v[224:227], v[4:7]\n"
        "v_cvt_pk_bf16_f32 v40, v108, v109\n"
        "s_waitcnt lgkmcnt(6)\n"
        "v_mfma_f32_16x16x32_bf16 v[4:7], v[68:71], v[228:231], v[4:7]\n"
        "v_cvt_pk_bf16_f32 v41, v110, v111\n"
        "s_waitcnt lgkmcnt(5)\n"
        "v_mfma_f32_16x16x32_bf16 v[4:7], v[72:75], v[232:235], v[4:7]\n"
        "ds_write_b128 v42, v[38:41]\n"
        "v_add_u32 v36, s50, v53\n"
        "s_waitcnt lgkmcnt(5)\n"
        "v_mfma_f32_16x16x32_bf16 v[4:7], v[76:79], v[236:239], v[4:7]\n"
        "v_fma_f32 v112, v112, v24, v16\n"
        "s_waitcnt lgkmcnt(4)\n"
        "v_mfma_f32_16x16x32_bf16 v[4:7], v[80:83], v[240:243], v[4:7]\n"
        "v_fma_f32 v113, v113, v25, v17\n"
        "s_waitcnt lgkmcnt(3)\n"
        "v_mfma_f32_16x16x32_bf16 v[4:7], v[84:87], v[244:247], v[4:7]\n"
        "v_fma_f32 v114, v114, v26, v18\n"
        "s_waitcnt lgkmcnt(2)\n"
        "v_mfma_f32_16x16x32_bf16 v[4:7], v[88:91], v[248:251], v[4:7]\n"
        "v_fma_f32 v115, v115, v27, v19\n"
        "s_waitcnt lgkmcnt(1)\n"
        "v_mfma_f32_16x16x32_bf16 v[4:7], v[92:95], v[252:255], v[4:7]\n"
        "v_fma_f32 v116, v116, v28, v20\n"
        "v_fma_f32 v117, v117, v29, v21\n"
        "v_fma_f32 v118, v118, v30, v22\n"
        "v_fma_f32 v119, v119, v31, v23\n"
        "v_cvt_pk_bf16_f32 v32, v112, v113\n"
        "v_cvt_pk_bf16_f32 v33, v114, v115\n"
        "v_cvt_pk_bf16_f32 v34, v116, v117\n"
        "v_cvt_pk_bf16_f32 v35, v118, v119\n"
        "ds_write_b128 v36, v[32:35]\n"
        "kvst_30:\n"
        "v_cvt_pk_bf16_f32 v4, v4, v5\n"
        "v_cvt_pk_bf16_f32 v5, v6, v7\n"
        "v_min_u32 v4, -2, v4\n"
        "v_min_u32 v5, -2, v5\n"
        "buffer_store_dwordx2 v[4:5], v59, s[32:35], s88 offen sc1\n"
        "s_branch rows_28\n"
        "other_27:\n"
        "v_add_u32 v42, s49, v53\n"
        "v_pk_fma_f32 v[104:105], v[104:105], v[24:25], v[16:17]\n"
        "v_pk_fma_f32 v[106:107], v[106:107], v[26:27], v[18:19]\n"
        "v_pk_fma_f32 v[108:109], v[108:109], v[28:29], v[20:21]\n"
        "v_pk_fma_f32 v[110:111], v[110:111], v[30:31], v[22:23]\n"
        "v_cvt_pk_bf16_f32 v38, v104, v105\n"
        "v_cvt_pk_bf16_f32 v39, v106, v107\n"
        "v_cvt_pk_bf16_f32 v40, v108, v109\n"
        "v_cvt_pk_bf16_f32 v41, v110, v111\n"
        "ds_write_b128 v42, v[38:41]\n"
        "v_add_u32 v36, s50, v53\n"
        "v_pk_fma_f32 v[112:113], v[112:113], v[24:25], v[16:17]\n"
        "v_pk_fma_f32 v[114:115], v[114:115], v[26:27], v[18:19]\n"
        "v_pk_fma_f32 v[116:117], v[116:117], v[28:29], v[20:21]\n"
        "v_pk_fma_f32 v[118:119], v[118:119], v[30:31], v[22:23]\n"
        "v_cvt_pk_bf16_f32 v32, v112, v113\n"
        "v_cvt_pk_bf16_f32 v33, v114, v115\n"
        "v_cvt_pk_bf16_f32 v34, v116, v117\n"
        "v_cvt_pk_bf16_f32 v35, v118, v119\n"
        "ds_write_b128 v36, v[32:35]\n"
        "rows_28:\n"
        "buffer_load_dwordx4 v[224:227], v53, s[32:35], s65 offen sc1\n"
        "buffer_load_dwordx4 v[228:231], v53, s[32:35], s66 offen sc1\n"
        "v_add_u32 v42, s51, v53\n"
        "v_pk_fma_f32 v[120:121], v[120:121], v[24:25], v[16:17]\n"
        "v_pk_fma_f32 v[122:123], v[122:123], v[26:27], v[18:19]\n"
        "v_pk_fma_f32 v[124:125], v[124:125], v[28:29], v[20:21]\n"
        "v_pk_fma_f32 v[126:127], v[126:127], v[30:31], v[22:23]\n"
        "v_cvt_pk_bf16_f32 v38, v120, v121\n"
        "v_cvt_pk_bf16_f32 v39, v122, v123\n"
        "v_cvt_pk_bf16_f32 v40, v124, v125\n"
        "v_cvt_pk_bf16_f32 v41, v126, v127\n"
        "ds_write_b128 v42, v[38:41]\n"
        "v_add_u32 v36, s52, v53\n"
        "v_pk_fma_f32 v[128:129], v[128:129], v[24:25], v[16:17]\n"
        "v_pk_fma_f32 v[130:131], v[130:131], v[26:27], v[18:19]\n"
        "v_pk_fma_f32 v[132:133], v[132:133], v[28:29], v[20:21]\n"
        "v_pk_fma_f32 v[134:135], v[134:135], v[30:31], v[22:23]\n"
        "v_cvt_pk_bf16_f32 v32, v128, v129\n"
        "v_cvt_pk_bf16_f32 v33, v130, v131\n"
        "v_cvt_pk_bf16_f32 v34, v132, v133\n"
        "v_cvt_pk_bf16_f32 v35, v134, v135\n"
        "ds_write_b128 v36, v[32:35]\n"
        "v_add_u32 v42, s53, v53\n"
        "v_pk_fma_f32 v[136:137], v[136:137], v[24:25], v[16:17]\n"
        "v_pk_fma_f32 v[138:139], v[138:139], v[26:27], v[18:19]\n"
        "v_pk_fma_f32 v[140:141], v[140:141], v[28:29], v[20:21]\n"
        "v_pk_fma_f32 v[142:143], v[142:143], v[30:31], v[22:23]\n"
        "v_cvt_pk_bf16_f32 v38, v136, v137\n"
        "v_cvt_pk_bf16_f32 v39, v138, v139\n"
        "v_cvt_pk_bf16_f32 v40, v140, v141\n"
        "v_cvt_pk_bf16_f32 v41, v142, v143\n"
        "ds_write_b128 v42, v[38:41]\n"
        "v_add_u32 v36, s54, v53\n"
        "v_pk_fma_f32 v[144:145], v[144:145], v[24:25], v[16:17]\n"
        "v_pk_fma_f32 v[146:147], v[146:147], v[26:27], v[18:19]\n"
        "v_pk_fma_f32 v[148:149], v[148:149], v[28:29], v[20:21]\n"
        "v_pk_fma_f32 v[150:151], v[150:151], v[30:31], v[22:23]\n"
        "v_cvt_pk_bf16_f32 v32, v144, v145\n"
        "v_cvt_pk_bf16_f32 v33, v146, v147\n"
        "v_cvt_pk_bf16_f32 v34, v148, v149\n"
        "v_cvt_pk_bf16_f32 v35, v150, v151\n"
        "ds_write_b128 v36, v[32:35]\n"
        "v_add_u32 v42, s55, v53\n"
        "page2:\n"
        "s_cbranch_execz warm_back\n"
        "v_pk_fma_f32 v[152:153], v[152:153], v[24:25], v[16:17]\n"
        "v_pk_fma_f32 v[154:155], v[154:155], v[26:27], v[18:19]\n"
        "v_pk_fma_f32 v[156:157], v[156:157], v[28:29], v[20:21]\n"
        "v_pk_fma_f32 v[158:159], v[158:159], v[30:31], v[22:23]\n"
        "v_cvt_pk_bf16_f32 v38, v152, v153\n"
        "v_cvt_pk_bf16_f32 v39, v154, v155\n"
        "v_cvt_pk_bf16_f32 v40, v156, v157\n"
        "v_cvt_pk_bf16_f32 v41, v158, v159\n"
        "ds_write_b128 v42, v[38:41]\n"
        "s_setprio 0\n"
        "s_cmp_lg_u32 s3, 2\n"
        "s_cbranch_scc1 skip_31\n"
        "v_lshrrev_b32 v8, 4, v2\n"
        "v_min_u32 v8, 2, v8\n"
        "v_cmp_eq_u32 vcc, 1, v8\n"
        "v_cmp_eq_u32 s[90:91], 2, v8\n"
        "v_lshl_or_b32 v5, v8, 8, v48\n"
        "v_add_u32 v5, 160000, v5\n"
        "v_mov_b32 v10, 0x1000\n"
        "v_mov_b32 v11, 0x2000\n"
        "v_cndmask_b32 v10, v10, v11, vcc\n"
        "v_mov_b32 v11, 0x2800\n"
        "v_cndmask_b32 v10, v10, v11, s[90:91]\n"
        "v_or_b32 v10, v10, v48\n"
        "s_add_u32 s89, s23, 12288\n"
        "buffer_load_dwordx4 v[236:239], v10, s[32:35], s23 offen sc1\n"
        "buffer_load_dwordx4 v[240:243], v10, s[32:35], s89 offen sc1\n"
        "v_cndmask_b32 v12, 0, 1.0, vcc\n"
        "v_mov_b32 v11, 0x3fb8aa3b\n"
        "v_cndmask_b32 v15, 1.0, v11, vcc\n"
        "v_mov_b32 v11, 0xbf317218\n"
        "v_cndmask_b32 v15, v15, v11, s[90:91]\n"
        "skip_31:\n"
        "s_waitcnt lgkmcnt(0)\n"
        "s_barrier\n"
        "v_mov_b64 v[96:97], v[50:51]\n"
        "v_mov_b64 v[98:99], v[50:51]\n"
        "v_mov_b64 v[100:101], v[50:51]\n"
        "v_mov_b64 v[102:103], v[50:51]\n"
        "v_mov_b64 v[104:105], v[50:51]\n"
        "v_mov_b64 v[106:107], v[50:51]\n"
        "v_mov_b64 v[108:109], v[50:51]\n"
        "v_mov_b64 v[110:111], v[50:51]\n"
        "v_mov_b64 v[112:113], v[50:51]\n"
        "v_mov_b64 v[114:115], v[50:51]\n"
        "v_mov_b64 v[116:117], v[50:51]\n"
        "v_mov_b64 v[118:119], v[50:51]\n"
        "v_mov_b64 v[120:121], v[50:51]\n"
        "v_mov_b64 v[122:123], v[50:51]\n"
        "v_mov_b64 v[124:125], v[50:51]\n"
        "v_mov_b64 v[126:127], v[50:51]\n"
        "v_mov_b64 v[128:129], v[50:51]\n"
        "v_mov_b64 v[130:131], v[50:51]\n"
        "v_mov_b64 v[132:133], v[50:51]\n"
        "v_mov_b64 v[134:135], v[50:51]\n"
        "v_mov_b64 v[136:137], v[50:51]\n"
        "v_mov_b64 v[138:139], v[50:51]\n"
        "v_mov_b64 v[140:141], v[50:51]\n"
        "v_mov_b64 v[142:143], v[50:51]\n"
        "v_mov_b64 v[144:145], v[50:51]\n"
        "v_mov_b64 v[146:147], v[50:51]\n"
        "v_mov_b64 v[148:149], v[50:51]\n"
        "v_mov_b64 v[150:151], v[50:51]\n"
        "v_mov_b64 v[152:153], v[50:51]\n"
        "v_mov_b64 v[154:155], v[50:51]\n"
        "v_mov_b64 v[156:157], v[50:51]\n"
        "v_mov_b64 v[158:159], v[50:51]\n"
        "v_mov_b64 v[32:33], 0\n"
        "v_mov_b64 v[34:35], 0\n"
        "v_mov_b64 v[36:37], 0\n"
        "v_mov_b64 v[38:39], 0\n"
        "v_mov_b64 v[40:41], 0\n"
        "v_mov_b64 v[42:43], 0\n"
        "v_mov_b64 v[44:45], 0\n"
        "v_mov_b64 v[46:47], 0\n"
        "v_mov_b64 v[180:181], -1\n"
        "v_mov_b64 v[182:183], -1\n"
        "v_mov_b64 v[208:209], 0\n"
        "v_mov_b64 v[210:211], 0\n"
        "v_mov_b64 v[212:213], 0\n"
        "v_mov_b64 v[214:215], 0\n"
        "v_mov_b64 v[216:217], 0\n"
        "v_mov_b64 v[218:219], 0\n"
        "v_mov_b64 v[220:221], 0\n"
        "v_mov_b64 v[222:223], 0\n"
        "chk_32:\n"
        "s_waitcnt vmcnt(1)\n"
        "v_xor_b32 v224, s92, v224\n"
        "v_xor_b32 v225, s92, v225\n"
        "v_xor_b32 v226, s92, v226\n"
        "v_xor_b32 v227, s92, v227\n"
        "s_waitcnt vmcnt(0)\n"
        "v_xor_b32 v228, s92, v228\n"
        "v_xor_b32 v229, s92, v229\n"
        "v_xor_b32 v230, s92, v230\n"
        "v_xor_b32 v231, s92, v231\n"
        "v_or3_b32 v232, v224, v225, v226\n"
        "v_or3_b32 v233, v227, v228, v229\n"
        "v_or3_b32 v232, v232, v230, v231\n"
        "v_or_b32 v232, v232, v233\n"
        "v_and_b32 v232, 0xff, v232\n"
        "v_cmp_ne_u32 vcc, 0, v232\n"
        "s_cbranch_vccnz rmodr_34\n"
        "go_33:\n"
        "v_add_f32 v224, v224, v228\n"
        "v_add_f32 v225, v225, v229\n"
        "v_add_f32 v226, v226, v230\n"
        "v_add_f32 v227, v227, v231\n"
        "v_cvt_pk_bf16_f32 v224, v224, v225\n"
        "v_cvt_pk_bf16_f32 v225, v226, v227\n"
        "v_lshlrev_b32 v228, 16, v224\n"
        "v_and_b32 v229, 0xffff0000, v224\n"
        "v_lshlrev_b32 v230, 16, v225\n"
        "v_and_b32 v231, 0xffff0000, v225\n"
        "v_add_f32 v228, s68, v228\n"
        "v_add_f32 v229, s68, v229\n"
        "v_add_f32 v230, s68, v230\n"
        "v_add_f32 v231, s68, v231\n"
        "v_cvt_pk_bf16_f32 v224, v228, v229\n"
        "v_cvt_pk_bf16_f32 v225, v230, v231\n"
        "v_lshrrev_b32 v8, 2, v2\n"
        "v_lshlrev_b32 v8, 5, v8\n"
        "v_and_b32 v9, 16, v53\n"
        "v_or_b32 v8, v8, v9\n"
        "v_bfe_u32 v9, v2, 1, 1\n"
        "v_lshl_or_b32 v8, v9, 3, v8\n"
        "v_add_u32 v8, s22, v8\n"
        "ds_write_b64 v8, v[224:225]\n"
        "buffer_load_dwordx4 v[64:67], v53, s[32:35], s24 offen offset:0 sc1\n"
        "buffer_load_dwordx4 v[68:71], v53, s[32:35], s24 offen offset:2048 sc1\n"
        "s_mov_b32 exec_lo, 0xffff\n"
        "s_mov_b32 exec_hi, 0xffff\n"
        "buffer_load_dwordx4 v[96:99], v61, s[32:35], s24 offen offset:1024 sc1\n"
        "buffer_load_dwordx4 v[100:103], v61, s[32:35], s24 offen offset:1536 sc1\n"
        "buffer_load_dwordx4 v[104:107], v61, s[32:35], s24 offen offset:3072 sc1\n"
        "buffer_load_dwordx4 v[108:111], v61, s[32:35], s24 offen offset:3584 sc1\n"
        "s_mov_b64 exec, -1\n"
        "buffer_load_dwordx4 v[72:75], v53, s[32:35], s25 offen offset:0 sc1\n"
        "buffer_load_dwordx4 v[76:79], v53, s[32:35], s25 offen offset:2048 sc1\n"
        "s_mov_b32 exec_lo, 0xffff\n"
        "s_mov_b32 exec_hi, 0xffff\n"
        "buffer_load_dwordx4 v[112:115], v61, s[32:35], s25 offen offset:1024 sc1\n"
        "buffer_load_dwordx4 v[116:119], v61, s[32:35], s25 offen offset:1536 sc1\n"
        "buffer_load_dwordx4 v[120:123], v61, s[32:35], s25 offen offset:3072 sc1\n"
        "buffer_load_dwordx4 v[124:127], v61, s[32:35], s25 offen offset:3584 sc1\n"
        "s_mov_b64 exec, -1\n"
        "s_cmp_lg_u32 s3, 2\n"
        "s_cbranch_scc1 skip_35\n"
        "chk_36:\n"
        "v_xor_b32 v236, s92, v236\n"
        "v_xor_b32 v237, s92, v237\n"
        "v_xor_b32 v238, s92, v238\n"
        "v_xor_b32 v239, s92, v239\n"
        "v_xor_b32 v240, s92, v240\n"
        "v_xor_b32 v241, s92, v241\n"
        "v_xor_b32 v242, s92, v242\n"
        "v_xor_b32 v243, s92, v243\n"
        "v_or3_b32 v244, v236, v237, v238\n"
        "v_or3_b32 v245, v239, v240, v241\n"
        "v_or3_b32 v244, v244, v242, v243\n"
        "v_or_b32 v244, v244, v245\n"
        "v_and_b32 v244, 0xff, v244\n"
        "v_cmp_ne_u32 vcc, 0, v244\n"
        "s_cbranch_vccnz rgate_38\n"
        "go_37:\n"
        "v_add_f32 v236, v236, v240\n"
        "v_add_f32 v237, v237, v241\n"
        "v_add_f32 v238, v238, v242\n"
        "v_add_f32 v239, v239, v243\n"
        "v_cvt_pk_bf16_f32 v236, v236, v237\n"
        "v_cvt_pk_bf16_f32 v237, v238, v239\n"
        "v_lshlrev_b32 v240, 16, v236\n"
        "v_and_b32 v241, 0xffff0000, v236\n"
        "v_lshlrev_b32 v242, 16, v237\n"
        "v_and_b32 v243, 0xffff0000, v237\n"
        "v_add_f32 v240, v12, v240\n"
        "v_add_f32 v241, v12, v241\n"
        "v_add_f32 v242, v12, v242\n"
        "v_add_f32 v243, v12, v243\n"
        "v_cvt_pk_bf16_f32 v236, v240, v241\n"
        "v_cvt_pk_bf16_f32 v237, v242, v243\n"
        "v_lshlrev_b32 v240, 16, v236\n"
        "v_and_b32 v241, 0xffff0000, v236\n"
        "v_lshlrev_b32 v242, 16, v237\n"
        "v_and_b32 v243, 0xffff0000, v237\n"
        "v_mul_f32 v240, v15, v240\n"
        "v_mul_f32 v241, v15, v241\n"
        "v_mul_f32 v242, v15, v242\n"
        "v_mul_f32 v243, v15, v243\n"
        "ds_write_b128 v5, v[240:243]\n"
        "skip_35:\n"
        "s_load_dwordx16 s[48:63], s[100:101], 0x100\n"
        "s_load_dwordx8 s[64:71], s[100:101], 0x140\n"
        "s_waitcnt lgkmcnt(0)\n"
        "s_barrier\n"
        "ds_read_b128 v[224:227], v53 offset:0\n"
        "ds_read_b128 v[48:51], v53 offset:1024\n"
        "s_branch attn_26\n"
        "qgemm_5:\n"
        "s_cbranch_execz warm_back\n"
        "ds_read_b64_tr_b16 v[64:65], v60 offset:0\n"
        "ds_read_b64_tr_b16 v[66:67], v60 offset:512\n"
        "ds_read_b64_tr_b16 v[68:69], v60 offset:2048\n"
        "ds_read_b64_tr_b16 v[70:71], v60 offset:2560\n"
        "ds_read_b64_tr_b16 v[72:73], v60 offset:4096\n"
        "ds_read_b64_tr_b16 v[74:75], v60 offset:4608\n"
        "ds_read_b64_tr_b16 v[76:77], v60 offset:6144\n"
        "ds_read_b64_tr_b16 v[78:79], v60 offset:6656\n"
        "ds_read_b64_tr_b16 v[80:81], v60 offset:8192\n"
        "ds_read_b64_tr_b16 v[82:83], v60 offset:8704\n"
        "ds_read_b64_tr_b16 v[84:85], v60 offset:10240\n"
        "ds_read_b64_tr_b16 v[86:87], v60 offset:10752\n"
        "s_waitcnt lgkmcnt(12)\n"
        "s_barrier\n"
        "v_add_u32 v42, s49, v53\n"
        "v_fma_f32 v104, v104, v24, v16\n"
        "v_fma_f32 v105, v105, v25, v17\n"
        "v_fma_f32 v106, v106, v26, v18\n"
        "v_fma_f32 v107, v107, v27, v19\n"
        "v_fma_f32 v108, v108, v28, v20\n"
        "v_fma_f32 v109, v109, v29, v21\n"
        "v_fma_f32 v110, v110, v30, v22\n"
        "v_fma_f32 v111, v111, v31, v23\n"
        "v_cvt_pk_bf16_f32 v38, v104, v105\n"
        "v_cvt_pk_bf16_f32 v39, v106, v107\n"
        "v_cvt_pk_bf16_f32 v40, v108, v109\n"
        "v_cvt_pk_bf16_f32 v41, v110, v111\n"
        "ds_write_b128 v42, v[38:41]\n"
        "v_add_u32 v36, s50, v53\n"
        "v_fma_f32 v112, v112, v24, v16\n"
        "v_fma_f32 v113, v113, v25, v17\n"
        "v_fma_f32 v114, v114, v26, v18\n"
        "v_fma_f32 v115, v115, v27, v19\n"
        "v_fma_f32 v116, v116, v28, v20\n"
        "v_fma_f32 v117, v117, v29, v21\n"
        "v_fma_f32 v118, v118, v30, v22\n"
        "v_fma_f32 v119, v119, v31, v23\n"
        "v_cvt_pk_bf16_f32 v32, v112, v113\n"
        "v_cvt_pk_bf16_f32 v33, v114, v115\n"
        "v_cvt_pk_bf16_f32 v34, v116, v117\n"
        "v_cvt_pk_bf16_f32 v35, v118, v119\n"
        "ds_write_b128 v36, v[32:35]\n"
        "v_add_u32 v42, s51, v53\n"
        "v_fma_f32 v120, v120, v24, v16\n"
        "v_fma_f32 v121, v121, v25, v17\n"
        "v_fma_f32 v122, v122, v26, v18\n"
        "v_fma_f32 v123, v123, v27, v19\n"
        "v_fma_f32 v124, v124, v28, v20\n"
        "v_fma_f32 v125, v125, v29, v21\n"
        "v_fma_f32 v126, v126, v30, v22\n"
        "v_fma_f32 v127, v127, v31, v23\n"
        "v_cvt_pk_bf16_f32 v38, v120, v121\n"
        "v_cvt_pk_bf16_f32 v39, v122, v123\n"
        "v_cvt_pk_bf16_f32 v40, v124, v125\n"
        "v_cvt_pk_bf16_f32 v41, v126, v127\n"
        "ds_write_b128 v42, v[38:41]\n"
        "v_add_u32 v36, s52, v53\n"
        "v_fma_f32 v128, v128, v24, v16\n"
        "v_fma_f32 v129, v129, v25, v17\n"
        "v_fma_f32 v130, v130, v26, v18\n"
        "v_fma_f32 v131, v131, v27, v19\n"
        "v_fma_f32 v132, v132, v28, v20\n"
        "v_fma_f32 v133, v133, v29, v21\n"
        "v_fma_f32 v134, v134, v30, v22\n"
        "v_fma_f32 v135, v135, v31, v23\n"
        "v_cvt_pk_bf16_f32 v32, v128, v129\n"
        "v_cvt_pk_bf16_f32 v33, v130, v131\n"
        "v_cvt_pk_bf16_f32 v34, v132, v133\n"
        "v_cvt_pk_bf16_f32 v35, v134, v135\n"
        "ds_write_b128 v36, v[32:35]\n"
        "v_add_u32 v42, s53, v53\n"
        "v_fma_f32 v136, v136, v24, v16\n"
        "v_fma_f32 v137, v137, v25, v17\n"
        "v_fma_f32 v138, v138, v26, v18\n"
        "v_fma_f32 v139, v139, v27, v19\n"
        "v_fma_f32 v140, v140, v28, v20\n"
        "v_fma_f32 v141, v141, v29, v21\n"
        "v_fma_f32 v142, v142, v30, v22\n"
        "v_fma_f32 v143, v143, v31, v23\n"
        "v_cvt_pk_bf16_f32 v38, v136, v137\n"
        "v_cvt_pk_bf16_f32 v39, v138, v139\n"
        "v_cvt_pk_bf16_f32 v40, v140, v141\n"
        "v_cvt_pk_bf16_f32 v41, v142, v143\n"
        "ds_write_b128 v42, v[38:41]\n"
        "v_add_u32 v36, s54, v53\n"
        "v_pk_fma_f32 v[144:145], v[144:145], v[24:25], v[16:17]\n"
        "v_pk_fma_f32 v[146:147], v[146:147], v[26:27], v[18:19]\n"
        "v_pk_fma_f32 v[148:149], v[148:149], v[28:29], v[20:21]\n"
        "v_pk_fma_f32 v[150:151], v[150:151], v[30:31], v[22:23]\n"
        "v_cvt_pk_bf16_f32 v32, v144, v145\n"
        "v_cvt_pk_bf16_f32 v33, v146, v147\n"
        "v_cvt_pk_bf16_f32 v34, v148, v149\n"
        "v_cvt_pk_bf16_f32 v35, v150, v151\n"
        "ds_write_b128 v36, v[32:35]\n"
        "v_add_u32 v42, s55, v53\n"
        "v_pk_fma_f32 v[152:153], v[152:153], v[24:25], v[16:17]\n"
        "v_pk_fma_f32 v[154:155], v[154:155], v[26:27], v[18:19]\n"
        "v_pk_fma_f32 v[156:157], v[156:157], v[28:29], v[20:21]\n"
        "v_pk_fma_f32 v[158:159], v[158:159], v[30:31], v[22:23]\n"
        "v_cvt_pk_bf16_f32 v38, v152, v153\n"
        "v_cvt_pk_bf16_f32 v39, v154, v155\n"
        "v_cvt_pk_bf16_f32 v40, v156, v157\n"
        "v_cvt_pk_bf16_f32 v41, v158, v159\n"
        "ds_write_b128 v42, v[38:41]\n"
        "ds_read_b64_tr_b16 v[88:89], v60 offset:12288\n"
        "ds_read_b64_tr_b16 v[90:91], v60 offset:12800\n"
        "ds_read_b64_tr_b16 v[92:93], v60 offset:14336\n"
        "ds_read_b64_tr_b16 v[94:95], v60 offset:14848\n"
        "ds_read_b64_tr_b16 v[176:177], v60 offset:16384\n"
        "ds_read_b64_tr_b16 v[178:179], v60 offset:16896\n"
        "ds_read_b64_tr_b16 v[180:181], v60 offset:18432\n"
        "ds_read_b64_tr_b16 v[182:183], v60 offset:18944\n"
        "ds_read_b64_tr_b16 v[184:185], v60 offset:20480\n"
        "ds_read_b64_tr_b16 v[186:187], v60 offset:20992\n"
        "ds_read_b64_tr_b16 v[188:189], v60 offset:22528\n"
        "ds_read_b64_tr_b16 v[190:191], v60 offset:23040\n"
        "s_waitcnt vmcnt(0) lgkmcnt(12)\n"
        "s_barrier\n"
        "s_setprio 3\n"
        "ds_read_b128 v[16:19], v58 offset:0\n"
        "ds_read_b128 v[20:23], v58 offset:32\n"
        "ds_read_b128 v[24:27], v58 offset:64\n"
        "ds_read_b128 v[28:31], v58 offset:96\n"
        "s_waitcnt lgkmcnt(3)\n"
        "v_mfma_f32_32x32x16_bf16 v[0:15], v[64:67], v[16:19], 0\n"
        "ds_read_b128 v[16:19], v58 offset:128\n"
        "ds_read_b64_tr_b16 v[192:193], v60 offset:24576\n"
        "ds_read_b64_tr_b16 v[194:195], v60 offset:25088\n"
        "v_mov_b64 v[96:97], v[50:51]\n"
        "v_mov_b64 v[32:33], 0\n"
        "s_waitcnt lgkmcnt(5)\n"
        "v_mfma_f32_32x32x16_bf16 v[0:15], v[68:71], v[20:23], v[0:15]\n"
        "ds_read_b128 v[20:23], v58 offset:160\n"
        "v_mov_b64 v[98:99], v[50:51]\n"
        "v_mov_b64 v[34:35], 0\n"
        "s_waitcnt lgkmcnt(5)\n"
        "v_mfma_f32_32x32x16_bf16 v[0:15], v[72:75], v[24:27], v[0:15]\n"
        "ds_read_b128 v[24:27], v58 offset:192\n"
        "ds_read_b64_tr_b16 v[196:197], v60 offset:26624\n"
        "ds_read_b64_tr_b16 v[198:199], v60 offset:27136\n"
        "v_mov_b64 v[100:101], v[50:51]\n"
        "v_mov_b64 v[36:37], 0\n"
        "s_waitcnt lgkmcnt(7)\n"
        "v_mfma_f32_32x32x16_bf16 v[0:15], v[76:79], v[28:31], v[0:15]\n"
        "ds_read_b128 v[28:31], v58 offset:224\n"
        "ds_read_b64_tr_b16 v[200:201], v60 offset:28672\n"
        "ds_read_b64_tr_b16 v[202:203], v60 offset:29184\n"
        "v_mov_b64 v[102:103], v[50:51]\n"
        "v_mov_b64 v[38:39], 0\n"
        "s_waitcnt lgkmcnt(9)\n"
        "v_mfma_f32_32x32x16_bf16 v[0:15], v[80:83], v[16:19], v[0:15]\n"
        "ds_read_b128 v[16:19], v58 offset:256\n"
        "v_mov_b64 v[104:105], v[50:51]\n"
        "v_mov_b64 v[40:41], 0\n"
        "s_waitcnt lgkmcnt(7)\n"
        "v_mfma_f32_32x32x16_bf16 v[0:15], v[84:87], v[20:23], v[0:15]\n"
        "ds_read_b128 v[20:23], v58 offset:288\n"
        "ds_read_b64_tr_b16 v[204:205], v60 offset:30720\n"
        "ds_read_b64_tr_b16 v[206:207], v60 offset:31232\n"
        "v_mov_b64 v[106:107], v[50:51]\n"
        "v_mov_b64 v[42:43], 0\n"
        "s_waitcnt lgkmcnt(9)\n"
        "v_mfma_f32_32x32x16_bf16 v[0:15], v[88:91], v[24:27], v[0:15]\n"
        "ds_read_b128 v[24:27], v58 offset:320\n"
        "ds_read_b64_tr_b16 v[208:209], v60 offset:32768\n"
        "ds_read_b64_tr_b16 v[210:211], v60 offset:33280\n"
        "v_mov_b64 v[108:109], v[50:51]\n"
        "v_mov_b64 v[44:45], 0\n"
        "s_waitcnt lgkmcnt(9)\n"
        "v_mfma_f32_32x32x16_bf16 v[0:15], v[92:95], v[28:31], v[0:15]\n"
        "ds_read_b128 v[28:31], v58 offset:352\n"
        "ds_read_b64_tr_b16 v[212:213], v60 offset:34816\n"
        "ds_read_b64_tr_b16 v[214:215], v60 offset:35328\n"
        "v_mov_b64 v[110:111], v[50:51]\n"
        "v_mov_b64 v[46:47], 0\n"
        "s_waitcnt lgkmcnt(9)\n"
        "v_mfma_f32_32x32x16_bf16 v[0:15], v[176:179], v[16:19], v[0:15]\n"
        "ds_read_b128 v[16:19], v58 offset:384\n"
        "v_mov_b64 v[112:113], v[50:51]\n"
        "s_waitcnt lgkmcnt(9)\n"
        "v_mfma_f32_32x32x16_bf16 v[0:15], v[180:183], v[20:23], v[0:15]\n"
        "ds_read_b128 v[20:23], v58 offset:416\n"
        "ds_read_b64_tr_b16 v[216:217], v60 offset:36864\n"
        "ds_read_b64_tr_b16 v[218:219], v60 offset:37376\n"
        "v_mov_b64 v[114:115], v[50:51]\n"
        "s_waitcnt lgkmcnt(9)\n"
        "v_mfma_f32_32x32x16_bf16 v[0:15], v[184:187], v[24:27], v[0:15]\n"
        "ds_read_b128 v[24:27], v58 offset:448\n"
        "ds_read_b64_tr_b16 v[220:221], v60 offset:38912\n"
        "ds_read_b64_tr_b16 v[222:223], v60 offset:39424\n"
        "v_mov_b64 v[116:117], v[50:51]\n"
        "s_waitcnt lgkmcnt(9)\n"
        "v_mfma_f32_32x32x16_bf16 v[0:15], v[188:191], v[28:31], v[0:15]\n"
        "ds_read_b128 v[28:31], v58 offset:480\n"
        "v_mov_b64 v[118:119], v[50:51]\n"
        "s_waitcnt lgkmcnt(7)\n"
        "v_mfma_f32_32x32x16_bf16 v[0:15], v[192:195], v[16:19], v[0:15]\n"
        "ds_read_b128 v[16:19], v58 offset:512\n"
        "ds_read_b64_tr_b16 v[224:225], v60 offset:40960\n"
        "ds_read_b64_tr_b16 v[226:227], v60 offset:41472\n"
        "v_mov_b64 v[120:121], v[50:51]\n"
        "s_waitcnt lgkmcnt(9)\n"
        "v_mfma_f32_32x32x16_bf16 v[0:15], v[196:199], v[20:23], v[0:15]\n"
        "ds_read_b128 v[20:23], v58 offset:544\n"
        "ds_read_b64_tr_b16 v[228:229], v60 offset:43008\n"
        "ds_read_b64_tr_b16 v[230:231], v60 offset:43520\n"
        "v_mov_b64 v[122:123], v[50:51]\n"
        "s_waitcnt lgkmcnt(9)\n"
        "v_mfma_f32_32x32x16_bf16 v[0:15], v[200:203], v[24:27], v[0:15]\n"
        "ds_read_b128 v[24:27], v58 offset:576\n"
        "ds_read_b64_tr_b16 v[232:233], v60 offset:45056\n"
        "ds_read_b64_tr_b16 v[234:235], v60 offset:45568\n"
        "v_mov_b64 v[124:125], v[50:51]\n"
        "s_waitcnt lgkmcnt(9)\n"
        "v_mfma_f32_32x32x16_bf16 v[0:15], v[204:207], v[28:31], v[0:15]\n"
        "ds_read_b128 v[28:31], v58 offset:608\n"
        "v_mov_b64 v[126:127], v[50:51]\n"
        "s_waitcnt lgkmcnt(9)\n"
        "v_mfma_f32_32x32x16_bf16 v[0:15], v[208:211], v[16:19], v[0:15]\n"
        "ds_read_b128 v[16:19], v58 offset:640\n"
        "ds_read_b64_tr_b16 v[236:237], v60 offset:47104\n"
        "ds_read_b64_tr_b16 v[238:239], v60 offset:47616\n"
        "v_mov_b64 v[128:129], v[50:51]\n"
        "s_waitcnt lgkmcnt(9)\n"
        "v_mfma_f32_32x32x16_bf16 v[0:15], v[212:215], v[20:23], v[0:15]\n"
        "ds_read_b128 v[20:23], v58 offset:672\n"
        "ds_read_b64_tr_b16 v[240:241], v60 offset:49152\n"
        "ds_read_b64_tr_b16 v[242:243], v60 offset:49664\n"
        "v_mov_b64 v[130:131], v[50:51]\n"
        "s_waitcnt lgkmcnt(9)\n"
        "v_mfma_f32_32x32x16_bf16 v[0:15], v[216:219], v[24:27], v[0:15]\n"
        "ds_read_b128 v[24:27], v58 offset:704\n"
        "v_mov_b64 v[132:133], v[50:51]\n"
        "s_waitcnt lgkmcnt(7)\n"
        "v_mfma_f32_32x32x16_bf16 v[0:15], v[220:223], v[28:31], v[0:15]\n"
        "ds_read_b128 v[28:31], v58 offset:736\n"
        "ds_read_b64_tr_b16 v[244:245], v60 offset:51200\n"
        "ds_read_b64_tr_b16 v[246:247], v60 offset:51712\n"
        "v_mov_b64 v[134:135], v[50:51]\n"
        "s_waitcnt lgkmcnt(9)\n"
        "v_mfma_f32_32x32x16_bf16 v[0:15], v[224:227], v[16:19], v[0:15]\n"
        "ds_read_b128 v[16:19], v58 offset:768\n"
        "ds_read_b64_tr_b16 v[248:249], v60 offset:53248\n"
        "ds_read_b64_tr_b16 v[250:251], v60 offset:53760\n"
        "v_mov_b64 v[136:137], v[50:51]\n"
        "s_waitcnt lgkmcnt(9)\n"
        "v_mfma_f32_32x32x16_bf16 v[0:15], v[228:231], v[20:23], v[0:15]\n"
        "ds_read_b128 v[20:23], v58 offset:800\n"
        "ds_read_b64_tr_b16 v[252:253], v60 offset:55296\n"
        "ds_read_b64_tr_b16 v[254:255], v60 offset:55808\n"
        "v_mov_b64 v[138:139], v[50:51]\n"
        "s_waitcnt lgkmcnt(9)\n"
        "v_mfma_f32_32x32x16_bf16 v[0:15], v[232:235], v[24:27], v[0:15]\n"
        "ds_read_b128 v[24:27], v58 offset:832\n"
        "v_mov_b64 v[140:141], v[50:51]\n"
        "s_waitcnt lgkmcnt(9)\n"
        "v_mfma_f32_32x32x16_bf16 v[0:15], v[236:239], v[28:31], v[0:15]\n"
        "ds_read_b128 v[28:31], v58 offset:864\n"
        "ds_read_b64_tr_b16 v[176:177], v60 offset:57344\n"
        "ds_read_b64_tr_b16 v[178:179], v60 offset:57856\n"
        "v_mov_b64 v[142:143], v[50:51]\n"
        "s_waitcnt lgkmcnt(9)\n"
        "v_mfma_f32_32x32x16_bf16 v[0:15], v[240:243], v[16:19], v[0:15]\n"
        "ds_read_b128 v[16:19], v58 offset:896\n"
        "ds_read_b64_tr_b16 v[180:181], v60 offset:59392\n"
        "ds_read_b64_tr_b16 v[182:183], v60 offset:59904\n"
        "v_mov_b64 v[144:145], v[50:51]\n"
        "s_waitcnt lgkmcnt(9)\n"
        "v_mfma_f32_32x32x16_bf16 v[0:15], v[244:247], v[20:23], v[0:15]\n"
        "ds_read_b128 v[20:23], v58 offset:928\n"
        "v_mov_b64 v[146:147], v[50:51]\n"
        "s_waitcnt lgkmcnt(7)\n"
        "v_mfma_f32_32x32x16_bf16 v[0:15], v[248:251], v[24:27], v[0:15]\n"
        "ds_read_b128 v[24:27], v58 offset:960\n"
        "ds_read_b64_tr_b16 v[184:185], v60 offset:61440\n"
        "ds_read_b64_tr_b16 v[186:187], v60 offset:61952\n"
        "v_mov_b64 v[148:149], v[50:51]\n"
        "s_waitcnt lgkmcnt(9)\n"
        "v_mfma_f32_32x32x16_bf16 v[0:15], v[252:255], v[28:31], v[0:15]\n"
        "ds_read_b128 v[28:31], v58 offset:992\n"
        "ds_read_b64_tr_b16 v[188:189], v60 offset:63488\n"
        "ds_read_b64_tr_b16 v[190:191], v60 offset:64000\n"
        "v_mov_b64 v[150:151], v[50:51]\n"
        "s_waitcnt lgkmcnt(9)\n"
        "v_mfma_f32_32x32x16_bf16 v[0:15], v[176:179], v[16:19], v[0:15]\n"
        "v_mov_b64 v[152:153], v[50:51]\n"
        "s_waitcnt lgkmcnt(6)\n"
        "v_mfma_f32_32x32x16_bf16 v[0:15], v[180:183], v[20:23], v[0:15]\n"
        "v_mov_b64 v[154:155], v[50:51]\n"
        "s_waitcnt lgkmcnt(3)\n"
        "v_mfma_f32_32x32x16_bf16 v[0:15], v[184:187], v[24:27], v[0:15]\n"
        "v_mov_b64 v[156:157], v[50:51]\n"
        "s_waitcnt lgkmcnt(0)\n"
        "v_mfma_f32_32x32x16_bf16 v[0:15], v[188:191], v[28:31], v[0:15]\n"
        "v_mov_b64 v[158:159], v[50:51]\n"
        "v_mov_b32 v16, 0x3eb8aa3b\n"
        "v_mov_b64 v[180:181], -1\n"
        "v_mov_b64 v[182:183], -1\n"
        "v_mov_b64 v[208:209], 0\n"
        "v_mov_b64 v[210:211], 0\n"
        "v_mov_b64 v[212:213], 0\n"
        "v_mov_b64 v[214:215], 0\n"
        "v_mov_b64 v[216:217], 0\n"
        "v_mov_b64 v[218:219], 0\n"
        "v_mov_b64 v[220:221], 0\n"
        "v_mov_b64 v[222:223], 0\n"
        "v_pk_mul_f32 v[0:1], v[0:1], v[16:17] op_sel_hi:[1,0]\n"
        "v_pk_mul_f32 v[2:3], v[2:3], v[16:17] op_sel_hi:[1,0]\n"
        "v_pk_mul_f32 v[4:5], v[4:5], v[16:17] op_sel_hi:[1,0]\n"
        "v_pk_mul_f32 v[6:7], v[6:7], v[16:17] op_sel_hi:[1,0]\n"
        "v_cvt_pk_bf16_f32 v20, v0, v1\n"
        "v_cvt_pk_bf16_f32 v21, v2, v3\n"
        "v_cvt_pk_bf16_f32 v22, v4, v5\n"
        "v_cvt_pk_bf16_f32 v23, v6, v7\n"
        "ds_write_b128 v59, v[20:23] offset:0\n"
        "v_pk_mul_f32 v[8:9], v[8:9], v[16:17] op_sel_hi:[1,0]\n"
        "v_pk_mul_f32 v[10:11], v[10:11], v[16:17] op_sel_hi:[1,0]\n"
        "v_pk_mul_f32 v[12:13], v[12:13], v[16:17] op_sel_hi:[1,0]\n"
        "v_pk_mul_f32 v[14:15], v[14:15], v[16:17] op_sel_hi:[1,0]\n"
        "v_cvt_pk_bf16_f32 v24, v8, v9\n"
        "v_cvt_pk_bf16_f32 v25, v10, v11\n"
        "v_cvt_pk_bf16_f32 v26, v12, v13\n"
        "v_cvt_pk_bf16_f32 v27, v14, v15\n"
        "ds_write_b128 v59, v[24:27] offset:1024\n"
        "s_setprio 0\n"
        "s_waitcnt lgkmcnt(0)\n"
        "s_barrier\n"
        "ds_read_b128 v[224:227], v53 offset:0\n"
        "ds_read_b128 v[48:51], v53 offset:1024\n"
        "s_load_dwordx16 s[48:63], s[100:101], 0x100\n"
        "s_load_dwordx8 s[64:71], s[100:101], 0x140\n"
        "buffer_load_dwordx4 v[64:67], v53, s[32:35], s24 offen offset:0 sc1\n"
        "buffer_load_dwordx4 v[68:71], v53, s[32:35], s24 offen offset:2048 sc1\n"
        "s_mov_b32 exec_lo, 0xffff\n"
        "s_mov_b32 exec_hi, 0xffff\n"
        "buffer_load_dwordx4 v[96:99], v61, s[32:35], s24 offen offset:1024 sc1\n"
        "buffer_load_dwordx4 v[100:103], v61, s[32:35], s24 offen offset:1536 sc1\n"
        "buffer_load_dwordx4 v[104:107], v61, s[32:35], s24 offen offset:3072 sc1\n"
        "buffer_load_dwordx4 v[108:111], v61, s[32:35], s24 offen offset:3584 sc1\n"
        "s_mov_b64 exec, -1\n"
        "buffer_load_dwordx4 v[72:75], v53, s[32:35], s25 offen offset:0 sc1\n"
        "buffer_load_dwordx4 v[76:79], v53, s[32:35], s25 offen offset:2048 sc1\n"
        "s_mov_b32 exec_lo, 0xffff\n"
        "s_mov_b32 exec_hi, 0xffff\n"
        "buffer_load_dwordx4 v[112:115], v61, s[32:35], s25 offen offset:1024 sc1\n"
        "buffer_load_dwordx4 v[116:119], v61, s[32:35], s25 offen offset:1536 sc1\n"
        "buffer_load_dwordx4 v[120:123], v61, s[32:35], s25 offen offset:3072 sc1\n"
        "buffer_load_dwordx4 v[124:127], v61, s[32:35], s25 offen offset:3584 sc1\n"
        "s_mov_b64 exec, -1\n"
        "s_mov_b64 exec, 0\n"
        "s_cmp_lg_u32 s3, 4\n"
        "s_cbranch_scc1 warm_40\n"
        "s_call_b64 s[88:89], tail_6\n"
        "s_branch warmed_39\n"
        "warm_40:\n"
        "s_cmp_lg_u32 s3, 5\n"
        "s_cbranch_scc1 warm_41\n"
        "s_call_b64 s[88:89], page7\n"
        "s_branch warmed_39\n"
        "warm_41:\n"
        "s_cmp_lg_u32 s3, 6\n"
        "s_cbranch_scc1 warm_42\n"
        "s_call_b64 s[88:89], page8\n"
        "s_branch warmed_39\n"
        "warm_42:\n"
        "warmed_39:\n"
        "s_mov_b64 exec, -1\n"
        "s_setprio 3\n"
        "attn_26:\n"
        "chk_44:\n"
        "s_waitcnt vmcnt(9)\n"
        "v_max_u32 v160, v64, v96\n"
        "s_waitcnt vmcnt(8)\n"
        "v_max3_u32 v160, v160, v100, v68\n"
        "s_waitcnt vmcnt(6)\n"
        "v_max3_u32 v160, v160, v104, v108\n"
        "v_cmp_eq_u32 vcc, -1, v160\n"
        "s_cbranch_vccnz rkv0_46\n"
        "go_45:\n"
        "s_waitcnt lgkmcnt(0)\n"
        "v_mfma_f32_32x32x16_bf16 v[0:15], v[64:67], v[224:227], 0\n"
        "s_setprio 3\n"
        "s_nop 10\n"
        "v_exp_f32 v0, v0\n"
        "v_exp_f32 v1, v1\n"
        "v_exp_f32 v2, v2\n"
        "v_exp_f32 v3, v3\n"
        "v_exp_f32 v4, v4\n"
        "v_mfma_f32_32x32x16_bf16 v[16:31], v[68:71], v[224:227], 0\n"
        "v_exp_f32 v5, v5\n"
        "v_exp_f32 v6, v6\n"
        "v_exp_f32 v7, v7\n"
        "v_exp_f32 v8, v8\n"
        "v_exp_f32 v9, v9\n"
        "v_smfmac_f32_32x32x32_bf16 v[208:223], v[96:99], v[0:7], v52\n"
        "v_exp_f32 v10, v10\n"
        "v_exp_f32 v11, v11\n"
        "v_exp_f32 v12, v12\n"
        "v_exp_f32 v13, v13\n"
        "v_exp_f32 v14, v14\n"
        "v_exp_f32 v15, v15\n"
        "v_exp_f32 v16, v16\n"
        "v_exp_f32 v17, v17\n"
        "v_smfmac_f32_32x32x32_bf16 v[208:223], v[100:103], v[8:15], v52\n"
        "v_exp_f32 v18, v18\n"
        "v_exp_f32 v19, v19\n"
        "v_exp_f32 v20, v20\n"
        "chk_47:\n"
        "s_waitcnt vmcnt(3)\n"
        "v_max_u32 v160, v72, v112\n"
        "s_waitcnt vmcnt(2)\n"
        "v_max3_u32 v160, v160, v116, v76\n"
        "s_waitcnt vmcnt(0)\n"
        "v_max3_u32 v160, v160, v120, v124\n"
        "v_cmp_eq_u32 vcc, -1, v160\n"
        "s_cbranch_vccnz rkv1_49\n"
        "go_48:\n"
        "v_mfma_f32_32x32x16_bf16 v[0:15], v[72:75], v[224:227], 0\n"
        "v_exp_f32 v21, v21\n"
        "v_exp_f32 v22, v22\n"
        "v_exp_f32 v23, v23\n"
        "v_exp_f32 v24, v24\n"
        "v_exp_f32 v25, v25\n"
        "v_smfmac_f32_32x32x32_bf16 v[208:223], v[104:107], v[16:23], v52\n"
        "v_exp_f32 v26, v26\n"
        "v_exp_f32 v27, v27\n"
        "v_exp_f32 v28, v28\n"
        "v_exp_f32 v29, v29\n"
        "v_exp_f32 v30, v30\n"
        "v_exp_f32 v31, v31\n"
        "v_exp_f32 v0, v0\n"
        "v_exp_f32 v1, v1\n"
        "v_smfmac_f32_32x32x32_bf16 v[208:223], v[108:111], v[24:31], v52\n"
        "v_exp_f32 v2, v2\n"
        "v_exp_f32 v3, v3\n"
        "v_exp_f32 v4, v4\n"
        "v_mfma_f32_32x32x16_bf16 v[16:31], v[76:79], v[224:227], 0\n"
        "v_exp_f32 v5, v5\n"
        "v_exp_f32 v6, v6\n"
        "v_exp_f32 v7, v7\n"
        "v_exp_f32 v8, v8\n"
        "v_exp_f32 v9, v9\n"
        "v_smfmac_f32_32x32x32_bf16 v[208:223], v[112:115], v[0:7], v52\n"
        "v_exp_f32 v10, v10\n"
        "v_exp_f32 v11, v11\n"
        "v_exp_f32 v12, v12\n"
        "v_exp_f32 v13, v13\n"
        "v_exp_f32 v14, v14\n"
        "v_exp_f32 v15, v15\n"
        "v_exp_f32 v16, v16\n"
        "v_exp_f32 v17, v17\n"
        "v_smfmac_f32_32x32x32_bf16 v[208:223], v[116:119], v[8:15], v52\n"
        "v_exp_f32 v18, v18\n"
        "v_exp_f32 v19, v19\n"
        "v_exp_f32 v20, v20\n"
        "s_waitcnt lgkmcnt(0)\n"
        "v_mfma_f32_32x32x16_bf16 v[0:15], v[64:67], v[48:51], 0\n"
        "v_exp_f32 v21, v21\n"
        "v_exp_f32 v22, v22\n"
        "buffer_load_dwordx4 v[80:83], v53, s[32:35], s26 offen offset:0 sc1\n"
        "buffer_load_dwordx4 v[84:87], v53, s[32:35], s26 offen offset:2048 sc1\n"
        "s_mov_b32 exec_lo, 0xffff\n"
        "s_mov_b32 exec_hi, 0xffff\n"
        "buffer_load_dwordx4 v[128:131], v61, s[32:35], s26 offen offset:1024 sc1\n"
        "buffer_load_dwordx4 v[132:135], v61, s[32:35], s26 offen offset:1536 sc1\n"
        "buffer_load_dwordx4 v[136:139], v61, s[32:35], s26 offen offset:3072 sc1\n"
        "buffer_load_dwordx4 v[140:143], v61, s[32:35], s26 offen offset:3584 sc1\n"
        "s_mov_b64 exec, -1\n"
        "v_exp_f32 v23, v23\n"
        "v_exp_f32 v24, v24\n"
        "v_exp_f32 v25, v25\n"
        "v_smfmac_f32_32x32x32_bf16 v[208:223], v[120:123], v[16:23], v52\n"
        "v_exp_f32 v26, v26\n"
        "v_exp_f32 v27, v27\n"
        "v_exp_f32 v28, v28\n"
        "v_exp_f32 v29, v29\n"
        "v_exp_f32 v30, v30\n"
        "v_exp_f32 v31, v31\n"
        "v_exp_f32 v0, v0\n"
        "v_exp_f32 v1, v1\n"
        "v_smfmac_f32_32x32x32_bf16 v[208:223], v[124:127], v[24:31], v52\n"
        "v_exp_f32 v2, v2\n"
        "v_exp_f32 v3, v3\n"
        "v_exp_f32 v4, v4\n"
        "v_mfma_f32_32x32x16_bf16 v[16:31], v[68:71], v[48:51], 0\n"
        "v_exp_f32 v5, v5\n"
        "v_exp_f32 v6, v6\n"
        "buffer_load_dwordx4 v[88:91], v53, s[32:35], s27 offen offset:0 sc1\n"
        "buffer_load_dwordx4 v[92:95], v53, s[32:35], s27 offen offset:2048 sc1\n"
        "s_mov_b32 exec_lo, 0xffff\n"
        "s_nop 0\n"
        "page3:\n"
        "s_cbranch_execz warm_back\n"
        "s_mov_b32 exec_hi, 0xffff\n"
        "buffer_load_dwordx4 v[144:147], v61, s[32:35], s27 offen offset:1024 sc1\n"
        "buffer_load_dwordx4 v[148:151], v61, s[32:35], s27 offen offset:1536 sc1\n"
        "buffer_load_dwordx4 v[152:155], v61, s[32:35], s27 offen offset:3072 sc1\n"
        "buffer_load_dwordx4 v[156:159], v61, s[32:35], s27 offen offset:3584 sc1\n"
        "s_mov_b64 exec, -1\n"
        "v_exp_f32 v7, v7\n"
        "v_exp_f32 v8, v8\n"
        "v_exp_f32 v9, v9\n"
        "v_smfmac_f32_32x32x32_bf16 v[32:47], v[96:99], v[0:7], v52\n"
        "v_exp_f32 v10, v10\n"
        "v_exp_f32 v11, v11\n"
        "v_exp_f32 v12, v12\n"
        "v_exp_f32 v13, v13\n"
        "v_exp_f32 v14, v14\n"
        "v_exp_f32 v15, v15\n"
        "v_exp_f32 v16, v16\n"
        "v_exp_f32 v17, v17\n"
        "v_smfmac_f32_32x32x32_bf16 v[32:47], v[100:103], v[8:15], v52\n"
        "v_exp_f32 v18, v18\n"
        "v_exp_f32 v19, v19\n"
        "v_exp_f32 v20, v20\n"
        "v_mfma_f32_32x32x16_bf16 v[0:15], v[72:75], v[48:51], 0\n"
        "v_exp_f32 v21, v21\n"
        "v_exp_f32 v22, v22\n"
        "v_exp_f32 v23, v23\n"
        "v_exp_f32 v24, v24\n"
        "v_exp_f32 v25, v25\n"
        "v_smfmac_f32_32x32x32_bf16 v[32:47], v[104:107], v[16:23], v52\n"
        "v_exp_f32 v26, v26\n"
        "v_exp_f32 v27, v27\n"
        "v_exp_f32 v28, v28\n"
        "v_exp_f32 v29, v29\n"
        "v_exp_f32 v30, v30\n"
        "v_exp_f32 v31, v31\n"
        "v_exp_f32 v0, v0\n"
        "v_exp_f32 v1, v1\n"
        "v_smfmac_f32_32x32x32_bf16 v[32:47], v[108:111], v[24:31], v52\n"
        "v_exp_f32 v2, v2\n"
        "v_exp_f32 v3, v3\n"
        "v_exp_f32 v4, v4\n"
        "v_mfma_f32_32x32x16_bf16 v[16:31], v[76:79], v[48:51], 0\n"
        "v_exp_f32 v5, v5\n"
        "v_exp_f32 v6, v6\n"
        "v_exp_f32 v7, v7\n"
        "v_exp_f32 v8, v8\n"
        "v_exp_f32 v9, v9\n"
        "v_smfmac_f32_32x32x32_bf16 v[32:47], v[112:115], v[0:7], v52\n"
        "v_exp_f32 v10, v10\n"
        "v_exp_f32 v11, v11\n"
        "v_exp_f32 v12, v12\n"
        "v_exp_f32 v13, v13\n"
        "v_exp_f32 v14, v14\n"
        "v_exp_f32 v15, v15\n"
        "s_setprio 2\n"
        "v_exp_f32 v16, v16\n"
        "v_exp_f32 v17, v17\n"
        "v_smfmac_f32_32x32x32_bf16 v[32:47], v[116:119], v[8:15], v52\n"
        "v_exp_f32 v18, v18\n"
        "v_exp_f32 v19, v19\n"
        "v_exp_f32 v20, v20\n"
        "chk_50:\n"
        "s_waitcnt vmcnt(9)\n"
        "v_max_u32 v160, v80, v128\n"
        "s_waitcnt vmcnt(8)\n"
        "v_max3_u32 v160, v160, v132, v84\n"
        "s_waitcnt vmcnt(6)\n"
        "v_max3_u32 v160, v160, v136, v140\n"
        "v_cmp_eq_u32 vcc, -1, v160\n"
        "s_cbranch_vccnz rkv2_52\n"
        "go_51:\n"
        "v_mfma_f32_32x32x16_bf16 v[0:15], v[80:83], v[224:227], 0\n"
        "v_exp_f32 v21, v21\n"
        "v_exp_f32 v22, v22\n"
        "s_mov_b32 m0, s15\n"
        "s_nop 0\n"
        "buffer_load_dwordx4 v164, s[40:43], s9 offen offset:0 lds\n"
        "v_exp_f32 v23, v23\n"
        "v_exp_f32 v24, v24\n"
        "v_exp_f32 v25, v25\n"
        "v_smfmac_f32_32x32x32_bf16 v[32:47], v[120:123], v[16:23], v52\n"
        "v_exp_f32 v26, v26\n"
        "v_exp_f32 v27, v27\n"
        "v_exp_f32 v28, v28\n"
        "s_add_u32 m0, s15, 1024\n"
        "s_nop 0\n"
        "buffer_load_dwordx4 v164, s[40:43], s11 offen offset:0 lds\n"
        "v_exp_f32 v29, v29\n"
        "v_exp_f32 v30, v30\n"
        "v_exp_f32 v31, v31\n"
        "v_exp_f32 v0, v0\n"
        "v_exp_f32 v1, v1\n"
        "v_smfmac_f32_32x32x32_bf16 v[32:47], v[124:127], v[24:31], v52\n"
        "v_exp_f32 v2, v2\n"
        "v_exp_f32 v3, v3\n"
        "v_exp_f32 v4, v4\n"
        "v_mfma_f32_32x32x16_bf16 v[16:31], v[84:87], v[224:227], 0\n"
        "v_exp_f32 v5, v5\n"
        "v_exp_f32 v6, v6\n"
        "buffer_store_dwordx4 v[180:183], v53, s[36:39], s16 offen sc1\n"
        "v_exp_f32 v7, v7\n"
        "v_exp_f32 v8, v8\n"
        "v_exp_f32 v9, v9\n"
        "v_smfmac_f32_32x32x32_bf16 v[208:223], v[128:131], v[0:7], v52\n"
        "v_exp_f32 v10, v10\n"
        "v_exp_f32 v11, v11\n"
        "v_exp_f32 v12, v12\n"
        "v_exp_f32 v13, v13\n"
        "v_exp_f32 v14, v14\n"
        "v_exp_f32 v15, v15\n"
        "v_exp_f32 v16, v16\n"
        "v_exp_f32 v17, v17\n"
        "v_smfmac_f32_32x32x32_bf16 v[208:223], v[132:135], v[8:15], v52\n"
        "v_exp_f32 v18, v18\n"
        "v_exp_f32 v19, v19\n"
        "v_exp_f32 v20, v20\n"
        "chk_53:\n"
        "s_waitcnt vmcnt(6)\n"
        "v_max_u32 v160, v88, v144\n"
        "s_waitcnt vmcnt(5)\n"
        "v_max3_u32 v160, v160, v148, v92\n"
        "s_waitcnt vmcnt(3)\n"
        "v_max3_u32 v160, v160, v152, v156\n"
        "v_cmp_eq_u32 vcc, -1, v160\n"
        "s_cbranch_vccnz rkv3_55\n"
        "go_54:\n"
        "v_mfma_f32_32x32x16_bf16 v[0:15], v[88:91], v[224:227], 0\n"
        "v_exp_f32 v21, v21\n"
        "v_exp_f32 v22, v22\n"
        "v_exp_f32 v23, v23\n"
        "v_exp_f32 v24, v24\n"
        "v_exp_f32 v25, v25\n"
        "v_smfmac_f32_32x32x32_bf16 v[208:223], v[136:139], v[16:23], v52\n"
        "v_exp_f32 v26, v26\n"
        "v_exp_f32 v27, v27\n"
        "v_exp_f32 v28, v28\n"
        "v_exp_f32 v29, v29\n"
        "v_exp_f32 v30, v30\n"
        "v_exp_f32 v31, v31\n"
        "v_exp_f32 v0, v0\n"
        "v_exp_f32 v1, v1\n"
        "v_smfmac_f32_32x32x32_bf16 v[208:223], v[140:143], v[24:31], v52\n"
        "v_exp_f32 v2, v2\n"
        "v_exp_f32 v3, v3\n"
        "v_exp_f32 v4, v4\n"
        "v_mfma_f32_32x32x16_bf16 v[16:31], v[92:95], v[224:227], 0\n"
        "v_exp_f32 v5, v5\n"
        "v_exp_f32 v6, v6\n"
        "v_exp_f32 v7, v7\n"
        "v_exp_f32 v8, v8\n"
        "v_exp_f32 v9, v9\n"
        "v_smfmac_f32_32x32x32_bf16 v[208:223], v[144:147], v[0:7], v52\n"
        "v_exp_f32 v10, v10\n"
        "v_exp_f32 v11, v11\n"
        "v_exp_f32 v12, v12\n"
        "v_exp_f32 v13, v13\n"
        "v_exp_f32 v14, v14\n"
        "v_exp_f32 v15, v15\n"
        "v_exp_f32 v16, v16\n"
        "v_exp_f32 v17, v17\n"
        "v_smfmac_f32_32x32x32_bf16 v[208:223], v[148:151], v[8:15], v52\n"
        "v_exp_f32 v18, v18\n"
        "v_exp_f32 v19, v19\n"
        "v_exp_f32 v20, v20\n"
        "v_mfma_f32_32x32x16_bf16 v[0:15], v[80:83], v[48:51], 0\n"
        "v_exp_f32 v21, v21\n"
        "v_exp_f32 v22, v22\n"
        "v_exp_f32 v23, v23\n"
        "v_exp_f32 v24, v24\n"
        "v_exp_f32 v25, v25\n"
        "v_smfmac_f32_32x32x32_bf16 v[208:223], v[152:155], v[16:23], v52\n"
        "v_exp_f32 v26, v26\n"
        "v_exp_f32 v27, v27\n"
        "v_exp_f32 v28, v28\n"
        "v_exp_f32 v29, v29\n"
        "v_exp_f32 v30, v30\n"
        "v_exp_f32 v31, v31\n"
        "v_exp_f32 v0, v0\n"
        "v_exp_f32 v1, v1\n"
        "v_smfmac_f32_32x32x32_bf16 v[208:223], v[156:159], v[24:31], v52\n"
        "v_exp_f32 v2, v2\n"
        "v_exp_f32 v3, v3\n"
        "v_exp_f32 v4, v4\n"
        "v_mfma_f32_32x32x16_bf16 v[16:31], v[84:87], v[48:51], 0\n"
        "v_exp_f32 v5, v5\n"
        "v_exp_f32 v6, v6\n"
        "v_exp_f32 v7, v7\n"
        "v_exp_f32 v8, v8\n"
        "v_exp_f32 v9, v9\n"
        "v_smfmac_f32_32x32x32_bf16 v[32:47], v[128:131], v[0:7], v52\n"
        "v_exp_f32 v10, v10\n"
        "v_exp_f32 v11, v11\n"
        "v_exp_f32 v12, v12\n"
        "v_exp_f32 v13, v13\n"
        "v_exp_f32 v14, v14\n"
        "v_exp_f32 v15, v15\n"
        "s_setprio 1\n"
        "v_exp_f32 v16, v16\n"
        "v_exp_f32 v17, v17\n"
        "v_smfmac_f32_32x32x32_bf16 v[32:47], v[132:135], v[8:15], v52\n"
        "v_exp_f32 v18, v18\n"
        "v_exp_f32 v19, v19\n"
        "v_exp_f32 v20, v20\n"
        "v_mfma_f32_32x32x16_bf16 v[0:15], v[88:91], v[48:51], 0\n"
        "v_exp_f32 v21, v21\n"
        "v_exp_f32 v22, v22\n"
        "ds_write_b128 v54, v[208:211] offset:0\n"
        "ds_write_b128 v55, v[212:215] offset:0\n"
        "ds_write_b32 v56, v216 offset:0\n"
        "v_exp_f32 v23, v23\n"
        "v_exp_f32 v24, v24\n"
        "v_exp_f32 v25, v25\n"
        "v_smfmac_f32_32x32x32_bf16 v[32:47], v[136:139], v[16:23], v52\n"
        "v_exp_f32 v26, v26\n"
        "v_exp_f32 v27, v27\n"
        "v_exp_f32 v28, v28\n"
        "v_exp_f32 v29, v29\n"
        "v_exp_f32 v30, v30\n"
        "v_exp_f32 v31, v31\n"
        "v_exp_f32 v0, v0\n"
        "v_exp_f32 v1, v1\n"
        "v_smfmac_f32_32x32x32_bf16 v[32:47], v[140:143], v[24:31], v52\n"
        "v_exp_f32 v2, v2\n"
        "v_exp_f32 v3, v3\n"
        "v_exp_f32 v4, v4\n"
        "v_mfma_f32_32x32x16_bf16 v[16:31], v[92:95], v[48:51], 0\n"
        "ds_read_b128 v[48:51], v53 offset:2048\n"
        "v_exp_f32 v5, v5\n"
        "v_exp_f32 v6, v6\n"
        "s_waitcnt vmcnt(1)\n"
        "ds_read_b64_tr_b16 v[172:173], v165\n"
        "ds_read_b64_tr_b16 v[174:175], v165 offset:1024\n"
        "ds_read_b64_tr_b16 v[176:177], v165 offset:64\n"
        "ds_read_b64_tr_b16 v[178:179], v165 offset:1088\n"
        "ds_read_b128 v[184:187], v170\n"
        "v_exp_f32 v7, v7\n"
        "v_exp_f32 v8, v8\n"
        "v_exp_f32 v9, v9\n"
        "v_smfmac_f32_32x32x32_bf16 v[32:47], v[144:147], v[0:7], v52\n"
        "v_exp_f32 v10, v10\n"
        "v_exp_f32 v11, v11\n"
        "v_exp_f32 v12, v12\n"
        "v_exp_f32 v13, v13\n"
        "v_exp_f32 v14, v14\n"
        "v_exp_f32 v15, v15\n"
        "v_exp_f32 v16, v16\n"
        "v_exp_f32 v17, v17\n"
        "v_smfmac_f32_32x32x32_bf16 v[32:47], v[148:151], v[8:15], v52\n"
        "v_exp_f32 v18, v18\n"
        "v_exp_f32 v19, v19\n"
        "v_exp_f32 v20, v20\n"
        "s_cmp_lt_u32 s3, 4\n"
        "s_cbranch_scc1 skip_56\n"
        "s_waitcnt lgkmcnt(0)\n"
        "v_mfma_f32_16x16x32_bf16 v[188:191], v[184:187], v[172:175], 0\n"
        "skip_56:\n"
        "s_waitcnt lgkmcnt(5)\n"
        "v_mfma_f32_32x32x16_bf16 v[0:15], v[64:67], v[48:51], 0\n"
        "v_exp_f32 v21, v21\n"
        "v_exp_f32 v22, v22\n"
        "s_waitcnt lgkmcnt(3)\n"
        "buffer_store_dwordx4 v[172:175], v166, s[32:35], s13 offen sc1\n"
        "v_exp_f32 v23, v23\n"
        "v_exp_f32 v24, v24\n"
        "v_exp_f32 v25, v25\n"
        "v_smfmac_f32_32x32x32_bf16 v[32:47], v[152:155], v[16:23], v52\n"
        "v_exp_f32 v26, v26\n"
        "v_exp_f32 v27, v27\n"
        "v_exp_f32 v28, v28\n"
        "s_cmp_lt_u32 s3, 4\n"
        "s_cbranch_scc1 skip_57\n"
        "v_mfma_f32_16x16x32_bf16 v[190:193], v[184:187], v[176:179], 0\n"
        "skip_57:\n"
        "s_waitcnt lgkmcnt(1)\n"
        "buffer_store_dwordx4 v[176:179], v166, s[32:35], s14 offen sc1\n"
        "v_exp_f32 v29, v29\n"
        "v_exp_f32 v30, v30\n"
        "v_exp_f32 v31, v31\n"
        "v_exp_f32 v0, v0\n"
        "v_exp_f32 v1, v1\n"
        "v_smfmac_f32_32x32x32_bf16 v[32:47], v[156:159], v[24:31], v52\n"
        "v_exp_f32 v2, v2\n"
        "v_exp_f32 v3, v3\n"
        "v_exp_f32 v4, v4\n"
        "v_mfma_f32_32x32x16_bf16 v[16:31], v[68:71], v[48:51], 0\n"
        "v_exp_f32 v5, v5\n"
        "v_exp_f32 v6, v6\n"
        "s_cmp_lt_u32 s3, 4\n"
        "s_cbranch_scc1 skip_58\n"
        "s_mov_b32 exec_hi, 0\n"
        "buffer_store_dwordx4 v[188:191], v53, s[32:35], s10 offen sc1\n"
        "s_mov_b32 exec_hi, -1\n"
        "skip_58:\n"
        "v_exp_f32 v7, v7\n"
        "v_exp_f32 v8, v8\n"
        "v_exp_f32 v9, v9\n"
        "v_exp_f32 v10, v10\n"
        "v_exp_f32 v11, v11\n"
        "v_exp_f32 v12, v12\n"
        "ds_write_b128 v54, v[32:35] offset:17408\n"
        "ds_write_b128 v55, v[36:39] offset:17408\n"
        "ds_write_b32 v56, v40 offset:17408\n"
        "v_mov_b64 v[32:33], 0\n"
        "v_mov_b64 v[34:35], 0\n"
        "v_mov_b64 v[36:37], 0\n"
        "v_mov_b64 v[38:39], 0\n"
        "v_mov_b32 v40, 0\n"
        "v_exp_f32 v13, v13\n"
        "v_exp_f32 v14, v14\n"
        "v_smfmac_f32_32x32x32_bf16 v[32:47], v[96:99], v[0:7], v52\n"
        "v_exp_f32 v15, v15\n"
        "s_setprio 0\n"
        "v_exp_f32 v16, v16\n"
        "v_exp_f32 v17, v17\n"
        "v_smfmac_f32_32x32x32_bf16 v[32:47], v[100:103], v[8:15], v52\n"
        "v_exp_f32 v18, v18\n"
        "v_exp_f32 v19, v19\n"
        "v_exp_f32 v20, v20\n"
        "v_mfma_f32_32x32x16_bf16 v[0:15], v[72:75], v[48:51], 0\n"
        "v_exp_f32 v21, v21\n"
        "v_exp_f32 v22, v22\n"
        "v_exp_f32 v23, v23\n"
        "v_exp_f32 v24, v24\n"
        "v_exp_f32 v25, v25\n"
        "v_smfmac_f32_32x32x32_bf16 v[32:47], v[104:107], v[16:23], v52\n"
        "v_exp_f32 v26, v26\n"
        "v_exp_f32 v27, v27\n"
        "v_exp_f32 v28, v28\n"
        "v_exp_f32 v29, v29\n"
        "v_exp_f32 v30, v30\n"
        "v_exp_f32 v31, v31\n"
        "v_exp_f32 v0, v0\n"
        "v_exp_f32 v1, v1\n"
        "v_smfmac_f32_32x32x32_bf16 v[32:47], v[108:111], v[24:31], v52\n"
        "s_waitcnt vmcnt(2)\n"
        "s_waitcnt lgkmcnt(0)\n"
        "s_barrier\n"
        "s_setprio 3\n"
        "s_cmp_ge_u32 s3, 4\n"
        "s_cbranch_scc1 skip_59\n"
        "ds_read_b128 v[16:19], v57 offset:0\n"
        "ds_read_b128 v[20:23], v57 offset:2048\n"
        "ds_read_b128 v[24:27], v57 offset:4096\n"
        "ds_read_b128 v[28:31], v57 offset:6144\n"
        "s_waitcnt lgkmcnt(2)\n"
        "v_add_f32 v16, v16, v20\n"
        "v_add_f32 v17, v17, v21\n"
        "v_add_f32 v18, v18, v22\n"
        "v_add_f32 v19, v19, v23\n"
        "ds_read_b128 v[20:23], v57 offset:8192\n"
        "s_waitcnt lgkmcnt(2)\n"
        "v_add_f32 v16, v16, v24\n"
        "v_add_f32 v17, v17, v25\n"
        "v_add_f32 v18, v18, v26\n"
        "v_add_f32 v19, v19, v27\n"
        "ds_read_b128 v[24:27], v57 offset:10240\n"
        "s_waitcnt lgkmcnt(2)\n"
        "v_add_f32 v16, v16, v28\n"
        "v_add_f32 v17, v17, v29\n"
        "v_add_f32 v18, v18, v30\n"
        "v_add_f32 v19, v19, v31\n"
        "ds_read_b128 v[28:31], v57 offset:12288\n"
        "s_waitcnt lgkmcnt(2)\n"
        "v_add_f32 v16, v16, v20\n"
        "v_add_f32 v17, v17, v21\n"
        "v_add_f32 v18, v18, v22\n"
        "v_add_f32 v19, v19, v23\n"
        "ds_read_b128 v[20:23], v57 offset:14336\n"
        "s_waitcnt lgkmcnt(2)\n"
        "v_add_f32 v16, v16, v24\n"
        "v_add_f32 v17, v17, v25\n"
        "v_add_f32 v18, v18, v26\n"
        "v_add_f32 v19, v19, v27\n"
        "ds_read_b128 v[24:27], v62 offset:0\n"
        "s_waitcnt lgkmcnt(2)\n"
        "v_add_f32 v16, v16, v28\n"
        "v_add_f32 v17, v17, v29\n"
        "v_add_f32 v18, v18, v30\n"
        "v_add_f32 v19, v19, v31\n"
        "ds_read_b128 v[28:31], v62 offset:16\n"
        "s_waitcnt lgkmcnt(2)\n"
        "v_add_f32 v16, v16, v20\n"
        "v_add_f32 v17, v17, v21\n"
        "v_add_f32 v18, v18, v22\n"
        "v_add_f32 v19, v19, v23\n"
        "s_waitcnt lgkmcnt(0)\n"
        "v_add_f32 v24, v24, v28\n"
        "v_add_f32 v25, v25, v29\n"
        "v_add_f32 v26, v26, v30\n"
        "v_add_f32 v27, v27, v31\n"
        "v_add_f32 v24, v24, v26\n"
        "v_add_f32 v25, v25, v27\n"
        "v_add_f32 v20, v16, v18\n"
        "v_add_f32 v24, v24, v25\n"
        "v_add_f32 v21, v17, v19\n"
        "v_rcp_f32 v28, v24\n"
        "v_add_f32 v20, v20, v21\n"
        "s_mov_b32 s88, 0x35800000\n"
        "v_cmp_lt_f32 vcc, 0x21800000, v24\n"
        "v_fma_f32 v20, |v20|, s88, v24\n"
        "s_mov_b32 s88, 0x71800000\n"
        "v_cmp_lt_f32 s[90:91], v20, s88\n"
        "v_mul_f32 v16, v16, v28\n"
        "v_mul_f32 v17, v17, v28\n"
        "s_and_b64 vcc, vcc, s[90:91]\n"
        "v_mul_f32 v18, v18, v28\n"
        "s_add_u32 s88, s72, 0\n"
        "v_mul_f32 v19, v19, v28\n"
        "s_cmp_eq_u64 vcc, -1\n"
        "v_lshrrev_b32 v20, 1, v53\n"
        "s_cselect_b32 s88, s88, 67108864\n"
        "v_cvt_pk_bf16_f32 v16, v16, v17\n"
        "s_cselect_b32 s89, 0, 1\n"
        "v_cvt_pk_bf16_f32 v17, v18, v19\n"
        "v_min_u32 v16, -2, v16\n"
        "v_min_u32 v17, -2, v17\n"
        "buffer_store_dwordx2 v[16:17], v20, s[36:39], s88 offen sc1\n"
        "s_or_b32 s47, s47, s89\n"
        "skip_59:\n"
        "v_exp_f32 v2, v2\n"
        "v_exp_f32 v3, v3\n"
        "v_exp_f32 v4, v4\n"
        "v_mfma_f32_32x32x16_bf16 v[16:31], v[76:79], v[48:51], 0\n"
        "v_exp_f32 v5, v5\n"
        "v_exp_f32 v6, v6\n"
        "v_exp_f32 v7, v7\n"
        "v_exp_f32 v8, v8\n"
        "v_exp_f32 v9, v9\n"
        "v_smfmac_f32_32x32x32_bf16 v[32:47], v[112:115], v[0:7], v52\n"
        "v_exp_f32 v10, v10\n"
        "v_exp_f32 v11, v11\n"
        "v_exp_f32 v12, v12\n"
        "v_exp_f32 v13, v13\n"
        "v_exp_f32 v14, v14\n"
        "v_exp_f32 v15, v15\n"
        "s_setprio 2\n"
        "v_exp_f32 v16, v16\n"
        "v_exp_f32 v17, v17\n"
        "v_smfmac_f32_32x32x32_bf16 v[32:47], v[116:119], v[8:15], v52\n"
        "v_exp_f32 v18, v18\n"
        "v_exp_f32 v19, v19\n"
        "v_exp_f32 v20, v20\n"
        "v_mfma_f32_32x32x16_bf16 v[0:15], v[80:83], v[48:51], 0\n"
        "v_exp_f32 v21, v21\n"
        "v_exp_f32 v22, v22\n"
        "buffer_load_dwordx2 v[58:59], v169, s[4:7], s84 offen\n"
        "v_exp_f32 v23, v23\n"
        "v_exp_f32 v24, v24\n"
        "v_exp_f32 v25, v25\n"
        "v_smfmac_f32_32x32x32_bf16 v[32:47], v[120:123], v[16:23], v52\n"
        "v_exp_f32 v26, v26\n"
        "v_exp_f32 v27, v27\n"
        "v_exp_f32 v28, v28\n"
        "buffer_load_dwordx2 v[60:61], v169, s[4:7], s85 offen\n"
        "v_exp_f32 v29, v29\n"
        "v_exp_f32 v30, v30\n"
        "v_exp_f32 v31, v31\n"
        "s_setprio 1\n"
        "v_exp_f32 v0, v0\n"
        "v_exp_f32 v1, v1\n"
        "v_smfmac_f32_32x32x32_bf16 v[32:47], v[124:127], v[24:31], v52\n"
        "v_exp_f32 v2, v2\n"
        "v_exp_f32 v3, v3\n"
        "v_exp_f32 v4, v4\n"
        "v_mfma_f32_32x32x16_bf16 v[16:31], v[84:87], v[48:51], 0\n"
        "v_exp_f32 v5, v5\n"
        "v_exp_f32 v6, v6\n"
        "v_exp_f32 v7, v7\n"
        "v_exp_f32 v8, v8\n"
        "v_exp_f32 v9, v9\n"
        "v_smfmac_f32_32x32x32_bf16 v[32:47], v[128:131], v[0:7], v52\n"
        "v_exp_f32 v10, v10\n"
        "v_exp_f32 v11, v11\n"
        "v_exp_f32 v12, v12\n"
        "v_exp_f32 v13, v13\n"
        "v_exp_f32 v14, v14\n"
        "v_exp_f32 v15, v15\n"
        "s_setprio 0\n"
        "v_exp_f32 v16, v16\n"
        "v_exp_f32 v17, v17\n"
        "v_smfmac_f32_32x32x32_bf16 v[32:47], v[132:135], v[8:15], v52\n"
        "v_exp_f32 v18, v18\n"
        "v_exp_f32 v19, v19\n"
        "v_exp_f32 v20, v20\n"
        "v_mfma_f32_32x32x16_bf16 v[0:15], v[88:91], v[48:51], 0\n"
        "v_exp_f32 v21, v21\n"
        "v_exp_f32 v22, v22\n"
        "v_exp_f32 v23, v23\n"
        "v_exp_f32 v24, v24\n"
        "v_exp_f32 v25, v25\n"
        "v_smfmac_f32_32x32x32_bf16 v[32:47], v[136:139], v[16:23], v52\n"
        "v_exp_f32 v26, v26\n"
        "v_exp_f32 v27, v27\n"
        "v_exp_f32 v28, v28\n"
        "v_exp_f32 v29, v29\n"
        "v_exp_f32 v30, v30\n"
        "v_exp_f32 v31, v31\n"
        "v_exp_f32 v0, v0\n"
        "v_exp_f32 v1, v1\n"
        "v_smfmac_f32_32x32x32_bf16 v[32:47], v[140:143], v[24:31], v52\n"
        "s_waitcnt vmcnt(2)\n"
        "s_barrier\n"
        "s_setprio 3\n"
        "s_cmp_lg_u32 s3, 7\n"
        "s_cbranch_scc1 skip_60\n"
        "s_lshl_b32 s88, s2, 2\n"
        "v_mov_b32 v184, s45\n"
        "s_mov_b64 exec, 1\n"
        "buffer_store_dword v184, off, s[32:35], s88 offset:1024 sc1\n"
        "s_mov_b64 exec, -1\n"
        "skip_60:\n"
        "v_exp_f32 v2, v2\n"
        "v_exp_f32 v3, v3\n"
        "v_exp_f32 v4, v4\n"
        "v_mfma_f32_32x32x16_bf16 v[16:31], v[92:95], v[48:51], 0\n"
        "ds_read_b128 v[48:51], v53 offset:3072\n"
        "v_exp_f32 v5, v5\n"
        "v_exp_f32 v6, v6\n"
        "v_exp_f32 v7, v7\n"
        "v_exp_f32 v8, v8\n"
        "v_exp_f32 v9, v9\n"
        "v_smfmac_f32_32x32x32_bf16 v[32:47], v[144:147], v[0:7], v52\n"
        "v_exp_f32 v10, v10\n"
        "v_exp_f32 v11, v11\n"
        "v_exp_f32 v12, v12\n"
        "v_exp_f32 v13, v13\n"
        "v_exp_f32 v14, v14\n"
        "v_exp_f32 v15, v15\n"
        "v_exp_f32 v16, v16\n"
        "v_exp_f32 v17, v17\n"
        "v_smfmac_f32_32x32x32_bf16 v[32:47], v[148:151], v[8:15], v52\n"
        "v_exp_f32 v18, v18\n"
        "v_exp_f32 v19, v19\n"
        "v_exp_f32 v20, v20\n"
        "s_waitcnt lgkmcnt(0)\n"
        "v_mfma_f32_32x32x16_bf16 v[0:15], v[64:67], v[48:51], 0\n"
        "v_exp_f32 v21, v21\n"
        "v_exp_f32 v22, v22\n"
        "buffer_store_dwordx4 v[180:183], v53, s[36:39], s17 offen sc1\n"
        "v_exp_f32 v23, v23\n"
        "v_exp_f32 v24, v24\n"
        "v_exp_f32 v25, v25\n"
        "v_smfmac_f32_32x32x32_bf16 v[32:47], v[152:155], v[16:23], v52\n"
        "v_exp_f32 v26, v26\n"
        "v_exp_f32 v27, v27\n"
        "v_exp_f32 v28, v28\n"
        "v_exp_f32 v29, v29\n"
        "v_exp_f32 v30, v30\n"
        "v_exp_f32 v31, v31\n"
        "v_exp_f32 v0, v0\n"
        "v_exp_f32 v1, v1\n"
        "v_smfmac_f32_32x32x32_bf16 v[32:47], v[156:159], v[24:31], v52\n"
        "v_exp_f32 v2, v2\n"
        "v_exp_f32 v3, v3\n"
        "v_exp_f32 v4, v4\n"
        "v_mfma_f32_32x32x16_bf16 v[16:31], v[68:71], v[48:51], 0\n"
        "v_exp_f32 v5, v5\n"
        "v_exp_f32 v6, v6\n"
        "v_exp_f32 v7, v7\n"
        "v_exp_f32 v8, v8\n"
        "v_exp_f32 v9, v9\n"
        "v_exp_f32 v10, v10\n"
        "v_exp_f32 v11, v11\n"
        "v_exp_f32 v12, v12\n"
        "ds_write_b128 v54, v[32:35] offset:0\n"
        "ds_write_b128 v55, v[36:39] offset:0\n"
        "ds_write_b32 v56, v40 offset:0\n"
        "v_mov_b64 v[32:33], 0\n"
        "v_mov_b64 v[34:35], 0\n"
        "v_mov_b64 v[36:37], 0\n"
        "v_mov_b64 v[38:39], 0\n"
        "v_mov_b32 v40, 0\n"
        "v_exp_f32 v13, v13\n"
        "v_exp_f32 v14, v14\n"
        "v_smfmac_f32_32x32x32_bf16 v[32:47], v[96:99], v[0:7], v52\n"
        "v_exp_f32 v15, v15\n"
        "v_exp_f32 v16, v16\n"
        "v_exp_f32 v17, v17\n"
        "v_smfmac_f32_32x32x32_bf16 v[32:47], v[100:103], v[8:15], v52\n"
        "v_exp_f32 v18, v18\n"
        "v_exp_f32 v19, v19\n"
        "v_exp_f32 v20, v20\n"
        "v_mfma_f32_32x32x16_bf16 v[0:15], v[72:75], v[48:51], 0\n"
        "v_exp_f32 v21, v21\n"
        "v_exp_f32 v22, v22\n"
        "buffer_store_dwordx4 v[180:183], v53, s[36:39], s18 offen sc1\n"
        "v_exp_f32 v23, v23\n"
        "v_exp_f32 v24, v24\n"
        "v_exp_f32 v25, v25\n"
        "v_smfmac_f32_32x32x32_bf16 v[32:47], v[104:107], v[16:23], v52\n"
        "v_exp_f32 v26, v26\n"
        "v_exp_f32 v27, v27\n"
        "v_exp_f32 v28, v28\n"
        "v_exp_f32 v29, v29\n"
        "v_exp_f32 v30, v30\n"
        "v_exp_f32 v31, v31\n"
        "s_setprio 2\n"
        "v_exp_f32 v0, v0\n"
        "v_exp_f32 v1, v1\n"
        "v_smfmac_f32_32x32x32_bf16 v[32:47], v[108:111], v[24:31], v52\n"
        "v_exp_f32 v2, v2\n"
        "v_exp_f32 v3, v3\n"
        "v_exp_f32 v4, v4\n"
        "v_mfma_f32_32x32x16_bf16 v[16:31], v[76:79], v[48:51], 0\n"
        "v_exp_f32 v5, v5\n"
        "v_exp_f32 v6, v6\n"
        "buffer_store_dwordx4 v[180:183], v53, s[36:39], s19 offen sc1\n"
        "v_exp_f32 v7, v7\n"
        "v_exp_f32 v8, v8\n"
        "v_exp_f32 v9, v9\n"
        "v_smfmac_f32_32x32x32_bf16 v[32:47], v[112:115], v[0:7], v52\n"
        "v_exp_f32 v10, v10\n"
        "v_exp_f32 v11, v11\n"
        "v_exp_f32 v12, v12\n"
        "v_exp_f32 v13, v13\n"
        "v_exp_f32 v14, v14\n"
        "v_exp_f32 v15, v15\n"
        "v_exp_f32 v16, v16\n"
        "v_exp_f32 v17, v17\n"
        "v_smfmac_f32_32x32x32_bf16 v[32:47], v[116:119], v[8:15], v52\n"
        "v_exp_f32 v18, v18\n"
        "v_exp_f32 v19, v19\n"
        "v_exp_f32 v20, v20\n"
        "v_mfma_f32_32x32x16_bf16 v[0:15], v[80:83], v[48:51], 0\n"
        "v_exp_f32 v21, v21\n"
        "v_exp_f32 v22, v22\n"
        "buffer_store_dwordx4 v[180:183], v53, s[36:39], s20 offen sc1\n"
        "v_exp_f32 v23, v23\n"
        "v_exp_f32 v24, v24\n"
        "v_exp_f32 v25, v25\n"
        "v_smfmac_f32_32x32x32_bf16 v[32:47], v[120:123], v[16:23], v52\n"
        "v_exp_f32 v26, v26\n"
        "v_exp_f32 v27, v27\n"
        "v_exp_f32 v28, v28\n"
        "v_exp_f32 v29, v29\n"
        "v_exp_f32 v30, v30\n"
        "v_exp_f32 v31, v31\n"
        "v_exp_f32 v0, v0\n"
        "v_exp_f32 v1, v1\n"
        "v_smfmac_f32_32x32x32_bf16 v[32:47], v[124:127], v[24:31], v52\n"
        "v_exp_f32 v2, v2\n"
        "v_exp_f32 v3, v3\n"
        "v_exp_f32 v4, v4\n"
        "v_mfma_f32_32x32x16_bf16 v[16:31], v[84:87], v[48:51], 0\n"
        "v_exp_f32 v5, v5\n"
        "v_exp_f32 v6, v6\n"
        "buffer_store_dwordx4 v[180:183], v53, s[36:39], s21 offen sc1\n"
        "v_exp_f32 v7, v7\n"
        "v_exp_f32 v8, v8\n"
        "v_exp_f32 v9, v9\n"
        "v_smfmac_f32_32x32x32_bf16 v[32:47], v[128:131], v[0:7], v52\n"
        "v_exp_f32 v10, v10\n"
        "v_exp_f32 v11, v11\n"
        "v_exp_f32 v12, v12\n"
        "s_load_dwordx16 s[8:23], s[100:101], 0x180\n"
        "s_load_dwordx4 s[24:27], s[100:101], 0x1c0\n"
        "v_exp_f32 v13, v13\n"
        "v_exp_f32 v14, v14\n"
        "v_exp_f32 v15, v15\n"
        "v_exp_f32 v16, v16\n"
        "v_exp_f32 v17, v17\n"
        "v_smfmac_f32_32x32x32_bf16 v[32:47], v[132:135], v[8:15], v52\n"
        "v_exp_f32 v18, v18\n"
        "v_exp_f32 v19, v19\n"
        "v_exp_f32 v20, v20\n"
        "v_mfma_f32_32x32x16_bf16 v[0:15], v[88:91], v[48:51], 0\n"
        "v_exp_f32 v21, v21\n"
        "v_exp_f32 v22, v22\n"
        "buffer_load_dwordx4 v[184:187], v53, s[32:35], 0 offen offset:1024 sc1\n"
        "v_exp_f32 v23, v23\n"
        "v_exp_f32 v24, v24\n"
        "v_exp_f32 v25, v25\n"
        "v_smfmac_f32_32x32x32_bf16 v[32:47], v[136:139], v[16:23], v52\n"
        "v_exp_f32 v26, v26\n"
        "v_exp_f32 v27, v27\n"
        "v_exp_f32 v28, v28\n"
        "v_exp_f32 v29, v29\n"
        "v_exp_f32 v30, v30\n"
        "v_exp_f32 v31, v31\n"
        "s_setprio 1\n"
        "v_exp_f32 v0, v0\n"
        "v_exp_f32 v1, v1\n"
        "v_smfmac_f32_32x32x32_bf16 v[32:47], v[140:143], v[24:31], v52\n"
        "v_exp_f32 v2, v2\n"
        "v_exp_f32 v3, v3\n"
        "v_exp_f32 v4, v4\n"
        "v_mfma_f32_32x32x16_bf16 v[16:31], v[92:95], v[48:51], 0\n"
        "ds_read_b128 v[48:51], v53 offset:4096\n"
        "v_exp_f32 v5, v5\n"
        "v_exp_f32 v6, v6\n"
        "v_exp_f32 v7, v7\n"
        "v_exp_f32 v8, v8\n"
        "v_exp_f32 v9, v9\n"
        "v_smfmac_f32_32x32x32_bf16 v[32:47], v[144:147], v[0:7], v52\n"
        "v_exp_f32 v10, v10\n"
        "v_exp_f32 v11, v11\n"
        "v_exp_f32 v12, v12\n"
        "v_exp_f32 v13, v13\n"
        "v_exp_f32 v14, v14\n"
        "v_exp_f32 v15, v15\n"
        "v_exp_f32 v16, v16\n"
        "v_exp_f32 v17, v17\n"
        "v_smfmac_f32_32x32x32_bf16 v[32:47], v[148:151], v[8:15], v52\n"
        "v_exp_f32 v18, v18\n"
        "v_exp_f32 v19, v19\n"
        "v_exp_f32 v20, v20\n"
        "chk_61:\n"
        "s_waitcnt vmcnt(0)\n"
        "v_min3_u32 v188, v184, v185, v186\n"
        "v_max3_u32 v184, v184, v185, v186\n"
        "v_min3_u32 v188, v188, v187, s45\n"
        "v_max3_u32 v184, v184, v187, s45\n"
        "v_cmp_ne_u32 vcc, v184, v188\n"
        "s_cbranch_vccnz rflag_63\n"
        "go_62:\n"
        "s_or_b32 s88, s2, s3\n"
        "s_cbranch_scc1 skip_64\n"
        "v_mov_b32 v184, s45\n"
        "s_mov_b64 exec, 1\n"
        "buffer_store_dword v184, off, s[32:35], 0 sc1\n"
        "s_mov_b64 exec, -1\n"
        "skip_64:\n"
        "s_xor_b32 s88, s3, 5\n"
        "s_lshl_b32 s88, s88, 8\n"
        "s_or_b32 s88, s88, s2\n"
        "s_cmp_ge_u32 s88, 176\n"
        "s_cbranch_scc1 skip_65\n"
        "s_lshr_b32 s88, s2, 3\n"
        "s_lshl_b32 s88, s88, 14\n"
        "s_and_b32 s89, s2, 7\n"
        "s_lshl_b32 s89, s89, 6\n"
        "s_add_u32 s88, s88, s89\n"
        "s_add_u32 s88, s88, 3571712\n"
        "s_add_u32 s89, s88, 8192\n"
        "v_and_b32 v249, 0x30, v53\n"
        "v_lshrrev_b32 v248, 6, v53\n"
        "v_lshl_or_b32 v248, v248, 9, v249\n"
        "buffer_load_dwordx4 v[240:243], v248, s[32:35], s88 offen sc1\n"
        "buffer_load_dwordx4 v[244:247], v248, s[32:35], s89 offen sc1\n"
        "skip_65:\n"
        "s_waitcnt lgkmcnt(0)\n"
        "v_mfma_f32_32x32x16_bf16 v[0:15], v[64:67], v[48:51], 0\n"
        "v_exp_f32 v21, v21\n"
        "v_exp_f32 v22, v22\n"
        "s_add_u32 m0, s81, 0\n"
        "s_nop 0\n"
        "buffer_load_dwordx4 v53, s[32:35], s79 offen offset:0 sc1 lds\n"
        "v_exp_f32 v23, v23\n"
        "v_exp_f32 v24, v24\n"
        "v_exp_f32 v25, v25\n"
        "v_smfmac_f32_32x32x32_bf16 v[32:47], v[152:155], v[16:23], v52\n"
        "v_exp_f32 v26, v26\n"
        "v_exp_f32 v27, v27\n"
        "v_exp_f32 v28, v28\n"
        "v_exp_f32 v29, v29\n"
        "v_exp_f32 v30, v30\n"
        "v_exp_f32 v31, v31\n"
        "v_exp_f32 v0, v0\n"
        "v_exp_f32 v1, v1\n"
        "v_smfmac_f32_32x32x32_bf16 v[32:47], v[156:159], v[24:31], v52\n"
        "v_exp_f32 v2, v2\n"
        "v_exp_f32 v3, v3\n"
        "v_exp_f32 v4, v4\n"
        "v_mfma_f32_32x32x16_bf16 v[16:31], v[68:71], v[48:51], 0\n"
        "v_exp_f32 v5, v5\n"
        "v_exp_f32 v6, v6\n"
        "buffer_load_dwordx4 v53, s[32:35], s79 offen offset:1024 sc1 lds\n"
        "v_exp_f32 v7, v7\n"
        "v_exp_f32 v8, v8\n"
        "v_exp_f32 v9, v9\n"
        "v_exp_f32 v10, v10\n"
        "v_exp_f32 v11, v11\n"
        "v_exp_f32 v12, v12\n"
        "ds_write_b128 v54, v[32:35] offset:17408\n"
        "ds_write_b128 v55, v[36:39] offset:17408\n"
        "ds_write_b32 v56, v40 offset:17408\n"
        "v_mov_b64 v[32:33], 0\n"
        "v_mov_b64 v[34:35], 0\n"
        "v_mov_b64 v[36:37], 0\n"
        "v_mov_b64 v[38:39], 0\n"
        "v_mov_b32 v40, 0\n"
        "v_exp_f32 v13, v13\n"
        "v_exp_f32 v14, v14\n"
        "v_smfmac_f32_32x32x32_bf16 v[32:47], v[96:99], v[0:7], v52\n"
        "v_exp_f32 v15, v15\n"
        "s_setprio 0\n"
        "v_exp_f32 v16, v16\n"
        "v_exp_f32 v17, v17\n"
        "v_smfmac_f32_32x32x32_bf16 v[32:47], v[100:103], v[8:15], v52\n"
        "v_exp_f32 v18, v18\n"
        "v_exp_f32 v19, v19\n"
        "v_exp_f32 v20, v20\n"
        "v_mfma_f32_32x32x16_bf16 v[0:15], v[72:75], v[48:51], 0\n"
        "v_exp_f32 v21, v21\n"
        "v_exp_f32 v22, v22\n"
        "v_exp_f32 v23, v23\n"
        "v_exp_f32 v24, v24\n"
        "v_exp_f32 v25, v25\n"
        "v_smfmac_f32_32x32x32_bf16 v[32:47], v[104:107], v[16:23], v52\n"
        "v_exp_f32 v26, v26\n"
        "v_exp_f32 v27, v27\n"
        "v_exp_f32 v28, v28\n"
        "v_exp_f32 v29, v29\n"
        "v_exp_f32 v30, v30\n"
        "v_exp_f32 v31, v31\n"
        "v_exp_f32 v0, v0\n"
        "v_exp_f32 v1, v1\n"
        "v_smfmac_f32_32x32x32_bf16 v[32:47], v[108:111], v[24:31], v52\n"
        "s_waitcnt lgkmcnt(0)\n"
        "s_barrier\n"
        "s_setprio 3\n"
        "s_cmp_ge_u32 s3, 4\n"
        "s_cbranch_scc1 skip_66\n"
        "ds_read_b128 v[16:19], v57 offset:0\n"
        "ds_read_b128 v[20:23], v57 offset:2048\n"
        "ds_read_b128 v[24:27], v57 offset:4096\n"
        "ds_read_b128 v[28:31], v57 offset:6144\n"
        "s_waitcnt lgkmcnt(2)\n"
        "v_add_f32 v16, v16, v20\n"
        "v_add_f32 v17, v17, v21\n"
        "v_add_f32 v18, v18, v22\n"
        "v_add_f32 v19, v19, v23\n"
        "ds_read_b128 v[20:23], v57 offset:8192\n"
        "s_waitcnt lgkmcnt(2)\n"
        "v_add_f32 v16, v16, v24\n"
        "v_add_f32 v17, v17, v25\n"
        "v_add_f32 v18, v18, v26\n"
        "v_add_f32 v19, v19, v27\n"
        "ds_read_b128 v[24:27], v57 offset:10240\n"
        "s_waitcnt lgkmcnt(2)\n"
        "v_add_f32 v16, v16, v28\n"
        "v_add_f32 v17, v17, v29\n"
        "v_add_f32 v18, v18, v30\n"
        "v_add_f32 v19, v19, v31\n"
        "ds_read_b128 v[28:31], v57 offset:12288\n"
        "s_waitcnt lgkmcnt(2)\n"
        "v_add_f32 v16, v16, v20\n"
        "v_add_f32 v17, v17, v21\n"
        "v_add_f32 v18, v18, v22\n"
        "v_add_f32 v19, v19, v23\n"
        "ds_read_b128 v[20:23], v57 offset:14336\n"
        "s_waitcnt lgkmcnt(2)\n"
        "v_add_f32 v16, v16, v24\n"
        "v_add_f32 v17, v17, v25\n"
        "v_add_f32 v18, v18, v26\n"
        "v_add_f32 v19, v19, v27\n"
        "ds_read_b128 v[24:27], v62 offset:0\n"
        "s_waitcnt lgkmcnt(2)\n"
        "v_add_f32 v16, v16, v28\n"
        "v_add_f32 v17, v17, v29\n"
        "v_add_f32 v18, v18, v30\n"
        "v_add_f32 v19, v19, v31\n"
        "ds_read_b128 v[28:31], v62 offset:16\n"
        "s_waitcnt lgkmcnt(2)\n"
        "v_add_f32 v16, v16, v20\n"
        "v_add_f32 v17, v17, v21\n"
        "page4:\n"
        "s_cbranch_execz warm_back\n"
        "v_add_f32 v18, v18, v22\n"
        "v_add_f32 v19, v19, v23\n"
        "s_waitcnt lgkmcnt(0)\n"
        "v_add_f32 v24, v24, v28\n"
        "v_add_f32 v25, v25, v29\n"
        "v_add_f32 v26, v26, v30\n"
        "v_add_f32 v27, v27, v31\n"
        "v_add_f32 v24, v24, v26\n"
        "v_add_f32 v25, v25, v27\n"
        "v_add_f32 v20, v16, v18\n"
        "v_add_f32 v24, v24, v25\n"
        "v_add_f32 v21, v17, v19\n"
        "v_rcp_f32 v28, v24\n"
        "v_add_f32 v20, v20, v21\n"
        "s_mov_b32 s88, 0x35800000\n"
        "v_cmp_lt_f32 vcc, 0x21800000, v24\n"
        "v_fma_f32 v20, |v20|, s88, v24\n"
        "s_mov_b32 s88, 0x71800000\n"
        "v_cmp_lt_f32 s[90:91], v20, s88\n"
        "v_mul_f32 v16, v16, v28\n"
        "v_mul_f32 v17, v17, v28\n"
        "s_and_b64 vcc, vcc, s[90:91]\n"
        "v_mul_f32 v18, v18, v28\n"
        "s_add_u32 s88, s72, 2048\n"
        "v_mul_f32 v19, v19, v28\n"
        "s_cmp_eq_u64 vcc, -1\n"
        "v_lshrrev_b32 v20, 1, v53\n"
        "s_cselect_b32 s88, s88, 67108864\n"
        "v_cvt_pk_bf16_f32 v16, v16, v17\n"
        "s_cselect_b32 s89, 0, 2\n"
        "v_cvt_pk_bf16_f32 v17, v18, v19\n"
        "v_min_u32 v16, -2, v16\n"
        "v_min_u32 v17, -2, v17\n"
        "buffer_store_dwordx2 v[16:17], v20, s[36:39], s88 offen sc1\n"
        "s_or_b32 s47, s47, s89\n"
        "skip_66:\n"
        "v_exp_f32 v2, v2\n"
        "v_exp_f32 v3, v3\n"
        "v_exp_f32 v4, v4\n"
        "v_mfma_f32_32x32x16_bf16 v[16:31], v[76:79], v[48:51], 0\n"
        "v_exp_f32 v5, v5\n"
        "v_exp_f32 v6, v6\n"
        "v_exp_f32 v7, v7\n"
        "v_exp_f32 v8, v8\n"
        "v_exp_f32 v9, v9\n"
        "v_smfmac_f32_32x32x32_bf16 v[32:47], v[112:115], v[0:7], v52\n"
        "v_exp_f32 v10, v10\n"
        "v_exp_f32 v11, v11\n"
        "v_exp_f32 v12, v12\n"
        "v_exp_f32 v13, v13\n"
        "v_exp_f32 v14, v14\n"
        "v_exp_f32 v15, v15\n"
        "s_setprio 2\n"
        "v_exp_f32 v16, v16\n"
        "v_exp_f32 v17, v17\n"
        "v_smfmac_f32_32x32x32_bf16 v[32:47], v[116:119], v[8:15], v52\n"
        "v_exp_f32 v18, v18\n"
        "v_exp_f32 v19, v19\n"
        "v_exp_f32 v20, v20\n"
        "v_mfma_f32_32x32x16_bf16 v[0:15], v[80:83], v[48:51], 0\n"
        "v_exp_f32 v21, v21\n"
        "v_exp_f32 v22, v22\n"
        "buffer_load_dwordx4 v53, s[32:35], s79 offen offset:2048 sc1 lds\n"
        "v_exp_f32 v23, v23\n"
        "v_exp_f32 v24, v24\n"
        "v_exp_f32 v25, v25\n"
        "v_smfmac_f32_32x32x32_bf16 v[32:47], v[120:123], v[16:23], v52\n"
        "v_exp_f32 v26, v26\n"
        "v_exp_f32 v27, v27\n"
        "v_exp_f32 v28, v28\n"
        "buffer_load_dwordx4 v53, s[32:35], s79 offen offset:3072 sc1 lds\n"
        "v_exp_f32 v29, v29\n"
        "v_exp_f32 v30, v30\n"
        "v_exp_f32 v31, v31\n"
        "s_setprio 1\n"
        "v_exp_f32 v0, v0\n"
        "v_exp_f32 v1, v1\n"
        "v_smfmac_f32_32x32x32_bf16 v[32:47], v[124:127], v[24:31], v52\n"
        "v_exp_f32 v2, v2\n"
        "v_exp_f32 v3, v3\n"
        "v_exp_f32 v4, v4\n"
        "v_mfma_f32_32x32x16_bf16 v[16:31], v[84:87], v[48:51], 0\n"
        "v_exp_f32 v5, v5\n"
        "v_exp_f32 v6, v6\n"
        "s_add_u32 m0, s81, 4096\n"
        "s_nop 0\n"
        "buffer_load_dwordx4 v53, s[32:35], s80 offen offset:0 sc1 lds\n"
        "v_exp_f32 v7, v7\n"
        "v_exp_f32 v8, v8\n"
        "v_exp_f32 v9, v9\n"
        "v_smfmac_f32_32x32x32_bf16 v[32:47], v[128:131], v[0:7], v52\n"
        "v_exp_f32 v10, v10\n"
        "v_exp_f32 v11, v11\n"
        "v_exp_f32 v12, v12\n"
        "buffer_load_dwordx4 v53, s[32:35], s80 offen offset:1024 sc1 lds\n"
        "v_exp_f32 v13, v13\n"
        "v_exp_f32 v14, v14\n"
        "v_exp_f32 v15, v15\n"
        "s_setprio 0\n"
        "v_exp_f32 v16, v16\n"
        "v_exp_f32 v17, v17\n"
        "v_smfmac_f32_32x32x32_bf16 v[32:47], v[132:135], v[8:15], v52\n"
        "v_exp_f32 v18, v18\n"
        "v_exp_f32 v19, v19\n"
        "v_exp_f32 v20, v20\n"
        "v_mfma_f32_32x32x16_bf16 v[0:15], v[88:91], v[48:51], 0\n"
        "v_exp_f32 v21, v21\n"
        "v_exp_f32 v22, v22\n"
        "v_exp_f32 v23, v23\n"
        "v_exp_f32 v24, v24\n"
        "v_exp_f32 v25, v25\n"
        "v_smfmac_f32_32x32x32_bf16 v[32:47], v[136:139], v[16:23], v52\n"
        "v_exp_f32 v26, v26\n"
        "v_exp_f32 v27, v27\n"
        "v_exp_f32 v28, v28\n"
        "v_exp_f32 v29, v29\n"
        "v_exp_f32 v30, v30\n"
        "v_exp_f32 v31, v31\n"
        "v_exp_f32 v0, v0\n"
        "v_exp_f32 v1, v1\n"
        "v_smfmac_f32_32x32x32_bf16 v[32:47], v[140:143], v[24:31], v52\n"
        "s_barrier\n"
        "s_setprio 3\n"
        "v_exp_f32 v2, v2\n"
        "v_exp_f32 v3, v3\n"
        "v_exp_f32 v4, v4\n"
        "v_mfma_f32_32x32x16_bf16 v[16:31], v[92:95], v[48:51], 0\n"
        "ds_read_b128 v[48:51], v53 offset:5120\n"
        "v_exp_f32 v5, v5\n"
        "v_exp_f32 v6, v6\n"
        "s_cmp_ge_u32 s3, 4\n"
        "s_cbranch_scc1 skip_67\n"
        "buffer_load_dwordx4 v53, s[32:35], s80 offen offset:2048 sc1 lds\n"
        "skip_67:\n"
        "v_exp_f32 v7, v7\n"
        "v_exp_f32 v8, v8\n"
        "v_exp_f32 v9, v9\n"
        "v_smfmac_f32_32x32x32_bf16 v[32:47], v[144:147], v[0:7], v52\n"
        "v_exp_f32 v10, v10\n"
        "v_exp_f32 v11, v11\n"
        "v_exp_f32 v12, v12\n"
        "v_exp_f32 v13, v13\n"
        "v_exp_f32 v14, v14\n"
        "v_exp_f32 v15, v15\n"
        "v_exp_f32 v16, v16\n"
        "v_exp_f32 v17, v17\n"
        "v_smfmac_f32_32x32x32_bf16 v[32:47], v[148:151], v[8:15], v52\n"
        "v_exp_f32 v18, v18\n"
        "v_exp_f32 v19, v19\n"
        "v_exp_f32 v20, v20\n"
        "s_xor_b32 s88, s3, 5\n"
        "s_lshl_b32 s88, s88, 8\n"
        "s_or_b32 s88, s88, s2\n"
        "s_cmp_ge_u32 s88, 176\n"
        "s_cbranch_scc1 skip_68\n"
        "s_waitcnt vmcnt(6)\n"
        "v_add_f32 v240, v240, v244\n"
        "v_add_f32 v241, v241, v245\n"
        "v_add_f32 v242, v242, v246\n"
        "v_add_f32 v243, v243, v247\n"
        "s_lshr_b32 s88, s2, 3\n"
        "s_lshl_b32 s88, s88, 10\n"
        "v_permlane32_swap_b32 v240, v242\n"
        "v_permlane32_swap_b32 v241, v243\n"
        "v_add_f32 v240, v240, v242\n"
        "v_add_f32 v241, v241, v243\n"
        "v_and_b32 v248, 0x200, v53\n"
        "v_or_b32 v248, v248, v249\n"
        "v_permlane16_swap_b32 v240, v241\n"
        "v_add_f32 v240, v240, v241\n"
        "s_and_b32 s89, s2, 7\n"
        "v_bfe_u32 v249, v53, 8, 1\n"
        "v_add_f32_dpp v240, v240, v240 row_ror:8 row_mask:0xf bank_mask:0xf bound_ctrl:1\n"
        "s_lshl_b32 s89, s89, 6\n"
        "v_lshl_or_b32 v248, v249, 3, v248\n"
        "v_add_f32_dpp v240, v240, v240 row_ror:4 row_mask:0xf bank_mask:0xf bound_ctrl:1\n"
        "s_add_u32 s88, s88, s89\n"
        "s_add_u32 s88, s88, 3932160\n"
        "v_mov_b32 v241, s45\n"
        "v_mul_f32 v240, 0x3fb8aa3b, v240\n"
        "buffer_store_dwordx2 v[240:241], v248, s[32:35], s88 offen sc1\n"
        "skip_68:\n"
        "s_waitcnt lgkmcnt(0)\n"
        "v_mfma_f32_32x32x16_bf16 v[0:15], v[64:67], v[48:51], 0\n"
        "v_exp_f32 v21, v21\n"
        "v_exp_f32 v22, v22\n"
        "s_cmp_ge_u32 s3, 4\n"
        "s_cbranch_scc1 skip_69\n"
        "buffer_load_dwordx4 v53, s[32:35], s80 offen offset:3072 sc1 lds\n"
        "skip_69:\n"
        "v_exp_f32 v23, v23\n"
        "v_exp_f32 v24, v24\n"
        "v_exp_f32 v25, v25\n"
        "v_smfmac_f32_32x32x32_bf16 v[32:47], v[152:155], v[16:23], v52\n"
        "v_exp_f32 v26, v26\n"
        "v_exp_f32 v27, v27\n"
        "v_exp_f32 v28, v28\n"
        "buffer_load_dwordx4 v[160:163], v53, s[32:35], s73 offen offset:0 sc1\n"
        "v_exp_f32 v29, v29\n"
        "v_exp_f32 v30, v30\n"
        "v_exp_f32 v31, v31\n"
        "v_exp_f32 v0, v0\n"
        "v_exp_f32 v1, v1\n"
        "v_smfmac_f32_32x32x32_bf16 v[32:47], v[156:159], v[24:31], v52\n"
        "v_exp_f32 v2, v2\n"
        "v_exp_f32 v3, v3\n"
        "v_exp_f32 v4, v4\n"
        "v_mfma_f32_32x32x16_bf16 v[16:31], v[68:71], v[48:51], 0\n"
        "v_exp_f32 v5, v5\n"
        "v_exp_f32 v6, v6\n"
        "buffer_load_dwordx4 v[164:167], v53, s[32:35], s73 offen offset:1024 sc1\n"
        "v_exp_f32 v7, v7\n"
        "v_exp_f32 v8, v8\n"
        "v_exp_f32 v9, v9\n"
        "v_exp_f32 v10, v10\n"
        "v_exp_f32 v11, v11\n"
        "v_exp_f32 v12, v12\n"
        "ds_write_b128 v54, v[32:35] offset:0\n"
        "ds_write_b128 v55, v[36:39] offset:0\n"
        "ds_write_b32 v56, v40 offset:0\n"
        "v_mov_b64 v[32:33], 0\n"
        "v_mov_b64 v[34:35], 0\n"
        "v_mov_b64 v[36:37], 0\n"
        "v_mov_b64 v[38:39], 0\n"
        "v_mov_b32 v40, 0\n"
        "v_exp_f32 v13, v13\n"
        "v_exp_f32 v14, v14\n"
        "v_smfmac_f32_32x32x32_bf16 v[32:47], v[96:99], v[0:7], v52\n"
        "v_exp_f32 v15, v15\n"
        "v_exp_f32 v16, v16\n"
        "v_exp_f32 v17, v17\n"
        "v_smfmac_f32_32x32x32_bf16 v[32:47], v[100:103], v[8:15], v52\n"
        "v_exp_f32 v18, v18\n"
        "v_exp_f32 v19, v19\n"
        "v_exp_f32 v20, v20\n"
        "v_mfma_f32_32x32x16_bf16 v[0:15], v[72:75], v[48:51], 0\n"
        "v_exp_f32 v21, v21\n"
        "v_exp_f32 v22, v22\n"
        "buffer_load_dwordx4 v[168:171], v53, s[32:35], s73 offen offset:2048 sc1\n"
        "v_exp_f32 v23, v23\n"
        "v_exp_f32 v24, v24\n"
        "v_exp_f32 v25, v25\n"
        "v_smfmac_f32_32x32x32_bf16 v[32:47], v[104:107], v[16:23], v52\n"
        "v_exp_f32 v26, v26\n"
        "v_exp_f32 v27, v27\n"
        "v_exp_f32 v28, v28\n"
        "buffer_load_dwordx4 v[172:175], v53, s[32:35], s73 offen offset:3072 sc1\n"
        "v_exp_f32 v29, v29\n"
        "v_exp_f32 v30, v30\n"
        "v_exp_f32 v31, v31\n"
        "s_setprio 2\n"
        "v_exp_f32 v0, v0\n"
        "v_exp_f32 v1, v1\n"
        "v_smfmac_f32_32x32x32_bf16 v[32:47], v[108:111], v[24:31], v52\n"
        "v_exp_f32 v2, v2\n"
        "v_exp_f32 v3, v3\n"
        "v_exp_f32 v4, v4\n"
        "v_mfma_f32_32x32x16_bf16 v[16:31], v[76:79], v[48:51], 0\n"
        "v_exp_f32 v5, v5\n"
        "v_exp_f32 v6, v6\n"
        "buffer_load_dwordx4 v[176:179], v53, s[32:35], s74 offen offset:0 sc1\n"
        "v_exp_f32 v7, v7\n"
        "v_exp_f32 v8, v8\n"
        "v_exp_f32 v9, v9\n"
        "v_smfmac_f32_32x32x32_bf16 v[32:47], v[112:115], v[0:7], v52\n"
        "v_exp_f32 v10, v10\n"
        "v_exp_f32 v11, v11\n"
        "v_exp_f32 v12, v12\n"
        "buffer_load_dwordx4 v[180:183], v53, s[32:35], s74 offen offset:1024 sc1\n"
        "v_exp_f32 v13, v13\n"
        "v_exp_f32 v14, v14\n"
        "v_exp_f32 v15, v15\n"
        "v_exp_f32 v16, v16\n"
        "v_exp_f32 v17, v17\n"
        "v_smfmac_f32_32x32x32_bf16 v[32:47], v[116:119], v[8:15], v52\n"
        "v_exp_f32 v18, v18\n"
        "v_exp_f32 v19, v19\n"
        "v_exp_f32 v20, v20\n"
        "v_mfma_f32_32x32x16_bf16 v[0:15], v[80:83], v[48:51], 0\n"
        "v_exp_f32 v21, v21\n"
        "v_exp_f32 v22, v22\n"
        "buffer_load_dwordx4 v[184:187], v53, s[32:35], s74 offen offset:2048 sc1\n"
        "v_exp_f32 v23, v23\n"
        "v_exp_f32 v24, v24\n"
        "v_exp_f32 v25, v25\n"
        "v_smfmac_f32_32x32x32_bf16 v[32:47], v[120:123], v[16:23], v52\n"
        "v_exp_f32 v26, v26\n"
        "v_exp_f32 v27, v27\n"
        "v_exp_f32 v28, v28\n"
        "buffer_load_dwordx4 v[188:191], v53, s[32:35], s74 offen offset:3072 sc1\n"
        "v_exp_f32 v29, v29\n"
        "v_exp_f32 v30, v30\n"
        "v_exp_f32 v31, v31\n"
        "v_exp_f32 v0, v0\n"
        "v_exp_f32 v1, v1\n"
        "v_smfmac_f32_32x32x32_bf16 v[32:47], v[124:127], v[24:31], v52\n"
        "v_exp_f32 v2, v2\n"
        "v_exp_f32 v3, v3\n"
        "v_exp_f32 v4, v4\n"
        "v_mfma_f32_32x32x16_bf16 v[16:31], v[84:87], v[48:51], 0\n"
        "v_exp_f32 v5, v5\n"
        "v_exp_f32 v6, v6\n"
        "buffer_load_dwordx4 v[192:195], v53, s[32:35], s75 offen offset:0 sc1\n"
        "v_exp_f32 v7, v7\n"
        "v_exp_f32 v8, v8\n"
        "v_exp_f32 v9, v9\n"
        "v_smfmac_f32_32x32x32_bf16 v[32:47], v[128:131], v[0:7], v52\n"
        "v_exp_f32 v10, v10\n"
        "v_exp_f32 v11, v11\n"
        "v_exp_f32 v12, v12\n"
        "buffer_load_dwordx4 v[196:199], v53, s[32:35], s75 offen offset:1024 sc1\n"
        "v_exp_f32 v13, v13\n"
        "v_exp_f32 v14, v14\n"
        "v_exp_f32 v15, v15\n"
        "v_exp_f32 v16, v16\n"
        "v_exp_f32 v17, v17\n"
        "v_smfmac_f32_32x32x32_bf16 v[32:47], v[132:135], v[8:15], v52\n"
        "v_exp_f32 v18, v18\n"
        "v_exp_f32 v19, v19\n"
        "v_exp_f32 v20, v20\n"
        "v_mfma_f32_32x32x16_bf16 v[0:15], v[88:91], v[48:51], 0\n"
        "v_exp_f32 v21, v21\n"
        "v_exp_f32 v22, v22\n"
        "buffer_load_dwordx4 v[200:203], v53, s[32:35], s75 offen offset:2048 sc1\n"
        "v_exp_f32 v23, v23\n"
        "v_exp_f32 v24, v24\n"
        "v_exp_f32 v25, v25\n"
        "v_smfmac_f32_32x32x32_bf16 v[32:47], v[136:139], v[16:23], v52\n"
        "v_exp_f32 v26, v26\n"
        "v_exp_f32 v27, v27\n"
        "v_exp_f32 v28, v28\n"
        "buffer_load_dwordx4 v[204:207], v53, s[32:35], s75 offen offset:3072 sc1\n"
        "v_exp_f32 v29, v29\n"
        "v_exp_f32 v30, v30\n"
        "v_exp_f32 v31, v31\n"
        "s_setprio 1\n"
        "v_exp_f32 v0, v0\n"
        "v_exp_f32 v1, v1\n"
        "v_smfmac_f32_32x32x32_bf16 v[32:47], v[140:143], v[24:31], v52\n"
        "v_exp_f32 v2, v2\n"
        "v_exp_f32 v3, v3\n"
        "v_exp_f32 v4, v4\n"
        "v_mfma_f32_32x32x16_bf16 v[16:31], v[92:95], v[48:51], 0\n"
        "ds_read_b128 v[48:51], v53 offset:6144\n"
        "v_exp_f32 v5, v5\n"
        "v_exp_f32 v6, v6\n"
        "s_mov_b32 m0, s83\n"
        "s_nop 0\n"
        "buffer_load_dwordx4 v53, s[36:39], s82 offen offset:0 sc1 lds\n"
        "v_exp_f32 v7, v7\n"
        "v_exp_f32 v8, v8\n"
        "v_exp_f32 v9, v9\n"
        "v_smfmac_f32_32x32x32_bf16 v[32:47], v[144:147], v[0:7], v52\n"
        "v_exp_f32 v10, v10\n"
        "v_exp_f32 v11, v11\n"
        "v_exp_f32 v12, v12\n"
        "buffer_load_dwordx4 v53, s[36:39], s82 offen offset:1024 sc1 lds\n"
        "v_exp_f32 v13, v13\n"
        "v_exp_f32 v14, v14\n"
        "v_exp_f32 v15, v15\n"
        "v_exp_f32 v16, v16\n"
        "v_exp_f32 v17, v17\n"
        "v_smfmac_f32_32x32x32_bf16 v[32:47], v[148:151], v[8:15], v52\n"
        "v_exp_f32 v18, v18\n"
        "v_exp_f32 v19, v19\n"
        "v_exp_f32 v20, v20\n"
        "s_waitcnt lgkmcnt(0)\n"
        "v_mfma_f32_32x32x16_bf16 v[0:15], v[64:67], v[48:51], 0\n"
        "v_exp_f32 v21, v21\n"
        "v_exp_f32 v22, v22\n"
        "buffer_load_dwordx4 v53, s[36:39], s82 offen offset:2048 sc1 lds\n"
        "v_exp_f32 v23, v23\n"
        "v_exp_f32 v24, v24\n"
        "v_exp_f32 v25, v25\n"
        "v_smfmac_f32_32x32x32_bf16 v[32:47], v[152:155], v[16:23], v52\n"
        "v_exp_f32 v26, v26\n"
        "v_exp_f32 v27, v27\n"
        "v_exp_f32 v28, v28\n"
        "buffer_load_dwordx4 v53, s[36:39], s82 offen offset:3072 sc1 lds\n"
        "v_exp_f32 v29, v29\n"
        "v_exp_f32 v30, v30\n"
        "v_exp_f32 v31, v31\n"
        "v_exp_f32 v0, v0\n"
        "v_exp_f32 v1, v1\n"
        "v_smfmac_f32_32x32x32_bf16 v[32:47], v[156:159], v[24:31], v52\n"
        "v_exp_f32 v2, v2\n"
        "v_exp_f32 v3, v3\n"
        "v_exp_f32 v4, v4\n"
        "v_mfma_f32_32x32x16_bf16 v[16:31], v[68:71], v[48:51], 0\n"
        "v_exp_f32 v5, v5\n"
        "v_exp_f32 v6, v6\n"
        "buffer_load_dwordx4 v[208:211], v53, s[32:35], s76 offen offset:0 sc1\n"
        "v_exp_f32 v7, v7\n"
        "v_exp_f32 v8, v8\n"
        "v_exp_f32 v9, v9\n"
        "v_exp_f32 v10, v10\n"
        "v_exp_f32 v11, v11\n"
        "v_exp_f32 v12, v12\n"
        "ds_write_b128 v54, v[32:35] offset:17408\n"
        "ds_write_b128 v55, v[36:39] offset:17408\n"
        "ds_write_b32 v56, v40 offset:17408\n"
        "v_mov_b64 v[32:33], 0\n"
        "v_mov_b64 v[34:35], 0\n"
        "v_mov_b64 v[36:37], 0\n"
        "v_mov_b64 v[38:39], 0\n"
        "v_mov_b32 v40, 0\n"
        "v_exp_f32 v13, v13\n"
        "v_exp_f32 v14, v14\n"
        "v_smfmac_f32_32x32x32_bf16 v[32:47], v[96:99], v[0:7], v52\n"
        "v_exp_f32 v15, v15\n"
        "s_setprio 0\n"
        "v_exp_f32 v16, v16\n"
        "v_exp_f32 v17, v17\n"
        "v_smfmac_f32_32x32x32_bf16 v[32:47], v[100:103], v[8:15], v52\n"
        "v_exp_f32 v18, v18\n"
        "v_exp_f32 v19, v19\n"
        "v_exp_f32 v20, v20\n"
        "v_mfma_f32_32x32x16_bf16 v[0:15], v[72:75], v[48:51], 0\n"
        "v_exp_f32 v21, v21\n"
        "v_exp_f32 v22, v22\n"
        "v_exp_f32 v23, v23\n"
        "v_exp_f32 v24, v24\n"
        "v_exp_f32 v25, v25\n"
        "v_smfmac_f32_32x32x32_bf16 v[32:47], v[104:107], v[16:23], v52\n"
        "v_exp_f32 v26, v26\n"
        "v_exp_f32 v27, v27\n"
        "v_exp_f32 v28, v28\n"
        "v_exp_f32 v29, v29\n"
        "v_exp_f32 v30, v30\n"
        "v_exp_f32 v31, v31\n"
        "v_exp_f32 v0, v0\n"
        "v_exp_f32 v1, v1\n"
        "v_smfmac_f32_32x32x32_bf16 v[32:47], v[108:111], v[24:31], v52\n"
        "s_waitcnt lgkmcnt(0)\n"
        "s_barrier\n"
        "s_setprio 3\n"
        "s_cmp_lg_u32 s3, 7\n"
        "s_cbranch_scc1 skip_70\n"
        "s_memrealtime s[92:93]\n"
        "v_mov_b32 v17, s45\n"
        "s_waitcnt lgkmcnt(0)\n"
        "v_mov_b32 v16, s92\n"
        "s_mov_b64 exec, 1\n"
        "buffer_store_dwordx2 v[16:17], off, s[36:39], s8 sc1\n"
        "s_mov_b64 exec, -1\n"
        "skip_70:\n"
        "s_cmp_ge_u32 s3, 4\n"
        "s_cbranch_scc1 skip_71\n"
        "ds_read_b128 v[16:19], v57 offset:0\n"
        "ds_read_b128 v[20:23], v57 offset:2048\n"
        "ds_read_b128 v[24:27], v57 offset:4096\n"
        "ds_read_b128 v[28:31], v57 offset:6144\n"
        "s_waitcnt lgkmcnt(2)\n"
        "v_add_f32 v16, v16, v20\n"
        "v_add_f32 v17, v17, v21\n"
        "v_add_f32 v18, v18, v22\n"
        "v_add_f32 v19, v19, v23\n"
        "ds_read_b128 v[20:23], v57 offset:8192\n"
        "s_waitcnt lgkmcnt(2)\n"
        "v_add_f32 v16, v16, v24\n"
        "v_add_f32 v17, v17, v25\n"
        "v_add_f32 v18, v18, v26\n"
        "v_add_f32 v19, v19, v27\n"
        "ds_read_b128 v[24:27], v57 offset:10240\n"
        "s_waitcnt lgkmcnt(2)\n"
        "v_add_f32 v16, v16, v28\n"
        "v_add_f32 v17, v17, v29\n"
        "v_add_f32 v18, v18, v30\n"
        "v_add_f32 v19, v19, v31\n"
        "ds_read_b128 v[28:31], v57 offset:12288\n"
        "s_waitcnt lgkmcnt(2)\n"
        "v_add_f32 v16, v16, v20\n"
        "v_add_f32 v17, v17, v21\n"
        "v_add_f32 v18, v18, v22\n"
        "v_add_f32 v19, v19, v23\n"
        "ds_read_b128 v[20:23], v57 offset:14336\n"
        "s_waitcnt lgkmcnt(2)\n"
        "v_add_f32 v16, v16, v24\n"
        "v_add_f32 v17, v17, v25\n"
        "v_add_f32 v18, v18, v26\n"
        "v_add_f32 v19, v19, v27\n"
        "ds_read_b128 v[24:27], v62 offset:0\n"
        "s_waitcnt lgkmcnt(2)\n"
        "v_add_f32 v16, v16, v28\n"
        "v_add_f32 v17, v17, v29\n"
        "v_add_f32 v18, v18, v30\n"
        "v_add_f32 v19, v19, v31\n"
        "ds_read_b128 v[28:31], v62 offset:16\n"
        "s_waitcnt lgkmcnt(2)\n"
        "v_add_f32 v16, v16, v20\n"
        "v_add_f32 v17, v17, v21\n"
        "v_add_f32 v18, v18, v22\n"
        "v_add_f32 v19, v19, v23\n"
        "s_waitcnt lgkmcnt(0)\n"
        "v_add_f32 v24, v24, v28\n"
        "v_add_f32 v25, v25, v29\n"
        "v_add_f32 v26, v26, v30\n"
        "v_add_f32 v27, v27, v31\n"
        "v_add_f32 v24, v24, v26\n"
        "v_add_f32 v25, v25, v27\n"
        "v_add_f32 v20, v16, v18\n"
        "v_add_f32 v24, v24, v25\n"
        "v_add_f32 v21, v17, v19\n"
        "v_rcp_f32 v28, v24\n"
        "v_add_f32 v20, v20, v21\n"
        "s_mov_b32 s88, 0x35800000\n"
        "v_cmp_lt_f32 vcc, 0x21800000, v24\n"
        "v_fma_f32 v20, |v20|, s88, v24\n"
        "s_mov_b32 s88, 0x71800000\n"
        "v_cmp_lt_f32 s[90:91], v20, s88\n"
        "v_mul_f32 v16, v16, v28\n"
        "v_mul_f32 v17, v17, v28\n"
        "s_and_b64 vcc, vcc, s[90:91]\n"
        "v_mul_f32 v18, v18, v28\n"
        "s_add_u32 s88, s72, 34816\n"
        "v_mul_f32 v19, v19, v28\n"
        "s_cmp_eq_u64 vcc, -1\n"
        "v_lshrrev_b32 v20, 1, v53\n"
        "s_cselect_b32 s88, s88, 67108864\n"
        "v_cvt_pk_bf16_f32 v16, v16, v17\n"
        "s_cselect_b32 s89, 0, 4\n"
        "v_cvt_pk_bf16_f32 v17, v18, v19\n"
        "v_min_u32 v16, -2, v16\n"
        "v_min_u32 v17, -2, v17\n"
        "buffer_store_dwordx2 v[16:17], v20, s[36:39], s88 offen sc1\n"
        "s_or_b32 s47, s47, s89\n"
        "skip_71:\n"
        "v_exp_f32 v2, v2\n"
        "v_exp_f32 v3, v3\n"
        "v_exp_f32 v4, v4\n"
        "v_mfma_f32_32x32x16_bf16 v[16:31], v[76:79], v[48:51], 0\n"
        "v_exp_f32 v5, v5\n"
        "v_exp_f32 v6, v6\n"
        "v_exp_f32 v7, v7\n"
        "v_exp_f32 v8, v8\n"
        "v_exp_f32 v9, v9\n"
        "v_smfmac_f32_32x32x32_bf16 v[32:47], v[112:115], v[0:7], v52\n"
        "v_exp_f32 v10, v10\n"
        "v_exp_f32 v11, v11\n"
        "v_exp_f32 v12, v12\n"
        "v_exp_f32 v13, v13\n"
        "v_exp_f32 v14, v14\n"
        "v_exp_f32 v15, v15\n"
        "s_setprio 2\n"
        "v_exp_f32 v16, v16\n"
        "v_exp_f32 v17, v17\n"
        "v_smfmac_f32_32x32x32_bf16 v[32:47], v[116:119], v[8:15], v52\n"
        "v_exp_f32 v18, v18\n"
        "v_exp_f32 v19, v19\n"
        "v_exp_f32 v20, v20\n"
        "v_mfma_f32_32x32x16_bf16 v[0:15], v[80:83], v[48:51], 0\n"
        "v_exp_f32 v21, v21\n"
        "v_exp_f32 v22, v22\n"
        "buffer_load_dwordx4 v[212:215], v53, s[32:35], s76 offen offset:1024 sc1\n"
        "v_exp_f32 v23, v23\n"
        "v_exp_f32 v24, v24\n"
        "v_exp_f32 v25, v25\n"
        "v_smfmac_f32_32x32x32_bf16 v[32:47], v[120:123], v[16:23], v52\n"
        "v_exp_f32 v26, v26\n"
        "v_exp_f32 v27, v27\n"
        "v_exp_f32 v28, v28\n"
        "buffer_load_dwordx4 v[216:219], v53, s[32:35], s76 offen offset:2048 sc1\n"
        "v_exp_f32 v29, v29\n"
        "v_exp_f32 v30, v30\n"
        "v_exp_f32 v31, v31\n"
        "s_setprio 1\n"
        "v_exp_f32 v0, v0\n"
        "v_exp_f32 v1, v1\n"
        "v_smfmac_f32_32x32x32_bf16 v[32:47], v[124:127], v[24:31], v52\n"
        "v_exp_f32 v2, v2\n"
        "v_exp_f32 v3, v3\n"
        "v_exp_f32 v4, v4\n"
        "v_mfma_f32_32x32x16_bf16 v[16:31], v[84:87], v[48:51], 0\n"
        "v_exp_f32 v5, v5\n"
        "v_exp_f32 v6, v6\n"
        "buffer_load_dwordx4 v[220:223], v53, s[32:35], s76 offen offset:3072 sc1\n"
        "v_exp_f32 v7, v7\n"
        "v_exp_f32 v8, v8\n"
        "v_exp_f32 v9, v9\n"
        "v_smfmac_f32_32x32x32_bf16 v[32:47], v[128:131], v[0:7], v52\n"
        "v_exp_f32 v10, v10\n"
        "v_exp_f32 v11, v11\n"
        "v_exp_f32 v12, v12\n"
        "buffer_load_dwordx4 v[224:227], v53, s[32:35], s77 offen offset:0 sc1\n"
        "v_exp_f32 v13, v13\n"
        "v_exp_f32 v14, v14\n"
        "v_exp_f32 v15, v15\n"
        "s_setprio 0\n"
        "v_exp_f32 v16, v16\n"
        "v_exp_f32 v17, v17\n"
        "v_smfmac_f32_32x32x32_bf16 v[32:47], v[132:135], v[8:15], v52\n"
        "v_exp_f32 v18, v18\n"
        "v_exp_f32 v19, v19\n"
        "v_exp_f32 v20, v20\n"
        "v_mfma_f32_32x32x16_bf16 v[0:15], v[88:91], v[48:51], 0\n"
        "v_exp_f32 v21, v21\n"
        "v_exp_f32 v22, v22\n"
        "v_exp_f32 v23, v23\n"
        "v_exp_f32 v24, v24\n"
        "v_exp_f32 v25, v25\n"
        "v_smfmac_f32_32x32x32_bf16 v[32:47], v[136:139], v[16:23], v52\n"
        "v_exp_f32 v26, v26\n"
        "v_exp_f32 v27, v27\n"
        "v_exp_f32 v28, v28\n"
        "v_exp_f32 v29, v29\n"
        "v_exp_f32 v30, v30\n"
        "v_exp_f32 v31, v31\n"
        "v_exp_f32 v0, v0\n"
        "v_exp_f32 v1, v1\n"
        "v_smfmac_f32_32x32x32_bf16 v[32:47], v[140:143], v[24:31], v52\n"
        "s_waitcnt vmcnt(5)\n"
        "s_barrier\n"
        "s_setprio 3\n"
        "v_exp_f32 v2, v2\n"
        "v_exp_f32 v3, v3\n"
        "v_exp_f32 v4, v4\n"
        "v_mfma_f32_32x32x16_bf16 v[16:31], v[92:95], v[48:51], 0\n"
        "ds_read_b128 v[48:51], v53 offset:7168\n"
        "v_exp_f32 v5, v5\n"
        "v_exp_f32 v6, v6\n"
        "buffer_load_dwordx4 v[228:231], v53, s[32:35], s77 offen offset:1024 sc1\n"
        "v_exp_f32 v7, v7\n"
        "v_exp_f32 v8, v8\n"
        "v_exp_f32 v9, v9\n"
        "v_smfmac_f32_32x32x32_bf16 v[32:47], v[144:147], v[0:7], v52\n"
        "v_exp_f32 v10, v10\n"
        "v_exp_f32 v11, v11\n"
        "v_exp_f32 v12, v12\n"
        "buffer_load_dwordx4 v[232:235], v53, s[32:35], s77 offen offset:2048 sc1\n"
        "v_exp_f32 v13, v13\n"
        "v_exp_f32 v14, v14\n"
        "v_exp_f32 v15, v15\n"
        "v_exp_f32 v16, v16\n"
        "v_exp_f32 v17, v17\n"
        "v_smfmac_f32_32x32x32_bf16 v[32:47], v[148:151], v[8:15], v52\n"
        "v_exp_f32 v18, v18\n"
        "v_exp_f32 v19, v19\n"
        "v_exp_f32 v20, v20\n"
        "s_waitcnt lgkmcnt(0)\n"
        "v_mfma_f32_32x32x16_bf16 v[0:15], v[64:67], v[48:51], 0\n"
        "v_exp_f32 v21, v21\n"
        "v_exp_f32 v22, v22\n"
        "buffer_load_dwordx4 v[236:239], v53, s[32:35], s77 offen offset:3072 sc1\n"
        "v_exp_f32 v23, v23\n"
        "v_exp_f32 v24, v24\n"
        "v_exp_f32 v25, v25\n"
        "v_smfmac_f32_32x32x32_bf16 v[32:47], v[152:155], v[16:23], v52\n"
        "v_exp_f32 v26, v26\n"
        "v_exp_f32 v27, v27\n"
        "v_exp_f32 v28, v28\n"
        "buffer_load_dwordx4 v[240:243], v53, s[32:35], s78 offen offset:0 sc1\n"
        "v_exp_f32 v29, v29\n"
        "v_exp_f32 v30, v30\n"
        "v_exp_f32 v31, v31\n"
        "s_setprio 2\n"
        "v_exp_f32 v0, v0\n"
        "v_exp_f32 v1, v1\n"
        "v_smfmac_f32_32x32x32_bf16 v[32:47], v[156:159], v[24:31], v52\n"
        "v_exp_f32 v2, v2\n"
        "v_exp_f32 v3, v3\n"
        "v_exp_f32 v4, v4\n"
        "v_mfma_f32_32x32x16_bf16 v[16:31], v[68:71], v[48:51], 0\n"
        "v_exp_f32 v5, v5\n"
        "v_exp_f32 v6, v6\n"
        "buffer_load_dwordx4 v[244:247], v53, s[32:35], s78 offen offset:1024 sc1\n"
        "v_exp_f32 v7, v7\n"
        "v_exp_f32 v8, v8\n"
        "v_exp_f32 v9, v9\n"
        "v_exp_f32 v10, v10\n"
        "v_exp_f32 v11, v11\n"
        "v_exp_f32 v12, v12\n"
        "ds_write_b128 v54, v[32:35] offset:0\n"
        "ds_write_b128 v55, v[36:39] offset:0\n"
        "ds_write_b32 v56, v40 offset:0\n"
        "v_mov_b64 v[32:33], 0\n"
        "v_mov_b64 v[34:35], 0\n"
        "v_mov_b64 v[36:37], 0\n"
        "v_mov_b64 v[38:39], 0\n"
        "v_mov_b32 v40, 0\n"
        "v_exp_f32 v13, v13\n"
        "v_exp_f32 v14, v14\n"
        "v_smfmac_f32_32x32x32_bf16 v[32:47], v[96:99], v[0:7], v52\n"
        "v_exp_f32 v15, v15\n"
        "v_exp_f32 v16, v16\n"
        "v_exp_f32 v17, v17\n"
        "v_smfmac_f32_32x32x32_bf16 v[32:47], v[100:103], v[8:15], v52\n"
        "v_exp_f32 v18, v18\n"
        "v_exp_f32 v19, v19\n"
        "v_exp_f32 v20, v20\n"
        "v_mfma_f32_32x32x16_bf16 v[0:15], v[72:75], v[48:51], 0\n"
        "v_exp_f32 v21, v21\n"
        "v_exp_f32 v22, v22\n"
        "buffer_load_dwordx4 v[248:251], v53, s[32:35], s78 offen offset:2048 sc1\n"
        "v_exp_f32 v23, v23\n"
        "v_exp_f32 v24, v24\n"
        "v_exp_f32 v25, v25\n"
        "v_smfmac_f32_32x32x32_bf16 v[32:47], v[104:107], v[16:23], v52\n"
        "v_exp_f32 v26, v26\n"
        "v_exp_f32 v27, v27\n"
        "v_exp_f32 v28, v28\n"
        "buffer_load_dwordx4 v[252:255], v53, s[32:35], s78 offen offset:3072 sc1\n"
        "v_exp_f32 v29, v29\n"
        "v_exp_f32 v30, v30\n"
        "v_exp_f32 v31, v31\n"
        "s_cmp_ge_u32 s3, 4\n"
        "s_cbranch_scc1 tail_6\n"
        "s_setprio 0\n"
        "v_exp_f32 v0, v0\n"
        "v_exp_f32 v1, v1\n"
        "v_smfmac_f32_32x32x32_bf16 v[32:47], v[108:111], v[24:31], v52\n"
        "v_exp_f32 v2, v2\n"
        "v_exp_f32 v3, v3\n"
        "v_exp_f32 v4, v4\n"
        "v_mfma_f32_32x32x16_bf16 v[16:31], v[76:79], v[48:51], 0\n"
        "v_exp_f32 v5, v5\n"
        "v_exp_f32 v6, v6\n"
        "buffer_load_dwordx4 v[64:67], v53, s[32:35], s17 offen offset:0 sc1\n"
        "v_exp_f32 v7, v7\n"
        "v_exp_f32 v8, v8\n"
        "v_exp_f32 v9, v9\n"
        "v_smfmac_f32_32x32x32_bf16 v[32:47], v[112:115], v[0:7], v52\n"
        "v_exp_f32 v10, v10\n"
        "v_exp_f32 v11, v11\n"
        "v_exp_f32 v12, v12\n"
        "buffer_load_dwordx4 v[68:71], v53, s[32:35], s17 offen offset:1024 sc1\n"
        "v_exp_f32 v13, v13\n"
        "v_exp_f32 v14, v14\n"
        "v_exp_f32 v15, v15\n"
        "v_exp_f32 v16, v16\n"
        "v_exp_f32 v17, v17\n"
        "v_smfmac_f32_32x32x32_bf16 v[32:47], v[116:119], v[8:15], v52\n"
        "v_exp_f32 v18, v18\n"
        "v_exp_f32 v19, v19\n"
        "v_exp_f32 v20, v20\n"
        "v_mfma_f32_32x32x16_bf16 v[0:15], v[80:83], v[48:51], 0\n"
        "v_exp_f32 v21, v21\n"
        "v_exp_f32 v22, v22\n"
        "buffer_load_dwordx4 v[72:75], v53, s[32:35], s17 offen offset:2048 sc1\n"
        "v_exp_f32 v23, v23\n"
        "v_exp_f32 v24, v24\n"
        "v_exp_f32 v25, v25\n"
        "v_smfmac_f32_32x32x32_bf16 v[32:47], v[120:123], v[16:23], v52\n"
        "v_exp_f32 v26, v26\n"
        "v_exp_f32 v27, v27\n"
        "v_exp_f32 v28, v28\n"
        "buffer_load_dwordx4 v[76:79], v53, s[32:35], s17 offen offset:3072 sc1\n"
        "v_exp_f32 v29, v29\n"
        "v_exp_f32 v30, v30\n"
        "v_exp_f32 v31, v31\n"
        "v_exp_f32 v0, v0\n"
        "v_exp_f32 v1, v1\n"
        "v_smfmac_f32_32x32x32_bf16 v[32:47], v[124:127], v[24:31], v52\n"
        "v_exp_f32 v2, v2\n"
        "v_exp_f32 v3, v3\n"
        "v_exp_f32 v4, v4\n"
        "v_mfma_f32_32x32x16_bf16 v[16:31], v[84:87], v[48:51], 0\n"
        "v_exp_f32 v5, v5\n"
        "v_exp_f32 v6, v6\n"
        "buffer_load_dwordx4 v[80:83], v53, s[32:35], s18 offen offset:0 sc1\n"
        "v_exp_f32 v7, v7\n"
        "v_exp_f32 v8, v8\n"
        "v_exp_f32 v9, v9\n"
        "v_smfmac_f32_32x32x32_bf16 v[32:47], v[128:131], v[0:7], v52\n"
        "v_exp_f32 v10, v10\n"
        "v_exp_f32 v11, v11\n"
        "v_exp_f32 v12, v12\n"
        "buffer_load_dwordx4 v[84:87], v53, s[32:35], s18 offen offset:1024 sc1\n"
        "v_exp_f32 v13, v13\n"
        "v_exp_f32 v14, v14\n"
        "v_exp_f32 v15, v15\n"
        "v_exp_f32 v16, v16\n"
        "v_exp_f32 v17, v17\n"
        "v_smfmac_f32_32x32x32_bf16 v[32:47], v[132:135], v[8:15], v52\n"
        "v_exp_f32 v18, v18\n"
        "v_exp_f32 v19, v19\n"
        "v_exp_f32 v20, v20\n"
        "v_mfma_f32_32x32x16_bf16 v[0:15], v[88:91], v[48:51], 0\n"
        "v_exp_f32 v21, v21\n"
        "v_exp_f32 v22, v22\n"
        "buffer_load_dwordx4 v[88:91], v53, s[32:35], s18 offen offset:2048 sc1\n"
        "v_exp_f32 v23, v23\n"
        "v_exp_f32 v24, v24\n"
        "v_exp_f32 v25, v25\n"
        "v_smfmac_f32_32x32x32_bf16 v[32:47], v[136:139], v[16:23], v52\n"
        "v_exp_f32 v26, v26\n"
        "v_exp_f32 v27, v27\n"
        "v_exp_f32 v28, v28\n"
        "buffer_load_dwordx4 v[96:99], v53, s[32:35], s9 offen offset:0 sc1\n"
        "v_exp_f32 v29, v29\n"
        "v_exp_f32 v30, v30\n"
        "v_exp_f32 v31, v31\n"
        "v_exp_f32 v0, v0\n"
        "v_exp_f32 v1, v1\n"
        "v_smfmac_f32_32x32x32_bf16 v[32:47], v[140:143], v[24:31], v52\n"
        "v_exp_f32 v2, v2\n"
        "v_exp_f32 v3, v3\n"
        "v_exp_f32 v4, v4\n"
        "v_mfma_f32_32x32x16_bf16 v[16:31], v[92:95], v[48:51], 0\n"
        "v_exp_f32 v5, v5\n"
        "v_exp_f32 v6, v6\n"
        "v_add_u32 v48, 160768, v53\n"
        "ds_read_b128 v[48:51], v48\n"
        "buffer_load_dwordx4 v[92:95], v53, s[32:35], s18 offen offset:3072 sc1\n"
        "v_exp_f32 v7, v7\n"
        "v_exp_f32 v8, v8\n"
        "v_exp_f32 v9, v9\n"
        "v_smfmac_f32_32x32x32_bf16 v[32:47], v[144:147], v[0:7], v52\n"
        "v_exp_f32 v10, v10\n"
        "v_exp_f32 v11, v11\n"
        "v_exp_f32 v12, v12\n"
        "buffer_load_dwordx4 v[100:103], v53, s[32:35], s10 offen offset:0 sc1\n"
        "v_exp_f32 v13, v13\n"
        "v_exp_f32 v14, v14\n"
        "v_exp_f32 v15, v15\n"
        "v_exp_f32 v16, v16\n"
        "v_exp_f32 v17, v17\n"
        "v_smfmac_f32_32x32x32_bf16 v[32:47], v[148:151], v[8:15], v52\n"
        "v_exp_f32 v18, v18\n"
        "v_exp_f32 v19, v19\n"
        "v_exp_f32 v20, v20\n"
        "v_exp_f32 v21, v21\n"
        "v_exp_f32 v22, v22\n"
        "buffer_load_dwordx4 v[104:107], v53, s[32:35], s10 offen offset:1024 sc1\n"
        "v_exp_f32 v23, v23\n"
        "v_exp_f32 v24, v24\n"
        "v_exp_f32 v25, v25\n"
        "v_smfmac_f32_32x32x32_bf16 v[32:47], v[152:155], v[16:23], v52\n"
        "v_exp_f32 v26, v26\n"
        "v_exp_f32 v27, v27\n"
        "v_exp_f32 v28, v28\n"
        "buffer_load_dwordx4 v[108:111], v53, s[32:35], s10 offen offset:2048 sc1\n"
        "v_exp_f32 v29, v29\n"
        "v_exp_f32 v30, v30\n"
        "v_exp_f32 v31, v31\n"
        "s_nop 1\n"
        "v_smfmac_f32_32x32x32_bf16 v[32:47], v[156:159], v[24:31], v52\n"
        "s_nop 11\n"
        "s_nop 0\n"
        "page5:\n"
        "s_cbranch_execz warm_back\n"
        "ds_write_b128 v54, v[32:35] offset:17408\n"
        "ds_write_b128 v55, v[36:39] offset:17408\n"
        "ds_write_b32 v56, v40 offset:17408\n"
        "s_add_u32 s88, s86, 2048\n"
        "s_mov_b32 m0, s87\n"
        "s_nop 0\n"
        "buffer_load_dwordx4 v53, s[36:39], s86 offen offset:0 sc1 lds\n"
        "buffer_load_dwordx4 v53, s[36:39], s86 offen offset:1024 sc1 lds\n"
        "buffer_load_dwordx4 v53, s[36:39], s88 offen offset:2048 sc1 lds\n"
        "buffer_load_dwordx4 v53, s[36:39], s88 offen offset:3072 sc1 lds\n"
        "s_waitcnt lgkmcnt(3)\n"
        "v_add_u32 v41, 59392, v48\n"
        "v_add_u32 v39, s25, v53\n"
        "ds_read_b128 v[8:11], v41 offset:0\n"
        "ds_read_b128 v[12:15], v41 offset:256\n"
        "ds_read_b128 v[16:19], v41 offset:4096\n"
        "ds_read_b128 v[20:23], v41 offset:4352\n"
        "ds_read_b128 v[24:27], v41 offset:8192\n"
        "ds_read_b128 v[28:31], v41 offset:8448\n"
        "s_waitcnt vmcnt(15) lgkmcnt(5)\n"
        "v_mfma_f32_16x16x32_bf16 v[0:3], v[64:67], v[8:11], 0\n"
        "ds_read_b128 v[8:11], v41 offset:12288\n"
        "buffer_load_dwordx4 v[112:115], v53, s[32:35], s13 offen offset:0 sc1\n"
        "s_waitcnt lgkmcnt(5)\n"
        "v_mfma_f32_16x16x32_bf16 v[4:7], v[64:67], v[12:15], 0\n"
        "ds_read_b128 v[12:15], v41 offset:12544\n"
        "buffer_load_dwordx4 v[116:119], v53, s[32:35], s14 offen offset:0 sc1\n"
        "s_waitcnt vmcnt(16) lgkmcnt(5)\n"
        "v_mfma_f32_16x16x32_bf16 v[0:3], v[68:71], v[16:19], v[0:3]\n"
        "ds_read_b128 v[16:19], v41 offset:16384\n"
        "buffer_load_dwordx4 v[120:123], v53, s[32:35], s14 offen offset:1024 sc1\n"
        "s_waitcnt lgkmcnt(5)\n"
        "v_mfma_f32_16x16x32_bf16 v[4:7], v[68:71], v[20:23], v[4:7]\n"
        "ds_read_b128 v[20:23], v41 offset:16640\n"
        "buffer_load_dwordx4 v[124:127], v53, s[32:35], s14 offen offset:2048 sc1\n"
        "s_waitcnt vmcnt(17) lgkmcnt(5)\n"
        "v_mfma_f32_16x16x32_bf16 v[0:3], v[72:75], v[24:27], v[0:3]\n"
        "ds_read_b128 v[24:27], v41 offset:20480\n"
        "buffer_load_dwordx4 v[128:131], v53, s[32:35], s10 offen offset:3072 sc1\n"
        "s_waitcnt lgkmcnt(5)\n"
        "v_mfma_f32_16x16x32_bf16 v[4:7], v[72:75], v[28:31], v[4:7]\n"
        "ds_read_b128 v[28:31], v41 offset:20736\n"
        "buffer_load_dwordx4 v[132:135], v53, s[32:35], s14 offen offset:3072 sc1\n"
        "s_waitcnt vmcnt(18) lgkmcnt(5)\n"
        "v_mfma_f32_16x16x32_bf16 v[0:3], v[76:79], v[8:11], v[0:3]\n"
        "ds_read_b128 v[8:11], v41 offset:24576\n"
        "buffer_load_dwordx4 v[136:139], v53, s[32:35], s11 offen offset:0 sc1\n"
        "s_waitcnt lgkmcnt(5)\n"
        "v_mfma_f32_16x16x32_bf16 v[4:7], v[76:79], v[12:15], v[4:7]\n"
        "ds_read_b128 v[12:15], v41 offset:24832\n"
        "buffer_load_dwordx4 v[140:143], v53, s[32:35], s15 offen offset:0 sc1\n"
        "s_waitcnt vmcnt(19) lgkmcnt(5)\n"
        "v_mfma_f32_16x16x32_bf16 v[0:3], v[80:83], v[16:19], v[0:3]\n"
        "ds_read_b128 v[16:19], v41 offset:28672\n"
        "buffer_load_dwordx4 v[144:147], v53, s[32:35], s11 offen offset:1024 sc1\n"
        "s_waitcnt lgkmcnt(5)\n"
        "v_mfma_f32_16x16x32_bf16 v[4:7], v[80:83], v[20:23], v[4:7]\n"
        "ds_read_b128 v[20:23], v41 offset:28928\n"
        "buffer_load_dwordx4 v[148:151], v53, s[32:35], s15 offen offset:1024 sc1\n"
        "s_waitcnt vmcnt(20) lgkmcnt(5)\n"
        "v_mfma_f32_16x16x32_bf16 v[0:3], v[84:87], v[24:27], v[0:3]\n"
        "s_waitcnt lgkmcnt(4)\n"
        "v_mfma_f32_16x16x32_bf16 v[4:7], v[84:87], v[28:31], v[4:7]\n"
        "s_waitcnt vmcnt(19) lgkmcnt(3)\n"
        "v_mfma_f32_16x16x32_bf16 v[0:3], v[88:91], v[8:11], v[0:3]\n"
        "s_waitcnt lgkmcnt(2)\n"
        "v_mfma_f32_16x16x32_bf16 v[4:7], v[88:91], v[12:15], v[4:7]\n"
        "s_waitcnt vmcnt(17) lgkmcnt(1)\n"
        "v_mfma_f32_16x16x32_bf16 v[0:3], v[92:95], v[16:19], v[0:3]\n"
        "s_waitcnt lgkmcnt(0)\n"
        "v_mfma_f32_16x16x32_bf16 v[4:7], v[92:95], v[20:23], v[4:7]\n"
        "s_nop 5\n"
        "ds_write_b128 v39, v[0:3]\n"
        "s_nop 0\n"
        "ds_write_b128 v39, v[4:7] offset:1024\n"
        "v_lshl_add_u32 v42, v53, 2, s87\n"
        "s_mov_b32 s93, 6\n"
        "s_waitcnt vmcnt(10)\n"
        "ds_read_b32 v43, v42\n"
        "chk_75:\n"
        "s_waitcnt lgkmcnt(0)\n"
        "v_cmp_eq_u32 vcc, -1, v43\n"
        "s_cbranch_vccnz robe_77\n"
        "go_76:\n"
        "s_waitcnt lgkmcnt(0)\n"
        "s_barrier\n"
        "s_setprio 3\n"
        "ds_read_b128 v[0:3], v57 offset:0\n"
        "ds_read_b128 v[4:7], v57 offset:2048\n"
        "ds_read_b128 v[8:11], v57 offset:4096\n"
        "ds_read_b128 v[12:15], v57 offset:6144\n"
        "ds_read_b128 v[16:19], v57 offset:8192\n"
        "ds_read_b128 v[20:23], v57 offset:10240\n"
        "ds_read_b128 v[24:27], v57 offset:12288\n"
        "ds_read_b128 v[28:31], v57 offset:14336\n"
        "ds_read_b128 v[32:35], v62 offset:0\n"
        "ds_read_b128 v[36:39], v62 offset:16\n"
        "s_waitcnt lgkmcnt(8)\n"
        "v_add_f32 v0, v0, v4\n"
        "v_add_f32 v1, v1, v5\n"
        "v_add_f32 v2, v2, v6\n"
        "v_add_f32 v3, v3, v7\n"
        "s_waitcnt lgkmcnt(7)\n"
        "v_add_f32 v0, v0, v8\n"
        "v_add_f32 v1, v1, v9\n"
        "v_add_f32 v2, v2, v10\n"
        "v_add_f32 v3, v3, v11\n"
        "s_waitcnt lgkmcnt(6)\n"
        "v_add_f32 v0, v0, v12\n"
        "v_add_f32 v1, v1, v13\n"
        "v_add_f32 v2, v2, v14\n"
        "v_add_f32 v3, v3, v15\n"
        "s_waitcnt lgkmcnt(5)\n"
        "v_add_f32 v0, v0, v16\n"
        "v_add_f32 v1, v1, v17\n"
        "v_add_f32 v2, v2, v18\n"
        "v_add_f32 v3, v3, v19\n"
        "s_waitcnt lgkmcnt(4)\n"
        "v_add_f32 v0, v0, v20\n"
        "v_add_f32 v1, v1, v21\n"
        "v_add_f32 v2, v2, v22\n"
        "v_add_f32 v3, v3, v23\n"
        "s_waitcnt lgkmcnt(3)\n"
        "v_add_f32 v0, v0, v24\n"
        "v_add_f32 v1, v1, v25\n"
        "v_add_f32 v2, v2, v26\n"
        "v_add_f32 v3, v3, v27\n"
        "s_waitcnt lgkmcnt(2)\n"
        "v_add_f32 v0, v0, v28\n"
        "v_add_f32 v1, v1, v29\n"
        "v_add_f32 v2, v2, v30\n"
        "v_add_f32 v3, v3, v31\n"
        "s_waitcnt lgkmcnt(0)\n"
        "v_add_f32 v32, v32, v36\n"
        "v_add_f32 v33, v33, v37\n"
        "v_add_f32 v34, v34, v38\n"
        "v_add_f32 v35, v35, v39\n"
        "v_add_f32 v32, v32, v34\n"
        "v_add_f32 v33, v33, v35\n"
        "v_add_f32 v4, v0, v2\n"
        "v_add_f32 v32, v32, v33\n"
        "v_add_f32 v5, v1, v3\n"
        "v_rcp_f32 v36, v32\n"
        "v_add_f32 v4, v4, v5\n"
        "s_mov_b32 s88, 0x35800000\n"
        "v_cmp_lt_f32 vcc, 0x21800000, v32\n"
        "v_fma_f32 v4, |v4|, s88, v32\n"
        "s_mov_b32 s88, 0x71800000\n"
        "v_cmp_lt_f32 s[90:91], v4, s88\n"
        "v_mul_f32 v0, v0, v36\n"
        "v_mul_f32 v1, v1, v36\n"
        "s_and_b64 vcc, vcc, s[90:91]\n"
        "v_mul_f32 v2, v2, v36\n"
        "s_add_u32 s88, s72, 32768\n"
        "v_mul_f32 v3, v3, v36\n"
        "s_cmp_eq_u64 vcc, -1\n"
        "v_lshrrev_b32 v4, 1, v53\n"
        "s_cselect_b32 s88, s88, 67108864\n"
        "v_cvt_pk_bf16_f32 v0, v0, v1\n"
        "s_cselect_b32 s89, 0, 8\n"
        "v_cvt_pk_bf16_f32 v1, v2, v3\n"
        "v_min_u32 v0, -2, v0\n"
        "v_min_u32 v1, -2, v1\n"
        "buffer_store_dwordx2 v[0:1], v4, s[36:39], s88 offen sc1\n"
        "s_or_b32 s47, s47, s89\n"
        "s_cmp_lg_u32 s47, 0\n"
        "s_cbranch_scc1 fallback_73\n"
        "fbret_74:\n"
        "s_setprio 0\n"
        "v_and_b32 v37, 0x1f0, v53\n"
        "buffer_load_dwordx4 v[54:57], v37, s[32:35], s27 offen sc1\n"
        "s_waitcnt lgkmcnt(0)\n"
        "v_and_b32 v8, 12, v51\n"
        "v_lshl_add_u32 v8, v8, 2, s69\n"
        "ds_read_b128 v[40:43], v8\n"
        "ds_read_b128 v[44:47], v8 offset:256\n"
        "v_lshrrev_b32 v52, 2, v53\n"
        "v_lshlrev_b32 v152, 16, v58\n"
        "v_and_b32 v153, 0xffff0000, v58\n"
        "v_lshlrev_b32 v154, 16, v59\n"
        "v_and_b32 v155, 0xffff0000, v59\n"
        "v_lshlrev_b32 v156, 16, v60\n"
        "v_and_b32 v157, 0xffff0000, v60\n"
        "v_lshlrev_b32 v158, 16, v61\n"
        "v_and_b32 v159, 0xffff0000, v61\n"
        "v_add_u32 v58, s68, v53\n"
        "v_add_u32 v59, 92160, v53\n"
        "v_add_u32 v39, s25, v53\n"
        "s_add_u32 s40, s36, 88064\n"
        "s_addc_u32 s41, s37, 0\n"
        "s_sub_u32 s42, s38, 88064\n"
        "s_mov_b32 s43, s39\n"
        "v_add_u32 v60, s22, v53\n"
        "v_add_u32 v61, s23, v59\n"
        "v_lshl_add_u32 v38, v53, 2, s67\n"
        "v_add_u32 v50, 124928, v48\n"
        "s_sleep 8\n"
        "s_mov_b32 m0, s67\n"
        "s_nop 0\n"
        "buffer_load_dwordx4 v53, s[36:39], s20 offen offset:0 sc1 lds\n"
        "buffer_load_dwordx4 v53, s[36:39], s20 offen offset:1024 sc1 lds\n"
        "buffer_load_dwordx4 v53, s[36:39], s21 offen offset:2048 sc1 lds\n"
        "buffer_load_dwordx4 v53, s[36:39], s21 offen offset:3072 sc1 lds\n"
        "chk_78:\n"
        "s_waitcnt vmcnt(4)\n"
        "v_cmp_ne_u32 s[90:91], s45, v57\n"
        "v_cmp_ne_u32 vcc, s45, v55\n"
        "s_or_b64 vcc, vcc, s[90:91]\n"
        "s_cbranch_scc1 rau_80\n"
        "go_79:\n"
        "v_cvt_pk_bf16_f32 v55, v54, v56\n"
        "s_mov_b32 exec_lo, 0\n"
        "v_lshlrev_b32 v57, 16, v55\n"
        "v_and_b32 v55, 0xffff0000, v55\n"
        "v_sub_f32 v54, v54, v57\n"
        "v_sub_f32 v56, v56, v55\n"
        "v_cvt_pk_bf16_f32 v55, v54, v56\n"
        "s_mov_b32 exec_lo, -1\n"
        "v_mov_b32 v54, v55\n"
        "s_waitcnt vmcnt(0)\n"
        "ds_read_b32 v36, v38\n"
        "chk_81:\n"
        "s_waitcnt lgkmcnt(0)\n"
        "v_cmp_eq_u32 vcc, -1, v36\n"
        "s_cbranch_vccnz robl_83\n"
        "go_82:\n"
        "s_setprio 3\n"
        "s_waitcnt lgkmcnt(0)\n"
        "s_barrier\n"
        "ds_read_b128 v[0:3], v39\n"
        "ds_read_b128 v[4:7], v39 offset:1024\n"
        "ds_read_b128 v[8:11], v50 offset:0\n"
        "ds_read_b128 v[12:15], v50 offset:256\n"
        "ds_read_b128 v[16:19], v50 offset:2048\n"
        "ds_read_b128 v[20:23], v50 offset:2304\n"
        "ds_read_b128 v[24:27], v50 offset:4096\n"
        "ds_read_b128 v[28:31], v50 offset:4352\n"
        "s_waitcnt lgkmcnt(5)\n"
        "v_mfma_f32_16x16x32_bf16 v[0:3], v[64:67], v[8:11], v[0:3]\n"
        "ds_read_b128 v[8:11], v50 offset:6144\n"
        "s_waitcnt lgkmcnt(5)\n"
        "v_mfma_f32_16x16x32_bf16 v[4:7], v[64:67], v[12:15], v[4:7]\n"
        "ds_read_b128 v[12:15], v50 offset:6400\n"
        "s_waitcnt lgkmcnt(5)\n"
        "v_mfma_f32_16x16x32_bf16 v[0:3], v[68:71], v[16:19], v[0:3]\n"
        "ds_read_b128 v[16:19], v50 offset:8192\n"
        "s_waitcnt lgkmcnt(5)\n"
        "v_mfma_f32_16x16x32_bf16 v[4:7], v[68:71], v[20:23], v[4:7]\n"
        "ds_read_b128 v[20:23], v50 offset:8448\n"
        "s_waitcnt lgkmcnt(5)\n"
        "v_mfma_f32_16x16x32_bf16 v[0:3], v[72:75], v[24:27], v[0:3]\n"
        "ds_read_b128 v[24:27], v50 offset:10240\n"
        "s_waitcnt lgkmcnt(5)\n"
        "v_mfma_f32_16x16x32_bf16 v[4:7], v[72:75], v[28:31], v[4:7]\n"
        "ds_read_b128 v[28:31], v50 offset:10496\n"
        "s_waitcnt lgkmcnt(5)\n"
        "v_mfma_f32_16x16x32_bf16 v[0:3], v[76:79], v[8:11], v[0:3]\n"
        "ds_read_b128 v[8:11], v50 offset:12288\n"
        "s_waitcnt lgkmcnt(5)\n"
        "v_mfma_f32_16x16x32_bf16 v[4:7], v[76:79], v[12:15], v[4:7]\n"
        "ds_read_b128 v[12:15], v50 offset:12544\n"
        "s_waitcnt lgkmcnt(5)\n"
        "v_mfma_f32_16x16x32_bf16 v[0:3], v[80:83], v[16:19], v[0:3]\n"
        "ds_read_b128 v[16:19], v50 offset:14336\n"
        "s_waitcnt lgkmcnt(5)\n"
        "v_mfma_f32_16x16x32_bf16 v[4:7], v[80:83], v[20:23], v[4:7]\n"
        "ds_read_b128 v[20:23], v50 offset:14592\n"
        "s_waitcnt lgkmcnt(5)\n"
        "v_mfma_f32_16x16x32_bf16 v[0:3], v[84:87], v[24:27], v[0:3]\n"
        "s_waitcnt lgkmcnt(4)\n"
        "v_mfma_f32_16x16x32_bf16 v[4:7], v[84:87], v[28:31], v[4:7]\n"
        "s_waitcnt lgkmcnt(3)\n"
        "v_mfma_f32_16x16x32_bf16 v[0:3], v[88:91], v[8:11], v[0:3]\n"
        "s_waitcnt lgkmcnt(2)\n"
        "v_mfma_f32_16x16x32_bf16 v[4:7], v[88:91], v[12:15], v[4:7]\n"
        "s_waitcnt lgkmcnt(1)\n"
        "v_mfma_f32_16x16x32_bf16 v[0:3], v[92:95], v[16:19], v[0:3]\n"
        "s_waitcnt lgkmcnt(0)\n"
        "v_mfma_f32_16x16x32_bf16 v[4:7], v[92:95], v[20:23], v[4:7]\n"
        "s_nop 7\n"
        "v_cmp_u_f32 vcc, v0, v4\n"
        "s_cbranch_vccnz woslow_84\n"
        "go_85:\n"
        "s_nop 1\n"
        "v_fmac_f32 v152, v0, v40\n"
        "v_fmac_f32 v153, v1, v41\n"
        "v_fmac_f32 v154, v2, v42\n"
        "v_fmac_f32 v155, v3, v43\n"
        "v_mul_f32 v8, v152, v44\n"
        "v_mul_f32 v9, v153, v45\n"
        "v_mul_f32 v10, v154, v46\n"
        "v_mul_f32 v11, v155, v47\n"
        "v_cvt_pk_bf16_f32 v8, v8, v9\n"
        "v_cvt_pk_bf16_f32 v9, v10, v11\n"
        "v_min_u32 v8, -2, v8\n"
        "v_min_u32 v9, -2, v9\n"
        "buffer_store_dwordx2 v[8:9], v49, s[36:39], s65 offen offset:0 sc1\n"
        "v_fmac_f32 v156, v4, v40\n"
        "v_fmac_f32 v157, v5, v41\n"
        "v_fmac_f32 v158, v6, v42\n"
        "v_fmac_f32 v159, v7, v43\n"
        "v_mul_f32 v12, v156, v44\n"
        "v_mul_f32 v13, v157, v45\n"
        "v_mul_f32 v14, v158, v46\n"
        "v_mul_f32 v15, v159, v47\n"
        "v_cvt_pk_bf16_f32 v12, v12, v13\n"
        "v_cvt_pk_bf16_f32 v13, v14, v15\n"
        "v_min_u32 v12, -2, v12\n"
        "v_min_u32 v13, -2, v13\n"
        "buffer_store_dwordx2 v[12:13], v49, s[36:39], s65 offen offset:256 sc1\n"
        "v_mul_f32 v18, v152, v152\n"
        "v_add_f32 v16, v152, v153\n"
        "v_fmac_f32 v18, v153, v153\n"
        "v_add_f32 v16, v16, v154\n"
        "v_fmac_f32 v18, v154, v154\n"
        "v_add_f32 v16, v16, v155\n"
        "v_fmac_f32 v18, v155, v155\n"
        "v_mul_f32 v19, v156, v156\n"
        "v_add_f32 v17, v156, v157\n"
        "v_fmac_f32 v19, v157, v157\n"
        "v_add_f32 v17, v17, v158\n"
        "v_fmac_f32 v19, v158, v158\n"
        "v_add_f32 v17, v17, v159\n"
        "v_fmac_f32 v19, v159, v159\n"
        "v_permlane32_swap_b32 v16, v18\n"
        "s_nop 0\n"
        "v_permlane32_swap_b32 v17, v19\n"
        "v_add_f32 v16, v16, v18\n"
        "v_add_f32 v17, v17, v19\n"
        "s_nop 1\n"
        "v_permlane16_swap_b32 v16, v17\n"
        "v_add_f32 v16, v16, v17\n"
        "v_min_u32 v16, -2, v16\n"
        "buffer_store_dword v16, v52, s[36:39], s66 offen sc1\n"
        "s_setprio 0\n"
        "buffer_load_dwordx4 v[64:67], v53, s[32:35], s11 offen offset:2048 sc1\n"
        "buffer_load_dwordx4 v[68:71], v53, s[32:35], s15 offen offset:2048 sc1\n"
        "s_waitcnt lgkmcnt(0)\n"
        "s_barrier\n"
        "s_setprio 2\n"
        "ds_read_b128 v[16:19], v59 offset:24576\n"
        "ds_read_b128 v[20:23], v59 offset:25600\n"
        "ds_read_b128 v[24:27], v59 offset:26624\n"
        "s_waitcnt lgkmcnt(2)\n"
        "v_mfma_f32_32x32x16_bf16 v[32:47], v[160:163], v[16:19], 0\n"
        "ds_read_b128 v[16:19], v59 offset:27648\n"
        "s_waitcnt lgkmcnt(2)\n"
        "v_mfma_f32_32x32x16_bf16 v[32:47], v[164:167], v[20:23], v[32:47]\n"
        "ds_read_b128 v[20:23], v59 offset:28672\n"
        "s_waitcnt lgkmcnt(2)\n"
        "v_mfma_f32_32x32x16_bf16 v[32:47], v[168:171], v[24:27], v[32:47]\n"
        "ds_read_b128 v[24:27], v59 offset:29696\n"
        "s_waitcnt lgkmcnt(2)\n"
        "v_mfma_f32_32x32x16_bf16 v[32:47], v[172:175], v[16:19], v[32:47]\n"
        "ds_read_b128 v[16:19], v59 offset:30720\n"
        "s_waitcnt lgkmcnt(2)\n"
        "v_mfma_f32_32x32x16_bf16 v[32:47], v[176:179], v[20:23], v[32:47]\n"
        "ds_read_b128 v[20:23], v59 offset:31744\n"
        "s_waitcnt lgkmcnt(2)\n"
        "v_mfma_f32_32x32x16_bf16 v[32:47], v[180:183], v[24:27], v[32:47]\n"
        "ds_read_b128 v[24:27], v61 offset:0\n"
        "s_waitcnt lgkmcnt(2)\n"
        "v_mfma_f32_32x32x16_bf16 v[32:47], v[184:187], v[16:19], v[32:47]\n"
        "ds_read_b128 v[16:19], v61 offset:1024\n"
        "s_waitcnt lgkmcnt(2)\n"
        "v_mfma_f32_32x32x16_bf16 v[32:47], v[188:191], v[20:23], v[32:47]\n"
        "ds_read_b128 v[20:23], v61 offset:2048\n"
        "s_waitcnt lgkmcnt(2)\n"
        "v_mfma_f32_32x32x16_bf16 v[0:15], v[192:195], v[24:27], 0\n"
        "ds_read_b128 v[24:27], v61 offset:3072\n"
        "s_waitcnt lgkmcnt(2)\n"
        "v_mfma_f32_32x32x16_bf16 v[0:15], v[196:199], v[16:19], v[0:15]\n"
        "ds_read_b128 v[16:19], v61 offset:4096\n"
        "s_waitcnt lgkmcnt(2)\n"
        "v_mfma_f32_32x32x16_bf16 v[0:15], v[200:203], v[20:23], v[0:15]\n"
        "ds_read_b128 v[20:23], v61 offset:5120\n"
        "s_waitcnt lgkmcnt(2)\n"
        "v_mfma_f32_32x32x16_bf16 v[0:15], v[204:207], v[24:27], v[0:15]\n"
        "ds_read_b128 v[24:27], v61 offset:6144\n"
        "s_waitcnt lgkmcnt(2)\n"
        "v_mfma_f32_32x32x16_bf16 v[0:15], v[208:211], v[16:19], v[0:15]\n"
        "ds_read_b128 v[16:19], v61 offset:7168\n"
        "ds_write_b128 v58, v[32:35] offset:0\n"
        "s_waitcnt lgkmcnt(3)\n"
        "v_mfma_f32_32x32x16_bf16 v[0:15], v[212:215], v[20:23], v[0:15]\n"
        "ds_read_b128 v[20:23], v61 offset:8192\n"
        "s_waitcnt lgkmcnt(3)\n"
        "v_mfma_f32_32x32x16_bf16 v[0:15], v[216:219], v[24:27], v[0:15]\n"
        "ds_read_b128 v[24:27], v61 offset:9216\n"
        "ds_write_b128 v58, v[36:39] offset:1024\n"
        "s_waitcnt lgkmcnt(4)\n"
        "v_mfma_f32_32x32x16_bf16 v[0:15], v[220:223], v[16:19], v[0:15]\n"
        "ds_read_b128 v[16:19], v61 offset:10240\n"
        "s_waitcnt lgkmcnt(3)\n"
        "v_mfma_f32_32x32x16_bf16 v[0:15], v[224:227], v[20:23], v[0:15]\n"
        "ds_read_b128 v[20:23], v61 offset:11264\n"
        "ds_write_b128 v58, v[40:43] offset:2048\n"
        "s_waitcnt lgkmcnt(4)\n"
        "v_mfma_f32_32x32x16_bf16 v[0:15], v[228:231], v[24:27], v[0:15]\n"
        "ds_read_b128 v[24:27], v61 offset:12288\n"
        "s_waitcnt lgkmcnt(3)\n"
        "v_mfma_f32_32x32x16_bf16 v[0:15], v[232:235], v[16:19], v[0:15]\n"
        "ds_read_b128 v[16:19], v61 offset:13312\n"
        "ds_write_b128 v58, v[44:47] offset:3072\n"
        "s_waitcnt lgkmcnt(4)\n"
        "v_mfma_f32_32x32x16_bf16 v[0:15], v[236:239], v[20:23], v[0:15]\n"
        "ds_read_b128 v[20:23], v61 offset:14336\n"
        "s_waitcnt lgkmcnt(3)\n"
        "v_mfma_f32_32x32x16_bf16 v[0:15], v[240:243], v[24:27], v[0:15]\n"
        "ds_read_b128 v[24:27], v61 offset:15360\n"
        "ds_read2st64_b32 v[32:33], v52 offset0:8 offset1:9\n"
        "ds_read2st64_b32 v[34:35], v52 offset0:10 offset1:11\n"
        "ds_read2st64_b32 v[36:37], v52 offset0:12 offset1:13\n"
        "ds_read2st64_b32 v[38:39], v52 offset0:14 offset1:15\n"
        "s_waitcnt lgkmcnt(7)\n"
        "v_mfma_f32_32x32x16_bf16 v[0:15], v[244:247], v[16:19], v[0:15]\n"
        "s_waitcnt lgkmcnt(5)\n"
        "v_mfma_f32_32x32x16_bf16 v[0:15], v[248:251], v[20:23], v[0:15]\n"
        "s_waitcnt lgkmcnt(4)\n"
        "v_mfma_f32_32x32x16_bf16 v[0:15], v[252:255], v[24:27], v[0:15]\n"
        "s_bitcmp1_b32 s3, 0\n"
        "s_cbranch_scc1 odd_86\n"
        "s_nop 9\n"
        "ds_write_b128 v60, v[8:11] offset:0\n"
        "ds_write_b128 v60, v[12:15] offset:1024\n"
        "s_branch par_87\n"
        "odd_86:\n"
        "s_nop 9\n"
        "ds_write_b128 v60, v[0:3] offset:0\n"
        "ds_write_b128 v60, v[4:7] offset:1024\n"
        "par_87:\n"
        "v_xor_b32 v45, 0x800, v60\n"
        "s_setprio 0\n"
        "s_waitcnt lgkmcnt(4)\n"
        "v_add_f32 v32, v32, v34\n"
        "v_add_f32 v33, v33, v35\n"
        "s_waitcnt lgkmcnt(2)\n"
        "v_add_f32 v36, v36, v38\n"
        "v_add_f32 v37, v37, v39\n"
        "v_add_f32 v32, v32, v36\n"
        "v_add_f32 v33, v33, v37\n"
        "v_add_f32 v34, v32, v33\n"
        "v_add_f32 v33, v32, v33\n"
        "s_nop 1\n"
        "v_permlane32_swap_b32 v34, v33\n"
        "v_mul_f32 v32, 0xbb000000, v34\n"
        "v_mul_f32 v34, v32, v32\n"
        "v_mov_b32 v35, 0x3b000000\n"
        "v_fma_f32 v33, v33, v35, -v34\n"
        "v_max_f32 v33, 0, v33\n"
        "v_add_f32 v33, 0x3727c5ac, v33\n"
        "v_rsq_f32 v44, v33\n"
        "s_nop 0\n"
        "v_mul_f32 v33, v33, v44\n"
        "v_sub_f32 v44, 0, v44\n"
        "v_cvt_pk_bf16_f32 v40, v32, v33\n"
        "v_lshlrev_b32 v34, 16, v40\n"
        "v_and_b32 v35, 0xffff0000, v40\n"
        "v_sub_f32 v32, v32, v34\n"
        "v_sub_f32 v33, v33, v35\n"
        "v_cvt_pk_bf16_f32 v41, v32, v33\n"
        "s_setprio 3\n"
        "s_nop 0\n"
        "v_mfma_f32_32x32x8bf16_1k v[0:15], v[54:55], v[40:41], v[0:15]\n"
        "s_setprio 0\n"
        "s_waitcnt lgkmcnt(0)\n"
        "s_barrier\n"
        "ds_read_b128 v[16:19], v45 offset:0\n"
        "ds_read_b128 v[20:23], v45 offset:1024\n"
        "s_bitcmp1_b32 s3, 0\n"
        "s_cbranch_scc1 odd_88\n"
        "s_waitcnt lgkmcnt(1)\n"
        "s_nop 3\n"
        "v_add_f32 v0, v0, v16\n"
        "v_add_f32 v1, v1, v17\n"
        "v_add_f32 v2, v2, v18\n"
        "v_add_f32 v3, v3, v19\n"
        "s_waitcnt lgkmcnt(0)\n"
        "v_add_f32 v4, v4, v20\n"
        "v_add_f32 v5, v5, v21\n"
        "v_add_f32 v6, v6, v22\n"
        "v_add_f32 v7, v7, v23\n"
        "s_branch par_89\n"
        "odd_88:\n"
        "s_waitcnt lgkmcnt(1)\n"
        "s_nop 3\n"
        "v_add_f32 v0, v8, v16\n"
        "v_add_f32 v1, v9, v17\n"
        "v_add_f32 v2, v10, v18\n"
        "v_add_f32 v3, v11, v19\n"
        "s_waitcnt lgkmcnt(0)\n"
        "v_add_f32 v4, v12, v20\n"
        "v_add_f32 v5, v13, v21\n"
        "v_add_f32 v6, v14, v22\n"
        "v_add_f32 v7, v15, v23\n"
        "par_89:\n"
        "v_cmp_u_f32 vcc, v0, v0\n"
        "s_cbranch_vccnz f1slow_90\n"
        "go_91:\n"
        "v_mul_f32 v0, v0, v44\n"
        "v_mul_f32 v1, v1, v44\n"
        "v_mul_f32 v2, v2, v44\n"
        "v_mul_f32 v3, v3, v44\n"
        "v_mul_f32 v4, v4, v44\n"
        "v_mul_f32 v5, v5, v44\n"
        "buffer_load_dwordx4 v[72:75], v53, s[32:35], s11 offen offset:3072 sc1\n"
        "v_mul_f32 v6, v6, v44\n"
        "v_mul_f32 v7, v7, v44\n"
        "v_exp_f32 v20, v0\n"
        "v_exp_f32 v21, v1\n"
        "v_exp_f32 v22, v2\n"
        "v_exp_f32 v23, v3\n"
        "buffer_load_dwordx4 v[76:79], v53, s[32:35], s15 offen offset:3072 sc1\n"
        "v_exp_f32 v24, v4\n"
        "v_exp_f32 v25, v5\n"
        "v_exp_f32 v26, v6\n"
        "v_exp_f32 v27, v7\n"
        "v_add_f32 v20, 1.0, v20\n"
        "v_add_f32 v21, 1.0, v21\n"
        "v_add_f32 v22, 1.0, v22\n"
        "v_add_f32 v23, 1.0, v23\n"
        "v_add_f32 v24, 1.0, v24\n"
        "v_add_f32 v25, 1.0, v25\n"
        "v_add_f32 v26, 1.0, v26\n"
        "v_add_f32 v27, 1.0, v27\n"
        "v_rcp_f32 v20, v20\n"
        "v_rcp_f32 v21, v21\n"
        "v_rcp_f32 v22, v22\n"
        "v_rcp_f32 v23, v23\n"
        "v_rcp_f32 v24, v24\n"
        "v_rcp_f32 v25, v25\n"
        "v_rcp_f32 v26, v26\n"
        "v_rcp_f32 v27, v27\n"
        "v_mul_f32 v0, v0, v20\n"
        "v_mul_f32 v1, v1, v21\n"
        "v_mul_f32 v2, v2, v22\n"
        "v_mul_f32 v3, v3, v23\n"
        "v_mul_f32 v4, v4, v24\n"
        "v_mul_f32 v5, v5, v25\n"
        "v_mul_f32 v6, v6, v26\n"
        "v_mul_f32 v7, v7, v27\n"
        "v_cvt_pk_bf16_f32 v48, v0, v1\n"
        "v_cvt_pk_bf16_f32 v49, v2, v3\n"
        "v_cvt_pk_bf16_f32 v50, v4, v5\n"
        "v_cvt_pk_bf16_f32 v51, v6, v7\n"
        "v_min_u32 v48, -2, v48\n"
        "v_min_u32 v49, -2, v49\n"
        "v_min_u32 v50, -2, v50\n"
        "v_min_u32 v51, -2, v51\n"
        "buffer_store_dwordx4 v[48:51], v53, s[36:39], s58 offen offset:0 sc1\n"
        "s_barrier\n"
        "s_setprio 2\n"
        "ds_read_b128 v[16:19], v59 offset:57344\n"
        "ds_read_b128 v[20:23], v59 offset:58368\n"
        "ds_read_b128 v[24:27], v59 offset:59392\n"
        "s_waitcnt lgkmcnt(2)\n"
        "v_mfma_f32_32x32x16_bf16 v[32:47], v[160:163], v[16:19], 0\n"
        "ds_read_b128 v[16:19], v59 offset:60416\n"
        "s_waitcnt lgkmcnt(2)\n"
        "v_mfma_f32_32x32x16_bf16 v[32:47], v[164:167], v[20:23], v[32:47]\n"
        "ds_read_b128 v[20:23], v59 offset:61440\n"
        "buffer_load_dwordx4 v[80:83], v53, s[32:35], s12 offen offset:0 sc1\n"
        "s_waitcnt lgkmcnt(2)\n"
        "v_mfma_f32_32x32x16_bf16 v[32:47], v[168:171], v[24:27], v[32:47]\n"
        "ds_read_b128 v[24:27], v59 offset:62464\n"
        "s_waitcnt lgkmcnt(2)\n"
        "v_mfma_f32_32x32x16_bf16 v[32:47], v[172:175], v[16:19], v[32:47]\n"
        "ds_read_b128 v[16:19], v59 offset:63488\n"
        "buffer_load_dwordx4 v[84:87], v53, s[32:35], s16 offen offset:0 sc1\n"
        "s_waitcnt lgkmcnt(2)\n"
        "v_mfma_f32_32x32x16_bf16 v[32:47], v[176:179], v[20:23], v[32:47]\n"
        "ds_read_b128 v[20:23], v59 offset:64512\n"
        "s_waitcnt lgkmcnt(2)\n"
        "v_mfma_f32_32x32x16_bf16 v[32:47], v[180:183], v[24:27], v[32:47]\n"
        "ds_read_b128 v[24:27], v61 offset:32768\n"
        "buffer_load_dwordx4 v[88:91], v53, s[32:35], s12 offen offset:1024 sc1\n"
        "s_waitcnt lgkmcnt(2)\n"
        "v_mfma_f32_32x32x16_bf16 v[32:47], v[184:187], v[16:19], v[32:47]\n"
        "ds_read_b128 v[16:19], v61 offset:33792\n"
        "s_waitcnt lgkmcnt(2)\n"
        "v_mfma_f32_32x32x16_bf16 v[32:47], v[188:191], v[20:23], v[32:47]\n"
        "ds_read_b128 v[20:23], v61 offset:34816\n"
        "buffer_load_dwordx4 v[92:95], v53, s[32:35], s16 offen offset:1024 sc1\n"
        "s_waitcnt lgkmcnt(2)\n"
        "v_mfma_f32_32x32x16_bf16 v[0:15], v[192:195], v[24:27], 0\n"
        "ds_read_b128 v[24:27], v61 offset:35840\n"
        "s_waitcnt lgkmcnt(2)\n"
        "v_mfma_f32_32x32x16_bf16 v[0:15], v[196:199], v[16:19], v[0:15]\n"
        "ds_read_b128 v[16:19], v61 offset:36864\n"
        "s_waitcnt lgkmcnt(2)\n"
        "v_mfma_f32_32x32x16_bf16 v[0:15], v[200:203], v[20:23], v[0:15]\n"
        "ds_read_b128 v[20:23], v61 offset:37888\n"
        "s_waitcnt lgkmcnt(2)\n"
        "v_mfma_f32_32x32x16_bf16 v[0:15], v[204:207], v[24:27], v[0:15]\n"
        "ds_read_b128 v[24:27], v61 offset:38912\n"
        "s_waitcnt lgkmcnt(2)\n"
        "v_mfma_f32_32x32x16_bf16 v[0:15], v[208:211], v[16:19], v[0:15]\n"
        "ds_read_b128 v[16:19], v61 offset:39936\n"
        "ds_write_b128 v58, v[32:35] offset:32768\n"
        "s_waitcnt lgkmcnt(3)\n"
        "v_mfma_f32_32x32x16_bf16 v[0:15], v[212:215], v[20:23], v[0:15]\n"
        "ds_read_b128 v[20:23], v61 offset:40960\n"
        "s_waitcnt lgkmcnt(3)\n"
        "v_mfma_f32_32x32x16_bf16 v[0:15], v[216:219], v[24:27], v[0:15]\n"
        "ds_read_b128 v[24:27], v61 offset:41984\n"
        "ds_write_b128 v58, v[36:39] offset:33792\n"
        "s_waitcnt lgkmcnt(4)\n"
        "v_mfma_f32_32x32x16_bf16 v[0:15], v[220:223], v[16:19], v[0:15]\n"
        "ds_read_b128 v[16:19], v61 offset:43008\n"
        "s_waitcnt lgkmcnt(3)\n"
        "v_mfma_f32_32x32x16_bf16 v[0:15], v[224:227], v[20:23], v[0:15]\n"
        "ds_read_b128 v[20:23], v61 offset:44032\n"
        "ds_write_b128 v58, v[40:43] offset:34816\n"
        "s_waitcnt lgkmcnt(4)\n"
        "v_mfma_f32_32x32x16_bf16 v[0:15], v[228:231], v[24:27], v[0:15]\n"
        "ds_read_b128 v[24:27], v61 offset:45056\n"
        "s_waitcnt lgkmcnt(3)\n"
        "v_mfma_f32_32x32x16_bf16 v[0:15], v[232:235], v[16:19], v[0:15]\n"
        "ds_read_b128 v[16:19], v61 offset:46080\n"
        "ds_write_b128 v58, v[44:47] offset:35840\n"
        "s_waitcnt lgkmcnt(4)\n"
        "v_mfma_f32_32x32x16_bf16 v[0:15], v[236:239], v[20:23], v[0:15]\n"
        "ds_read_b128 v[20:23], v61 offset:47104\n"
        "s_waitcnt lgkmcnt(3)\n"
        "v_mfma_f32_32x32x16_bf16 v[0:15], v[240:243], v[24:27], v[0:15]\n"
        "ds_read_b128 v[24:27], v61 offset:48128\n"
        "ds_read2st64_b32 v[32:33], v52 offset0:16 offset1:17\n"
        "ds_read2st64_b32 v[34:35], v52 offset0:18 offset1:19\n"
        "ds_read2st64_b32 v[36:37], v52 offset0:20 offset1:21\n"
        "ds_read2st64_b32 v[38:39], v52 offset0:22 offset1:23\n"
        "s_waitcnt lgkmcnt(7)\n"
        "v_mfma_f32_32x32x16_bf16 v[0:15], v[244:247], v[16:19], v[0:15]\n"
        "s_waitcnt lgkmcnt(5)\n"
        "v_mfma_f32_32x32x16_bf16 v[0:15], v[248:251], v[20:23], v[0:15]\n"
        "s_waitcnt lgkmcnt(4)\n"
        "v_mfma_f32_32x32x16_bf16 v[0:15], v[252:255], v[24:27], v[0:15]\n"
        "s_bitcmp1_b32 s3, 0\n"
        "s_cbranch_scc1 odd_92\n"
        "s_nop 9\n"
        "ds_write_b128 v60, v[8:11] offset:32768\n"
        "ds_write_b128 v60, v[12:15] offset:33792\n"
        "s_branch par_93\n"
        "odd_92:\n"
        "s_nop 9\n"
        "ds_write_b128 v60, v[0:3] offset:32768\n"
        "ds_write_b128 v60, v[4:7] offset:33792\n"
        "par_93:\n"
        "v_xor_b32 v45, 0x800, v60\n"
        "s_setprio 2\n"
        "s_waitcnt lgkmcnt(4)\n"
        "v_add_f32 v32, v32, v34\n"
        "v_add_f32 v33, v33, v35\n"
        "s_waitcnt lgkmcnt(2)\n"
        "v_add_f32 v36, v36, v38\n"
        "v_add_f32 v37, v37, v39\n"
        "v_add_f32 v32, v32, v36\n"
        "buffer_load_dwordx4 v[164:167], v53, s[36:39], s48 offen offset:0 sc1\n"
        "v_add_f32 v33, v33, v37\n"
        "v_add_f32 v34, v32, v33\n"
        "v_add_f32 v33, v32, v33\n"
        "s_nop 1\n"
        "v_permlane32_swap_b32 v34, v33\n"
        "v_mul_f32 v32, 0xbb000000, v34\n"
        "buffer_load_dwordx4 v[168:171], v53, s[36:39], s49 offen offset:0 sc1\n"
        "v_mul_f32 v34, v32, v32\n"
        "v_mov_b32 v35, 0x3b000000\n"
        "v_fma_f32 v33, v33, v35, -v34\n"
        "v_max_f32 v33, 0, v33\n"
        "v_add_f32 v33, 0x3727c5ac, v33\n"
        "buffer_load_dwordx4 v[172:175], v53, s[36:39], s50 offen offset:0 sc1\n"
        "v_rsq_f32 v44, v33\n"
        "s_nop 0\n"
        "v_mul_f32 v33, v33, v44\n"
        "v_sub_f32 v44, 0, v44\n"
        "v_cvt_pk_bf16_f32 v40, v32, v33\n"
        "v_lshlrev_b32 v34, 16, v40\n"
        "buffer_load_dwordx4 v[176:179], v53, s[36:39], s51 offen offset:0 sc1\n"
        "v_and_b32 v35, 0xffff0000, v40\n"
        "v_sub_f32 v32, v32, v34\n"
        "v_sub_f32 v33, v33, v35\n"
        "v_cvt_pk_bf16_f32 v41, v32, v33\n"
        "s_setprio 3\n"
        "s_nop 0\n"
        "v_mfma_f32_32x32x8bf16_1k v[0:15], v[54:55], v[40:41], v[0:15]\n"
        "s_setprio 2\n"
        "s_waitcnt lgkmcnt(0)\n"
        "s_barrier\n"
        "ds_read_b128 v[16:19], v45 offset:32768\n"
        "ds_read_b128 v[20:23], v45 offset:33792\n"
        "s_bitcmp1_b32 s3, 0\n"
        "s_cbranch_scc1 odd_94\n"
        "s_waitcnt lgkmcnt(1)\n"
        "s_nop 3\n"
        "v_add_f32 v0, v0, v16\n"
        "v_add_f32 v1, v1, v17\n"
        "v_add_f32 v2, v2, v18\n"
        "v_add_f32 v3, v3, v19\n"
        "s_waitcnt lgkmcnt(0)\n"
        "v_add_f32 v4, v4, v20\n"
        "v_add_f32 v5, v5, v21\n"
        "v_add_f32 v6, v6, v22\n"
        "v_add_f32 v7, v7, v23\n"
        "s_branch par_95\n"
        "odd_94:\n"
        "s_waitcnt lgkmcnt(1)\n"
        "s_nop 3\n"
        "v_add_f32 v0, v8, v16\n"
        "v_add_f32 v1, v9, v17\n"
        "v_add_f32 v2, v10, v18\n"
        "v_add_f32 v3, v11, v19\n"
        "s_waitcnt lgkmcnt(0)\n"
        "v_add_f32 v4, v12, v20\n"
        "v_add_f32 v5, v13, v21\n"
        "v_add_f32 v6, v14, v22\n"
        "v_add_f32 v7, v15, v23\n"
        "par_95:\n"
        "v_cmp_u_f32 vcc, v0, v0\n"
        "s_cbranch_vccnz f1slow_96\n"
        "go_97:\n"
        "v_mul_f32 v0, v0, v44\n"
        "v_mul_f32 v1, v1, v44\n"
        "v_mul_f32 v2, v2, v44\n"
        "v_mul_f32 v3, v3, v44\n"
        "v_mul_f32 v4, v4, v44\n"
        "v_mul_f32 v5, v5, v44\n"
        "v_mul_f32 v6, v6, v44\n"
        "v_mul_f32 v7, v7, v44\n"
        "v_exp_f32 v20, v0\n"
        "v_exp_f32 v21, v1\n"
        "v_exp_f32 v22, v2\n"
        "v_exp_f32 v23, v3\n"
        "v_exp_f32 v24, v4\n"
        "v_exp_f32 v25, v5\n"
        "v_exp_f32 v26, v6\n"
        "v_exp_f32 v27, v7\n"
        "v_add_f32 v20, 1.0, v20\n"
        "v_add_f32 v21, 1.0, v21\n"
        "v_add_f32 v22, 1.0, v22\n"
        "v_add_f32 v23, 1.0, v23\n"
        "v_add_f32 v24, 1.0, v24\n"
        "v_add_f32 v25, 1.0, v25\n"
        "v_add_f32 v26, 1.0, v26\n"
        "v_add_f32 v27, 1.0, v27\n"
        "page6:\n"
        "s_cbranch_execz warm_back\n"
        "v_rcp_f32 v20, v20\n"
        "v_rcp_f32 v21, v21\n"
        "v_rcp_f32 v22, v22\n"
        "v_rcp_f32 v23, v23\n"
        "v_rcp_f32 v24, v24\n"
        "v_rcp_f32 v25, v25\n"
        "v_rcp_f32 v26, v26\n"
        "v_rcp_f32 v27, v27\n"
        "v_mul_f32 v0, v0, v20\n"
        "v_mul_f32 v1, v1, v21\n"
        "v_mul_f32 v2, v2, v22\n"
        "v_mul_f32 v3, v3, v23\n"
        "v_mul_f32 v4, v4, v24\n"
        "v_mul_f32 v5, v5, v25\n"
        "v_mul_f32 v6, v6, v26\n"
        "v_mul_f32 v7, v7, v27\n"
        "v_cvt_pk_bf16_f32 v204, v0, v1\n"
        "v_cvt_pk_bf16_f32 v205, v2, v3\n"
        "v_cvt_pk_bf16_f32 v206, v4, v5\n"
        "v_cvt_pk_bf16_f32 v207, v6, v7\n"
        "v_min_u32 v204, -2, v204\n"
        "v_min_u32 v205, -2, v205\n"
        "v_min_u32 v206, -2, v206\n"
        "v_min_u32 v207, -2, v207\n"
        "buffer_store_dwordx4 v[204:207], v53, s[36:39], s59 offen offset:0 sc1\n"
        "s_setprio 0\n"
        "buffer_load_dwordx4 v[180:183], v53, s[36:39], s52 offen offset:0 sc1\n"
        "buffer_load_dwordx4 v[184:187], v53, s[36:39], s53 offen offset:0 sc1\n"
        "buffer_load_dwordx4 v[188:191], v53, s[36:39], s54 offen offset:0 sc1\n"
        "buffer_load_dwordx4 v[192:195], v53, s[36:39], s55 offen offset:0 sc1\n"
        "buffer_load_dwordx4 v[196:199], v53, s[36:39], s56 offen offset:0 sc1\n"
        "buffer_load_dwordx4 v[200:203], v53, s[36:39], s57 offen offset:0 sc1\n"
        "v_add_u32 v54, s70, v53\n"
        "v_add_u32 v55, 65536, v54\n"
        "s_add_u32 s88, s71, 139264\n"
        "v_add_u32 v59, s88, v53\n"
        "v_lshrrev_b32 v32, 8, v53\n"
        "v_and_b32 v33, 0xf0, v53\n"
        "v_lshl_or_b32 v56, v32, 9, v33\n"
        "s_add_u32 s88, s71, 8192\n"
        "v_add_u32 v56, s88, v56\n"
        "v_lshlrev_b32 v33, 6, v33\n"
        "v_lshl_or_b32 v58, v32, 3, v33\n"
        "v_lshl_add_u32 v32, v32, 4, s69\n"
        "ds_read_b128 v[248:251], v32 offset:512\n"
        "s_sub_u32 s90, s84, 32768\n"
        "v_add_u32 v57, 65536, v56\n"
        "s_barrier\n"
        "s_setprio 2\n"
        "v_mfma_f32_32x32x16_bf16 v[0:15], v[96:99], v[48:51], 0\n"
        "v_mfma_f32_32x32x16_bf16 v[16:31], v[112:115], v[48:51], 0\n"
        "s_waitcnt vmcnt(10)\n"
        "v_mfma_f32_32x32x16_bf16 v[0:15], v[100:103], v[164:167], v[0:15]\n"
        "v_max3_u32 v36, v164, v165, v166\n"
        "v_max_u32 v36, v36, v167\n"
        "v_mfma_f32_32x32x16_bf16 v[16:31], v[116:119], v[164:167], v[16:31]\n"
        "s_waitcnt vmcnt(9)\n"
        "v_mfma_f32_32x32x16_bf16 v[0:15], v[104:107], v[168:171], v[0:15]\n"
        "v_max3_u32 v36, v36, v168, v169\n"
        "v_max3_u32 v36, v36, v170, v171\n"
        "v_mfma_f32_32x32x16_bf16 v[16:31], v[120:123], v[168:171], v[16:31]\n"
        "s_waitcnt vmcnt(8)\n"
        "v_mfma_f32_32x32x16_bf16 v[0:15], v[108:111], v[172:175], v[0:15]\n"
        "v_max3_u32 v36, v36, v172, v173\n"
        "v_max3_u32 v36, v36, v174, v175\n"
        "v_mfma_f32_32x32x16_bf16 v[16:31], v[124:127], v[172:175], v[16:31]\n"
        "s_waitcnt vmcnt(7)\n"
        "v_mfma_f32_32x32x16_bf16 v[0:15], v[128:131], v[176:179], v[0:15]\n"
        "v_max3_u32 v36, v36, v176, v177\n"
        "v_max3_u32 v36, v36, v178, v179\n"
        "v_mfma_f32_32x32x16_bf16 v[16:31], v[132:135], v[176:179], v[16:31]\n"
        "buffer_load_dwordx4 v[208:211], v53, s[40:43], s48 offen offset:0 sc1\n"
        "s_waitcnt vmcnt(6)\n"
        "v_mfma_f32_32x32x16_bf16 v[0:15], v[136:139], v[180:183], v[0:15]\n"
        "v_max3_u32 v36, v36, v180, v181\n"
        "v_max3_u32 v36, v36, v182, v183\n"
        "v_mfma_f32_32x32x16_bf16 v[16:31], v[140:143], v[180:183], v[16:31]\n"
        "s_waitcnt vmcnt(5)\n"
        "v_mfma_f32_32x32x16_bf16 v[0:15], v[144:147], v[184:187], v[0:15]\n"
        "v_max3_u32 v36, v36, v184, v185\n"
        "v_max3_u32 v36, v36, v186, v187\n"
        "v_mfma_f32_32x32x16_bf16 v[16:31], v[148:151], v[184:187], v[16:31]\n"
        "buffer_load_dwordx4 v[212:215], v53, s[40:43], s49 offen offset:0 sc1\n"
        "s_waitcnt vmcnt(5)\n"
        "v_mfma_f32_32x32x16_bf16 v[0:15], v[64:67], v[188:191], v[0:15]\n"
        "v_max3_u32 v36, v36, v188, v189\n"
        "v_max3_u32 v36, v36, v190, v191\n"
        "v_mfma_f32_32x32x16_bf16 v[16:31], v[68:71], v[188:191], v[16:31]\n"
        "s_waitcnt vmcnt(4)\n"
        "v_mfma_f32_32x32x16_bf16 v[0:15], v[72:75], v[192:195], v[0:15]\n"
        "v_max3_u32 v36, v36, v192, v193\n"
        "v_max3_u32 v36, v36, v194, v195\n"
        "v_mfma_f32_32x32x16_bf16 v[16:31], v[76:79], v[192:195], v[16:31]\n"
        "buffer_load_dwordx4 v[216:219], v53, s[40:43], s50 offen offset:0 sc1\n"
        "s_waitcnt vmcnt(4)\n"
        "v_mfma_f32_32x32x16_bf16 v[0:15], v[80:83], v[196:199], v[0:15]\n"
        "v_max3_u32 v36, v36, v196, v197\n"
        "v_max3_u32 v36, v36, v198, v199\n"
        "v_mfma_f32_32x32x16_bf16 v[16:31], v[84:87], v[196:199], v[16:31]\n"
        "s_waitcnt vmcnt(3)\n"
        "v_mfma_f32_32x32x16_bf16 v[0:15], v[88:91], v[200:203], v[0:15]\n"
        "v_max3_u32 v36, v36, v200, v201\n"
        "v_max3_u32 v36, v36, v202, v203\n"
        "v_mfma_f32_32x32x16_bf16 v[16:31], v[92:95], v[200:203], v[16:31]\n"
        "buffer_load_dwordx4 v[220:223], v53, s[40:43], s51 offen offset:0 sc1\n"
        "chk_98:\n"
        "v_cmp_eq_u32 vcc, -1, v36\n"
        "s_cbranch_vccnz rh0_100\n"
        "go_99:\n"
        "s_setprio 0\n"
        "buffer_load_dwordx4 v[224:227], v53, s[40:43], s52 offen offset:0 sc1\n"
        "buffer_load_dwordx4 v[228:231], v53, s[40:43], s53 offen offset:0 sc1\n"
        "buffer_load_dwordx4 v[232:235], v53, s[40:43], s54 offen offset:0 sc1\n"
        "buffer_load_dwordx4 v[236:239], v53, s[40:43], s55 offen offset:0 sc1\n"
        "buffer_load_dwordx4 v[240:243], v53, s[40:43], s56 offen offset:0 sc1\n"
        "buffer_load_dwordx4 v[244:247], v53, s[40:43], s57 offen offset:0 sc1\n"
        "v_mfma_f32_32x32x16_bf16 v[160:175], v[96:99], v[204:207], 0\n"
        "v_mfma_f32_32x32x16_bf16 v[176:191], v[112:115], v[204:207], 0\n"
        "ds_write_b128 v54, v[0:3] offset:0\n"
        "s_waitcnt vmcnt(9)\n"
        "v_mfma_f32_32x32x16_bf16 v[160:175], v[100:103], v[208:211], v[160:175]\n"
        "v_max3_u32 v36, v208, v209, v210\n"
        "v_max_u32 v36, v36, v211\n"
        "v_mfma_f32_32x32x16_bf16 v[176:191], v[116:119], v[208:211], v[176:191]\n"
        "ds_write_b128 v54, v[4:7] offset:1024\n"
        "s_waitcnt vmcnt(8)\n"
        "v_mfma_f32_32x32x16_bf16 v[160:175], v[104:107], v[212:215], v[160:175]\n"
        "v_max3_u32 v36, v36, v212, v213\n"
        "v_max3_u32 v36, v36, v214, v215\n"
        "v_mfma_f32_32x32x16_bf16 v[176:191], v[120:123], v[212:215], v[176:191]\n"
        "ds_write_b128 v54, v[8:11] offset:2048\n"
        "s_waitcnt vmcnt(7)\n"
        "v_mfma_f32_32x32x16_bf16 v[160:175], v[108:111], v[216:219], v[160:175]\n"
        "v_max3_u32 v36, v36, v216, v217\n"
        "v_max3_u32 v36, v36, v218, v219\n"
        "v_mfma_f32_32x32x16_bf16 v[176:191], v[124:127], v[216:219], v[176:191]\n"
        "ds_write_b128 v54, v[12:15] offset:3072\n"
        "s_waitcnt vmcnt(6)\n"
        "v_mfma_f32_32x32x16_bf16 v[160:175], v[128:131], v[220:223], v[160:175]\n"
        "v_max3_u32 v36, v36, v220, v221\n"
        "v_max3_u32 v36, v36, v222, v223\n"
        "v_mfma_f32_32x32x16_bf16 v[176:191], v[132:135], v[220:223], v[176:191]\n"
        "ds_write_b128 v54, v[16:19] offset:4096\n"
        "s_waitcnt vmcnt(5)\n"
        "v_mfma_f32_32x32x16_bf16 v[160:175], v[136:139], v[224:227], v[160:175]\n"
        "v_max3_u32 v36, v36, v224, v225\n"
        "v_max3_u32 v36, v36, v226, v227\n"
        "v_mfma_f32_32x32x16_bf16 v[176:191], v[140:143], v[224:227], v[176:191]\n"
        "ds_write_b128 v54, v[20:23] offset:5120\n"
        "s_waitcnt vmcnt(4)\n"
        "v_mfma_f32_32x32x16_bf16 v[160:175], v[144:147], v[228:231], v[160:175]\n"
        "v_max3_u32 v36, v36, v228, v229\n"
        "v_max3_u32 v36, v36, v230, v231\n"
        "v_mfma_f32_32x32x16_bf16 v[176:191], v[148:151], v[228:231], v[176:191]\n"
        "ds_write_b128 v54, v[24:27] offset:6144\n"
        "s_waitcnt vmcnt(3)\n"
        "v_mfma_f32_32x32x16_bf16 v[160:175], v[64:67], v[232:235], v[160:175]\n"
        "v_max3_u32 v36, v36, v232, v233\n"
        "v_max3_u32 v36, v36, v234, v235\n"
        "v_mfma_f32_32x32x16_bf16 v[176:191], v[68:71], v[232:235], v[176:191]\n"
        "ds_write_b128 v54, v[28:31] offset:7168\n"
        "s_waitcnt vmcnt(2)\n"
        "v_mfma_f32_32x32x16_bf16 v[160:175], v[72:75], v[236:239], v[160:175]\n"
        "v_max3_u32 v36, v36, v236, v237\n"
        "v_max3_u32 v36, v36, v238, v239\n"
        "v_mfma_f32_32x32x16_bf16 v[176:191], v[76:79], v[236:239], v[176:191]\n"
        "s_waitcnt vmcnt(1)\n"
        "v_mfma_f32_32x32x16_bf16 v[160:175], v[80:83], v[240:243], v[160:175]\n"
        "v_max3_u32 v36, v36, v240, v241\n"
        "v_max3_u32 v36, v36, v242, v243\n"
        "v_mfma_f32_32x32x16_bf16 v[176:191], v[84:87], v[240:243], v[176:191]\n"
        "s_waitcnt vmcnt(0)\n"
        "v_mfma_f32_32x32x16_bf16 v[160:175], v[88:91], v[244:247], v[160:175]\n"
        "v_max3_u32 v36, v36, v244, v245\n"
        "v_max3_u32 v36, v36, v246, v247\n"
        "v_mfma_f32_32x32x16_bf16 v[176:191], v[92:95], v[244:247], v[176:191]\n"
        "chk_101:\n"
        "v_cmp_eq_u32 vcc, -1, v36\n"
        "s_cbranch_vccnz rh1_103\n"
        "go_102:\n"
        "s_nop 6\n"
        "ds_write_b128 v55, v[160:163] offset:0\n"
        "ds_write_b128 v55, v[164:167] offset:1024\n"
        "ds_write_b128 v55, v[168:171] offset:2048\n"
        "ds_write_b128 v55, v[172:175] offset:3072\n"
        "ds_write_b128 v55, v[176:179] offset:4096\n"
        "ds_write_b128 v55, v[180:183] offset:5120\n"
        "ds_write_b128 v55, v[184:187] offset:6144\n"
        "ds_write_b128 v55, v[188:191] offset:7168\n"
        "s_waitcnt lgkmcnt(0)\n"
        "s_barrier\n"
        "s_setprio 2\n"
        "ds_read_b128 v[32:35], v59\n"
        "ds_read_b128 v[64:67], v57 offset:0\n"
        "ds_read_b128 v[68:71], v57 offset:8192\n"
        "ds_read_b128 v[72:75], v57 offset:16384\n"
        "ds_read_b128 v[76:79], v57 offset:24576\n"
        "ds_read_b128 v[96:99], v57 offset:256\n"
        "ds_read_b128 v[100:103], v57 offset:8448\n"
        "ds_read_b128 v[104:107], v57 offset:16640\n"
        "ds_read_b128 v[108:111], v57 offset:24832\n"
        "ds_read_b128 v[0:3], v56 offset:0\n"
        "ds_read_b128 v[4:7], v56 offset:8192\n"
        "ds_read_b128 v[8:11], v56 offset:16384\n"
        "ds_read_b128 v[12:15], v56 offset:24576\n"
        "ds_read_b128 v[16:19], v56 offset:32768\n"
        "ds_read_b128 v[20:23], v56 offset:40960\n"
        "ds_read_b128 v[24:27], v56 offset:49152\n"
        "ds_read_b128 v[28:31], v56 offset:57344\n"
        "s_waitcnt lgkmcnt(14)\n"
        "v_add_f32 v64, v64, v68\n"
        "v_add_f32 v65, v65, v69\n"
        "v_add_f32 v66, v66, v70\n"
        "v_add_f32 v67, v67, v71\n"
        "s_waitcnt lgkmcnt(12)\n"
        "v_add_f32 v72, v72, v76\n"
        "v_add_f32 v73, v73, v77\n"
        "v_add_f32 v74, v74, v78\n"
        "v_add_f32 v75, v75, v79\n"
        "v_add_f32 v64, v64, v72\n"
        "v_add_f32 v65, v65, v73\n"
        "v_add_f32 v66, v66, v74\n"
        "v_add_f32 v67, v67, v75\n"
        "s_waitcnt lgkmcnt(10)\n"
        "v_add_f32 v96, v96, v100\n"
        "v_add_f32 v97, v97, v101\n"
        "v_add_f32 v98, v98, v102\n"
        "v_add_f32 v99, v99, v103\n"
        "s_waitcnt lgkmcnt(8)\n"
        "v_add_f32 v104, v104, v108\n"
        "v_add_f32 v105, v105, v109\n"
        "v_add_f32 v106, v106, v110\n"
        "v_add_f32 v107, v107, v111\n"
        "v_add_f32 v96, v96, v104\n"
        "v_add_f32 v97, v97, v105\n"
        "v_add_f32 v98, v98, v106\n"
        "v_add_f32 v99, v99, v107\n"
        "s_waitcnt lgkmcnt(6)\n"
        "v_add_f32 v0, v0, v4\n"
        "v_add_f32 v1, v1, v5\n"
        "v_add_f32 v2, v2, v6\n"
        "v_add_f32 v3, v3, v7\n"
        "s_waitcnt lgkmcnt(4)\n"
        "v_add_f32 v8, v8, v12\n"
        "v_add_f32 v9, v9, v13\n"
        "v_add_f32 v10, v10, v14\n"
        "v_add_f32 v11, v11, v15\n"
        "v_add_f32 v0, v0, v8\n"
        "v_add_f32 v1, v1, v9\n"
        "v_add_f32 v2, v2, v10\n"
        "v_add_f32 v3, v3, v11\n"
        "s_waitcnt lgkmcnt(2)\n"
        "v_add_f32 v16, v16, v20\n"
        "v_add_f32 v17, v17, v21\n"
        "v_add_f32 v18, v18, v22\n"
        "v_add_f32 v19, v19, v23\n"
        "s_waitcnt lgkmcnt(0)\n"
        "v_add_f32 v24, v24, v28\n"
        "v_add_f32 v25, v25, v29\n"
        "v_add_f32 v26, v26, v30\n"
        "v_add_f32 v27, v27, v31\n"
        "v_add_f32 v16, v16, v24\n"
        "v_add_f32 v17, v17, v25\n"
        "v_add_f32 v18, v18, v26\n"
        "v_add_f32 v19, v19, v27\n"
        "v_add_f32 v0, v0, v16\n"
        "v_add_f32 v1, v1, v17\n"
        "v_add_f32 v2, v2, v18\n"
        "v_add_f32 v3, v3, v19\n"
        "v_fmac_f32 v32, v0, v248\n"
        "v_fmac_f32 v33, v1, v249\n"
        "v_fmac_f32 v34, v2, v250\n"
        "v_fmac_f32 v35, v3, v251\n"
        "v_cvt_pk_bf16_f32 v0, v32, v33\n"
        "v_cvt_pk_bf16_f32 v1, v34, v35\n"
        "buffer_store_dwordx2 v[0:1], v58, s[28:31], s90 offen\n"
        "s_barrier\n"
        "s_setprio 1\n"
        "ds_read_b128 v[80:83], v57 offset:32768\n"
        "ds_read_b128 v[84:87], v57 offset:40960\n"
        "ds_read_b128 v[88:91], v57 offset:49152\n"
        "ds_read_b128 v[92:95], v57 offset:57344\n"
        "ds_read_b128 v[112:115], v57 offset:33024\n"
        "ds_read_b128 v[116:119], v57 offset:41216\n"
        "ds_read_b128 v[120:123], v57 offset:49408\n"
        "ds_read_b128 v[124:127], v57 offset:57600\n"
        "s_waitcnt lgkmcnt(6)\n"
        "v_pk_add_f32 v[80:81], v[80:81], v[84:85]\n"
        "v_pk_add_f32 v[82:83], v[82:83], v[86:87]\n"
        "s_waitcnt lgkmcnt(4)\n"
        "v_pk_add_f32 v[88:89], v[88:89], v[92:93]\n"
        "v_pk_add_f32 v[90:91], v[90:91], v[94:95]\n"
        "v_pk_add_f32 v[80:81], v[80:81], v[88:89]\n"
        "v_pk_add_f32 v[82:83], v[82:83], v[90:91]\n"
        "v_pk_add_f32 v[64:65], v[64:65], v[80:81]\n"
        "v_pk_add_f32 v[66:67], v[66:67], v[82:83]\n"
        "v_fmac_f32 v152, v64, v248\n"
        "v_fmac_f32 v153, v65, v249\n"
        "v_fmac_f32 v154, v66, v250\n"
        "v_fmac_f32 v155, v67, v251\n"
        "v_cvt_pk_bf16_f32 v64, v152, v153\n"
        "v_cvt_pk_bf16_f32 v65, v154, v155\n"
        "buffer_store_dwordx2 v[64:65], v58, s[28:31], s84 offen\n"
        "s_waitcnt lgkmcnt(2)\n"
        "v_pk_add_f32 v[112:113], v[112:113], v[116:117]\n"
        "v_pk_add_f32 v[114:115], v[114:115], v[118:119]\n"
        "s_waitcnt lgkmcnt(0)\n"
        "v_pk_add_f32 v[120:121], v[120:121], v[124:125]\n"
        "v_pk_add_f32 v[122:123], v[122:123], v[126:127]\n"
        "v_pk_add_f32 v[112:113], v[112:113], v[120:121]\n"
        "v_pk_add_f32 v[114:115], v[114:115], v[122:123]\n"
        "v_pk_add_f32 v[96:97], v[96:97], v[112:113]\n"
        "v_pk_add_f32 v[98:99], v[98:99], v[114:115]\n"
        "v_fmac_f32 v156, v96, v248\n"
        "v_fmac_f32 v157, v97, v249\n"
        "v_fmac_f32 v158, v98, v250\n"
        "v_fmac_f32 v159, v99, v251\n"
        "v_cvt_pk_bf16_f32 v96, v156, v157\n"
        "v_cvt_pk_bf16_f32 v97, v158, v159\n"
        "buffer_store_dwordx2 v[96:97], v58, s[28:31], s85 offen\n"
        "s_branch end_72\n"
        "tail_6:\n"
        "s_cbranch_execz warm_back\n"
        "s_setprio 0\n"
        "v_exp_f32 v0, v0\n"
        "v_exp_f32 v1, v1\n"
        "v_smfmac_f32_32x32x32_bf16 v[32:47], v[108:111], v[24:31], v52\n"
        "v_exp_f32 v2, v2\n"
        "v_exp_f32 v3, v3\n"
        "v_exp_f32 v4, v4\n"
        "v_mfma_f32_32x32x16_bf16 v[16:31], v[76:79], v[48:51], 0\n"
        "v_exp_f32 v5, v5\n"
        "v_exp_f32 v6, v6\n"
        "s_cmp_lg_u32 s3, 7\n"
        "s_cbranch_scc1 skip_104\n"
        "v_and_b32 v64, 0x70, v53\n"
        "v_lshlrev_b32 v64, 3, v64\n"
        "s_mov_b32 s88, 323584\n"
        "buffer_load_dwordx2 v[66:67], v64, s[36:39], s88 offen sc1\n"
        "skip_104:\n"
        "v_exp_f32 v7, v7\n"
        "v_exp_f32 v8, v8\n"
        "v_exp_f32 v9, v9\n"
        "v_smfmac_f32_32x32x32_bf16 v[32:47], v[112:115], v[0:7], v52\n"
        "v_exp_f32 v10, v10\n"
        "v_exp_f32 v11, v11\n"
        "v_exp_f32 v12, v12\n"
        "v_exp_f32 v13, v13\n"
        "v_exp_f32 v14, v14\n"
        "v_exp_f32 v15, v15\n"
        "v_exp_f32 v16, v16\n"
        "v_exp_f32 v17, v17\n"
        "v_smfmac_f32_32x32x32_bf16 v[32:47], v[116:119], v[8:15], v52\n"
        "v_exp_f32 v18, v18\n"
        "v_exp_f32 v19, v19\n"
        "v_exp_f32 v20, v20\n"
        "v_mfma_f32_32x32x16_bf16 v[0:15], v[80:83], v[48:51], 0\n"
        "v_exp_f32 v21, v21\n"
        "v_exp_f32 v22, v22\n"
        "v_exp_f32 v23, v23\n"
        "v_exp_f32 v24, v24\n"
        "v_exp_f32 v25, v25\n"
        "v_smfmac_f32_32x32x32_bf16 v[32:47], v[120:123], v[16:23], v52\n"
        "v_exp_f32 v26, v26\n"
        "v_exp_f32 v27, v27\n"
        "v_exp_f32 v28, v28\n"
        "v_exp_f32 v29, v29\n"
        "v_exp_f32 v30, v30\n"
        "v_exp_f32 v31, v31\n"
        "v_exp_f32 v0, v0\n"
        "v_exp_f32 v1, v1\n"
        "v_smfmac_f32_32x32x32_bf16 v[32:47], v[124:127], v[24:31], v52\n"
        "v_exp_f32 v2, v2\n"
        "v_exp_f32 v3, v3\n"
        "v_exp_f32 v4, v4\n"
        "v_mfma_f32_32x32x16_bf16 v[16:31], v[84:87], v[48:51], 0\n"
        "v_exp_f32 v5, v5\n"
        "v_exp_f32 v6, v6\n"
        "v_exp_f32 v7, v7\n"
        "v_exp_f32 v8, v8\n"
        "v_exp_f32 v9, v9\n"
        "v_smfmac_f32_32x32x32_bf16 v[32:47], v[128:131], v[0:7], v52\n"
        "v_exp_f32 v10, v10\n"
        "v_exp_f32 v11, v11\n"
        "v_exp_f32 v12, v12\n"
        "v_exp_f32 v13, v13\n"
        "v_exp_f32 v14, v14\n"
        "v_exp_f32 v15, v15\n"
        "v_exp_f32 v16, v16\n"
        "v_exp_f32 v17, v17\n"
        "v_smfmac_f32_32x32x32_bf16 v[32:47], v[132:135], v[8:15], v52\n"
        "v_exp_f32 v18, v18\n"
        "v_exp_f32 v19, v19\n"
        "v_exp_f32 v20, v20\n"
        "v_mfma_f32_32x32x16_bf16 v[0:15], v[88:91], v[48:51], 0\n"
        "v_exp_f32 v21, v21\n"
        "v_exp_f32 v22, v22\n"
        "v_exp_f32 v23, v23\n"
        "v_exp_f32 v24, v24\n"
        "v_exp_f32 v25, v25\n"
        "v_smfmac_f32_32x32x32_bf16 v[32:47], v[136:139], v[16:23], v52\n"
        "v_exp_f32 v26, v26\n"
        "v_exp_f32 v27, v27\n"
        "v_exp_f32 v28, v28\n"
        "s_mov_b32 s93, 0\n"
        "s_cmp_lg_u32 s3, 7\n"
        "s_cbranch_scc1 skip_105\n"
        "chk_106:\n"
        "s_waitcnt vmcnt(0)\n"
        "v_cmp_ne_u32 vcc, s45, v67\n"
        "s_cbranch_vccnz rclk_108\n"
        "go_107:\n"
        "v_subrev_u32 v66, s92, v66\n"
        "s_nop 1\n"
        "v_max_i32_dpp v66, v66, v66 row_ror:4 row_mask:0xf bank_mask:0xf bound_ctrl:1\n"
        "s_nop 1\n"
        "v_max_i32_dpp v66, v66, v66 row_ror:2 row_mask:0xf bank_mask:0xf bound_ctrl:1\n"
        "s_nop 1\n"
        "v_max_i32_dpp v66, v66, v66 row_ror:1 row_mask:0xf bank_mask:0xf bound_ctrl:1\n"
        "s_nop 0\n"
        "v_readfirstlane_b32 s93, v66\n"
        "s_nop 0\n"
        "s_min_u32 s93, s93, 200\n"
        "skip_105:\n"
        "v_exp_f32 v29, v29\n"
        "v_exp_f32 v30, v30\n"
        "v_exp_f32 v31, v31\n"
        "v_exp_f32 v0, v0\n"
        "v_exp_f32 v1, v1\n"
        "v_smfmac_f32_32x32x32_bf16 v[32:47], v[140:143], v[24:31], v52\n"
        "v_exp_f32 v2, v2\n"
        "v_exp_f32 v3, v3\n"
        "v_exp_f32 v4, v4\n"
        "v_mfma_f32_32x32x16_bf16 v[16:31], v[92:95], v[48:51], 0\n"
        "v_exp_f32 v5, v5\n"
        "v_exp_f32 v6, v6\n"
        "v_add_u32 v48, 160768, v53\n"
        "ds_read_b128 v[48:51], v48\n"
        "v_add_u32 v57, s19, v53\n"
        "ds_read_b128 v[64:67], v57 offset:0\n"
        "ds_read_b128 v[68:71], v57 offset:1024\n"
        "ds_read_b128 v[72:75], v57 offset:2048\n"
        "ds_read_b128 v[76:79], v57 offset:3072\n"
        "v_exp_f32 v7, v7\n"
        "v_exp_f32 v8, v8\n"
        "v_exp_f32 v9, v9\n"
        "v_smfmac_f32_32x32x32_bf16 v[32:47], v[144:147], v[0:7], v52\n"
        "v_exp_f32 v10, v10\n"
        "v_exp_f32 v11, v11\n"
        "v_exp_f32 v12, v12\n"
        "ds_read_b128 v[80:83], v57 offset:4096\n"
        "ds_read_b128 v[84:87], v57 offset:5120\n"
        "ds_read_b128 v[88:91], v57 offset:6144\n"
        "ds_read_b128 v[92:95], v57 offset:7168\n"
        "v_exp_f32 v13, v13\n"
        "v_exp_f32 v14, v14\n"
        "v_exp_f32 v15, v15\n"
        "v_exp_f32 v16, v16\n"
        "v_exp_f32 v17, v17\n"
        "v_smfmac_f32_32x32x32_bf16 v[32:47], v[148:151], v[8:15], v52\n"
        "v_exp_f32 v18, v18\n"
        "v_exp_f32 v19, v19\n"
        "v_exp_f32 v20, v20\n"
        "v_exp_f32 v21, v21\n"
        "v_exp_f32 v22, v22\n"
        "ds_read_b128 v[96:99], v57 offset:8192\n"
        "ds_read_b128 v[100:103], v57 offset:9216\n"
        "ds_read_b128 v[104:107], v57 offset:10240\n"
        "ds_read_b128 v[108:111], v57 offset:11264\n"
        "v_exp_f32 v23, v23\n"
        "v_exp_f32 v24, v24\n"
        "v_exp_f32 v25, v25\n"
        "v_smfmac_f32_32x32x32_bf16 v[32:47], v[152:155], v[16:23], v52\n"
        "v_exp_f32 v26, v26\n"
        "v_exp_f32 v27, v27\n"
        "v_exp_f32 v28, v28\n"
        "ds_read_b128 v[112:115], v57 offset:12288\n"
        "ds_read_b128 v[116:119], v57 offset:13312\n"
        "v_exp_f32 v29, v29\n"
        "v_exp_f32 v30, v30\n"
        "v_exp_f32 v31, v31\n"
        "s_nop 1\n"
        "v_smfmac_f32_32x32x32_bf16 v[32:47], v[156:159], v[24:31], v52\n"
        "s_waitcnt lgkmcnt(14)\n"
        "v_add_u32 v62, 61440, v48\n"
        "v_add_u32 v57, s25, v53\n"
        "s_nop 8\n"
        "ds_write_b128 v54, v[32:35] offset:17408\n"
        "ds_write_b128 v55, v[36:39] offset:17408\n"
        "ds_write_b32 v56, v40 offset:17408\n"
        "s_cmp_eq_u32 s93, 0\n"
        "s_cbranch_scc1 skip_110\n"
        "s_memrealtime s[88:89]\n"
        "lineup_109:\n"
        "s_memrealtime s[90:91]\n"
        "s_waitcnt lgkmcnt(0)\n"
        "s_sub_u32 s90, s90, s88\n"
        "s_cmp_lt_u32 s90, s93\n"
        "s_cbranch_scc1 lineup_109\n"
        "skip_110:\n"
        "s_waitcnt lgkmcnt(0)\n"
        "s_barrier\n"
        "ds_read_b128 v[0:3], v57\n"
        "ds_read_b128 v[4:7], v57 offset:1024\n"
        "ds_read_b128 v[8:11], v62 offset:0\n"
        "ds_read_b128 v[12:15], v62 offset:256\n"
        "ds_read_b128 v[16:19], v62 offset:4096\n"
        "ds_read_b128 v[20:23], v62 offset:4352\n"
        "ds_read_b128 v[24:27], v62 offset:8192\n"
        "ds_read_b128 v[28:31], v62 offset:8448\n"
        "s_waitcnt lgkmcnt(5)\n"
        "v_mfma_f32_16x16x32_bf16 v[0:3], v[64:67], v[8:11], v[0:3]\n"
        "ds_read_b128 v[8:11], v62 offset:12288\n"
        "v_and_b32 v32, 12, v51\n"
        "s_waitcnt lgkmcnt(5)\n"
        "v_mfma_f32_16x16x32_bf16 v[4:7], v[64:67], v[12:15], v[4:7]\n"
        "ds_read_b128 v[12:15], v62 offset:12544\n"
        "v_lshl_add_u32 v32, v32, 2, s69\n"
        "s_waitcnt lgkmcnt(5)\n"
        "v_mfma_f32_16x16x32_bf16 v[0:3], v[68:71], v[16:19], v[0:3]\n"
        "ds_read_b128 v[16:19], v62 offset:16384\n"
        "ds_read_b128 v[40:43], v32\n"
        "s_waitcnt lgkmcnt(6)\n"
        "v_mfma_f32_16x16x32_bf16 v[4:7], v[68:71], v[20:23], v[4:7]\n"
        "ds_read_b128 v[20:23], v62 offset:16640\n"
        "ds_read_b128 v[44:47], v32 offset:256\n"
        "s_waitcnt lgkmcnt(7)\n"
        "v_mfma_f32_16x16x32_bf16 v[0:3], v[72:75], v[24:27], v[0:3]\n"
        "ds_read_b128 v[24:27], v62 offset:20480\n"
        "v_lshrrev_b32 v52, 2, v53\n"
        "s_waitcnt lgkmcnt(7)\n"
        "v_mfma_f32_16x16x32_bf16 v[4:7], v[72:75], v[28:31], v[4:7]\n"
        "ds_read_b128 v[28:31], v62 offset:20736\n"
        "v_lshlrev_b32 v152, 16, v58\n"
        "s_waitcnt lgkmcnt(7)\n"
        "v_mfma_f32_16x16x32_bf16 v[0:3], v[76:79], v[8:11], v[0:3]\n"
        "ds_read_b128 v[8:11], v62 offset:24576\n"
        "v_and_b32 v153, 0xffff0000, v58\n"
        "s_waitcnt lgkmcnt(7)\n"
        "v_mfma_f32_16x16x32_bf16 v[4:7], v[76:79], v[12:15], v[4:7]\n"
        "ds_read_b128 v[12:15], v62 offset:24832\n"
        "v_lshlrev_b32 v154, 16, v59\n"
        "s_waitcnt lgkmcnt(7)\n"
        "v_mfma_f32_16x16x32_bf16 v[0:3], v[80:83], v[16:19], v[0:3]\n"
        "ds_read_b128 v[16:19], v62 offset:28672\n"
        "v_and_b32 v155, 0xffff0000, v59\n"
        "s_waitcnt lgkmcnt(6)\n"
        "v_mfma_f32_16x16x32_bf16 v[4:7], v[80:83], v[20:23], v[4:7]\n"
        "ds_read_b128 v[20:23], v62 offset:28928\n"
        "v_lshlrev_b32 v156, 16, v60\n"
        "s_waitcnt lgkmcnt(5)\n"
        "v_mfma_f32_16x16x32_bf16 v[0:3], v[84:87], v[24:27], v[0:3]\n"
        "v_and_b32 v157, 0xffff0000, v60\n"
        "s_waitcnt lgkmcnt(4)\n"
        "v_mfma_f32_16x16x32_bf16 v[4:7], v[84:87], v[28:31], v[4:7]\n"
        "v_lshlrev_b32 v158, 16, v61\n"
        "s_waitcnt lgkmcnt(3)\n"
        "v_mfma_f32_16x16x32_bf16 v[0:3], v[88:91], v[8:11], v[0:3]\n"
        "v_and_b32 v159, 0xffff0000, v61\n"
        "s_waitcnt lgkmcnt(2)\n"
        "v_mfma_f32_16x16x32_bf16 v[4:7], v[88:91], v[12:15], v[4:7]\n"
        "s_waitcnt lgkmcnt(1)\n"
        "v_mfma_f32_16x16x32_bf16 v[0:3], v[92:95], v[16:19], v[0:3]\n"
        "s_waitcnt lgkmcnt(0)\n"
        "v_mfma_f32_16x16x32_bf16 v[4:7], v[92:95], v[20:23], v[4:7]\n"
        "s_setprio 1\n"
        "s_nop 6\n"
        "v_cmp_u_f32 vcc, v0, v4\n"
        "s_cbranch_vccnz woslow_111\n"
        "go_112:\n"
        "s_nop 1\n"
        "v_fmac_f32 v152, v0, v40\n"
        "v_fmac_f32 v153, v1, v41\n"
        "v_fmac_f32 v154, v2, v42\n"
        "v_fmac_f32 v155, v3, v43\n"
        "v_mul_f32 v8, v152, v44\n"
        "v_mul_f32 v9, v153, v45\n"
        "v_mul_f32 v10, v154, v46\n"
        "v_mul_f32 v11, v155, v47\n"
        "v_cvt_pk_bf16_f32 v8, v8, v9\n"
        "v_cvt_pk_bf16_f32 v9, v10, v11\n"
        "v_min_u32 v8, -2, v8\n"
        "v_min_u32 v9, -2, v9\n"
        "buffer_store_dwordx2 v[8:9], v49, s[36:39], s65 offen offset:0 sc1\n"
        "v_fmac_f32 v156, v4, v40\n"
        "v_fmac_f32 v157, v5, v41\n"
        "v_fmac_f32 v158, v6, v42\n"
        "v_fmac_f32 v159, v7, v43\n"
        "v_mul_f32 v12, v156, v44\n"
        "v_mul_f32 v13, v157, v45\n"
        "v_mul_f32 v14, v158, v46\n"
        "v_mul_f32 v15, v159, v47\n"
        "v_cvt_pk_bf16_f32 v12, v12, v13\n"
        "v_cvt_pk_bf16_f32 v13, v14, v15\n"
        "v_min_u32 v12, -2, v12\n"
        "v_min_u32 v13, -2, v13\n"
        "buffer_store_dwordx2 v[12:13], v49, s[36:39], s65 offen offset:256 sc1\n"
        "v_mul_f32 v18, v152, v152\n"
        "v_add_f32 v16, v152, v153\n"
        "v_fmac_f32 v18, v153, v153\n"
        "v_add_f32 v16, v16, v154\n"
        "v_fmac_f32 v18, v154, v154\n"
        "v_add_f32 v16, v16, v155\n"
        "v_fmac_f32 v18, v155, v155\n"
        "v_mul_f32 v19, v156, v156\n"
        "v_add_f32 v17, v156, v157\n"
        "v_fmac_f32 v19, v157, v157\n"
        "v_add_f32 v17, v17, v158\n"
        "v_fmac_f32 v19, v158, v158\n"
        "v_add_f32 v17, v17, v159\n"
        "v_fmac_f32 v19, v159, v159\n"
        "v_permlane32_swap_b32 v16, v18\n"
        "s_nop 0\n"
        "v_permlane32_swap_b32 v17, v19\n"
        "v_add_f32 v16, v16, v18\n"
        "v_add_f32 v17, v17, v19\n"
        "s_nop 1\n"
        "v_permlane16_swap_b32 v16, v17\n"
        "v_add_f32 v16, v16, v17\n"
        "v_min_u32 v16, -2, v16\n"
        "buffer_store_dword v16, v52, s[36:39], s66 offen sc1\n"
        "v_and_b32 v37, 0x1f0, v53\n"
        "buffer_load_dwordx4 v[54:57], v37, s[32:35], s27 offen sc1\n"
        "buffer_load_dwordx4 v[120:123], v53, s[32:35], s14 offen offset:1024 sc1\n"
        "buffer_load_dwordx4 v[124:127], v53, s[32:35], s14 offen offset:2048 sc1\n"
        "ds_read_b128 v[8:11], v48 offset:43008\n"
        "ds_read_b128 v[12:15], v48 offset:43264\n"
        "ds_read_b128 v[16:19], v48 offset:45056\n"
        "ds_read_b128 v[20:23], v48 offset:45312\n"
        "ds_read_b128 v[24:27], v48 offset:47104\n"
        "ds_read_b128 v[28:31], v48 offset:47360\n"
        "s_waitcnt lgkmcnt(5)\n"
        "v_mfma_f32_16x16x32_bf16 v[0:3], v[64:67], v[8:11], 0\n"
        "ds_read_b128 v[8:11], v48 offset:49152\n"
        "v_add_u32 v58, s68, v53\n"
        "s_waitcnt lgkmcnt(5)\n"
        "v_mfma_f32_16x16x32_bf16 v[4:7], v[64:67], v[12:15], 0\n"
        "ds_read_b128 v[12:15], v48 offset:49408\n"
        "v_add_u32 v59, 92160, v53\n"
        "s_waitcnt lgkmcnt(5)\n"
        "v_mfma_f32_16x16x32_bf16 v[0:3], v[68:71], v[16:19], v[0:3]\n"
        "ds_read_b128 v[16:19], v48 offset:51200\n"
        "v_add_u32 v39, s25, v53\n"
        "s_waitcnt lgkmcnt(5)\n"
        "v_mfma_f32_16x16x32_bf16 v[4:7], v[68:71], v[20:23], v[4:7]\n"
        "ds_read_b128 v[20:23], v48 offset:51456\n"
        "s_add_u32 s40, s36, 88064\n"
        "s_waitcnt lgkmcnt(5)\n"
        "v_mfma_f32_16x16x32_bf16 v[0:3], v[72:75], v[24:27], v[0:3]\n"
        "ds_read_b128 v[24:27], v48 offset:53248\n"
        "s_addc_u32 s41, s37, 0\n"
        "s_waitcnt lgkmcnt(5)\n"
        "v_mfma_f32_16x16x32_bf16 v[4:7], v[72:75], v[28:31], v[4:7]\n"
        "ds_read_b128 v[28:31], v48 offset:53504\n"
        "s_sub_u32 s42, s38, 88064\n"
        "s_waitcnt lgkmcnt(5)\n"
        "v_mfma_f32_16x16x32_bf16 v[0:3], v[76:79], v[8:11], v[0:3]\n"
        "ds_read_b128 v[8:11], v48 offset:55296\n"
        "s_mov_b32 s43, s39\n"
        "s_waitcnt lgkmcnt(5)\n"
        "v_mfma_f32_16x16x32_bf16 v[4:7], v[76:79], v[12:15], v[4:7]\n"
        "ds_read_b128 v[12:15], v48 offset:55552\n"
        "v_add_u32 v62, s24, v51\n"
        "s_waitcnt lgkmcnt(5)\n"
        "v_mfma_f32_16x16x32_bf16 v[0:3], v[80:83], v[16:19], v[0:3]\n"
        "ds_read_b128 v[16:19], v48 offset:57344\n"
        "s_waitcnt lgkmcnt(5)\n"
        "v_mfma_f32_16x16x32_bf16 v[4:7], v[80:83], v[20:23], v[4:7]\n"
        "ds_read_b128 v[20:23], v48 offset:57600\n"
        "s_waitcnt lgkmcnt(5)\n"
        "v_mfma_f32_16x16x32_bf16 v[0:3], v[84:87], v[24:27], v[0:3]\n"
        "s_waitcnt lgkmcnt(4)\n"
        "v_mfma_f32_16x16x32_bf16 v[4:7], v[84:87], v[28:31], v[4:7]\n"
        "s_waitcnt lgkmcnt(3)\n"
        "v_mfma_f32_16x16x32_bf16 v[0:3], v[88:91], v[8:11], v[0:3]\n"
        "s_waitcnt lgkmcnt(2)\n"
        "v_mfma_f32_16x16x32_bf16 v[4:7], v[88:91], v[12:15], v[4:7]\n"
        "s_waitcnt lgkmcnt(1)\n"
        "v_mfma_f32_16x16x32_bf16 v[0:3], v[92:95], v[16:19], v[0:3]\n"
        "s_waitcnt lgkmcnt(0)\n"
        "v_mfma_f32_16x16x32_bf16 v[4:7], v[92:95], v[20:23], v[4:7]\n"
        "s_nop 5\n"
        "ds_write_b128 v39, v[0:3]\n"
        "s_nop 0\n"
        "ds_write_b128 v39, v[4:7] offset:1024\n"
        "s_setprio 0\n"
        "s_sleep 1\n"
        "buffer_load_dwordx4 v[32:35], v53, s[36:39], s63 offen offset:0 sc1\n"
        "s_add_u32 m0, s62, 0\n"
        "s_nop 0\n"
        "buffer_load_dwordx4 v53, s[36:39], s60 offen offset:0 sc1 lds\n"
        "buffer_load_dwordx4 v53, s[36:39], s60 offen offset:1024 sc1 lds\n"
        "buffer_load_dwordx4 v53, s[36:39], s60 offen offset:2048 sc1 lds\n"
        "buffer_load_dwordx4 v53, s[36:39], s60 offen offset:3072 sc1 lds\n"
        "s_sub_u32 s88, s63, 4096\n"
        "buffer_load_dwordx4 v[28:31], v53, s[36:39], s88 offen offset:0 sc1\n"
        "s_sub_u32 s88, s60, 16384\n"
        "s_add_u32 m0, s62, -16384\n"
        "s_nop 0\n"
        "buffer_load_dwordx4 v53, s[36:39], s88 offen offset:0 sc1 lds\n"
        "buffer_load_dwordx4 v53, s[36:39], s88 offen offset:1024 sc1 lds\n"
        "s_waitcnt lgkmcnt(0)\n"
        "s_barrier\n"
        "buffer_load_dwordx4 v53, s[36:39], s88 offen offset:2048 sc1 lds\n"
        "buffer_load_dwordx4 v53, s[36:39], s88 offen offset:3072 sc1 lds\n"
        "chk_113:\n"
        "s_waitcnt vmcnt(12)\n"
        "s_nop 0\n"
        "page7:\n"
        "s_cbranch_execz warm_back\n"
        "v_cmp_ne_u32 s[90:91], s45, v57\n"
        "v_cmp_ne_u32 vcc, s45, v55\n"
        "s_or_b64 vcc, vcc, s[90:91]\n"
        "s_cbranch_scc1 rau_115\n"
        "go_114:\n"
        "v_cvt_pk_bf16_f32 v55, v54, v56\n"
        "s_mov_b32 exec_lo, 0\n"
        "v_lshlrev_b32 v57, 16, v55\n"
        "v_and_b32 v55, 0xffff0000, v55\n"
        "v_sub_f32 v54, v54, v57\n"
        "v_sub_f32 v56, v56, v55\n"
        "v_cvt_pk_bf16_f32 v55, v54, v56\n"
        "s_mov_b32 exec_lo, -1\n"
        "v_mov_b32 v54, v55\n"
        "buffer_load_dwordx4 v[128:131], v53, s[32:35], s10 offen offset:3072 sc1\n"
        "buffer_load_dwordx4 v[132:135], v53, s[32:35], s14 offen offset:3072 sc1\n"
        "chk_116:\n"
        "s_waitcnt vmcnt(11)\n"
        "v_max_u32 v39, v32, v33\n"
        "v_max3_u32 v39, v39, v34, v35\n"
        "v_cmp_eq_u32 vcc, -1, v39\n"
        "s_cbranch_vccnz ry0_118\n"
        "go_117:\n"
        "v_permlane32_swap_b32 v32, v34\n"
        "v_permlane32_swap_b32 v33, v35\n"
        "v_add_f32 v32, v32, v34\n"
        "v_add_f32 v33, v33, v35\n"
        "s_nop 1\n"
        "v_permlane16_swap_b32 v32, v33\n"
        "v_add_f32 v32, v32, v33\n"
        "ds_write_b32 v62, v32 offset:2048\n"
        "chk_119:\n"
        "s_waitcnt vmcnt(6)\n"
        "v_max_u32 v39, v28, v29\n"
        "v_max3_u32 v39, v39, v30, v31\n"
        "v_cmp_eq_u32 vcc, -1, v39\n"
        "s_cbranch_vccnz ry0_121\n"
        "go_120:\n"
        "v_permlane32_swap_b32 v28, v30\n"
        "v_permlane32_swap_b32 v29, v31\n"
        "v_add_f32 v28, v28, v30\n"
        "v_add_f32 v29, v29, v31\n"
        "s_nop 1\n"
        "v_permlane16_swap_b32 v28, v29\n"
        "v_add_f32 v28, v28, v29\n"
        "ds_write_b32 v62, v28 offset:1024\n"
        "s_sub_u32 s89, s62, 16384\n"
        "v_lshl_add_u32 v47, v53, 2, s89\n"
        "s_waitcnt vmcnt(2)\n"
        "ds_read_b32 v46, v47 offset:16384\n"
        "ds_read_b32 v45, v47 offset:0\n"
        "chk_122:\n"
        "s_waitcnt lgkmcnt(0)\n"
        "v_max_u32 v46, v46, v45\n"
        "v_cmp_eq_u32 vcc, -1, v46\n"
        "s_cbranch_vccnz ryp0_124\n"
        "go_123:\n"
        "s_waitcnt lgkmcnt(0)\n"
        "s_barrier\n"
        "ds_read2st64_b32 v[32:33], v52 offset0:8 offset1:9\n"
        "ds_read2st64_b32 v[34:35], v52 offset0:10 offset1:11\n"
        "ds_read2st64_b32 v[36:37], v52 offset0:12 offset1:13\n"
        "ds_read2st64_b32 v[38:39], v52 offset0:14 offset1:15\n"
        "s_waitcnt lgkmcnt(2)\n"
        "v_add_f32 v32, v32, v34\n"
        "v_add_f32 v33, v33, v35\n"
        "s_waitcnt lgkmcnt(0)\n"
        "v_add_f32 v36, v36, v38\n"
        "v_add_f32 v37, v37, v39\n"
        "v_add_f32 v32, v32, v36\n"
        "v_add_f32 v33, v33, v37\n"
        "v_add_f32 v34, v32, v33\n"
        "buffer_load_dwordx4 v[136:139], v53, s[32:35], s11 offen offset:0 sc1\n"
        "v_add_f32 v33, v32, v33\n"
        "s_nop 1\n"
        "v_permlane32_swap_b32 v34, v33\n"
        "v_mul_f32 v32, 0xbb000000, v34\n"
        "v_mul_f32 v34, v32, v32\n"
        "v_mov_b32 v35, 0x3b000000\n"
        "v_fma_f32 v33, v33, v35, -v34\n"
        "v_max_f32 v33, 0, v33\n"
        "buffer_load_dwordx4 v[140:143], v53, s[32:35], s15 offen offset:0 sc1\n"
        "v_add_f32 v33, 0x3727c5ac, v33\n"
        "v_rsq_f32 v60, v33\n"
        "s_nop 0\n"
        "v_mul_f32 v33, v33, v60\n"
        "v_sub_f32 v60, 0, v60\n"
        "v_cvt_pk_bf16_f32 v40, v32, v33\n"
        "v_lshlrev_b32 v34, 16, v40\n"
        "v_and_b32 v35, 0xffff0000, v40\n"
        "buffer_load_dwordx4 v[144:147], v53, s[32:35], s11 offen offset:1024 sc1\n"
        "v_sub_f32 v32, v32, v34\n"
        "v_sub_f32 v33, v33, v35\n"
        "v_cvt_pk_bf16_f32 v41, v32, v33\n"
        "buffer_load_dwordx4 v[48:51], v53, s[36:39], s64 offen offset:0 sc1\n"
        "s_add_u32 m0, s62, 32768\n"
        "s_nop 0\n"
        "buffer_load_dwordx4 v53, s[36:39], s61 offen offset:0 sc1 lds\n"
        "buffer_load_dwordx4 v53, s[36:39], s61 offen offset:1024 sc1 lds\n"
        "buffer_load_dwordx4 v53, s[36:39], s61 offen offset:2048 sc1 lds\n"
        "buffer_load_dwordx4 v53, s[36:39], s61 offen offset:3072 sc1 lds\n"
        "s_sub_u32 s88, s64, 4096\n"
        "buffer_load_dwordx4 v[28:31], v53, s[36:39], s88 offen offset:0 sc1\n"
        "s_sub_u32 s88, s61, 16384\n"
        "s_add_u32 m0, s62, 16384\n"
        "s_nop 0\n"
        "buffer_load_dwordx4 v53, s[36:39], s88 offen offset:0 sc1 lds\n"
        "buffer_load_dwordx4 v53, s[36:39], s88 offen offset:1024 sc1 lds\n"
        "buffer_load_dwordx4 v53, s[36:39], s88 offen offset:2048 sc1 lds\n"
        "buffer_load_dwordx4 v53, s[36:39], s88 offen offset:3072 sc1 lds\n"
        "ds_read_b128 v[16:19], v59 offset:0\n"
        "ds_read_b128 v[20:23], v59 offset:1024\n"
        "ds_read_b128 v[24:27], v59 offset:2048\n"
        "s_waitcnt lgkmcnt(2)\n"
        "v_mfma_f32_32x32x16_bf16 v[0:15], v[160:163], v[16:19], 0\n"
        "ds_read_b128 v[16:19], v59 offset:3072\n"
        "s_waitcnt lgkmcnt(2)\n"
        "v_mfma_f32_32x32x16_bf16 v[0:15], v[164:167], v[20:23], v[0:15]\n"
        "ds_read_b128 v[20:23], v59 offset:4096\n"
        "s_setprio 1\n"
        "buffer_load_dwordx4 v[148:151], v53, s[32:35], s15 offen offset:1024 sc1\n"
        "s_waitcnt lgkmcnt(2)\n"
        "v_mfma_f32_32x32x16_bf16 v[0:15], v[168:171], v[24:27], v[0:15]\n"
        "ds_read_b128 v[24:27], v59 offset:5120\n"
        "s_waitcnt lgkmcnt(2)\n"
        "v_mfma_f32_32x32x16_bf16 v[0:15], v[172:175], v[16:19], v[0:15]\n"
        "ds_read_b128 v[16:19], v59 offset:6144\n"
        "buffer_load_dwordx4 v[64:67], v53, s[32:35], s11 offen offset:2048 sc1\n"
        "s_waitcnt lgkmcnt(2)\n"
        "v_mfma_f32_32x32x16_bf16 v[0:15], v[176:179], v[20:23], v[0:15]\n"
        "ds_read_b128 v[20:23], v59 offset:7168\n"
        "s_waitcnt lgkmcnt(2)\n"
        "v_mfma_f32_32x32x16_bf16 v[0:15], v[180:183], v[24:27], v[0:15]\n"
        "ds_read_b128 v[24:27], v59 offset:8192\n"
        "buffer_load_dwordx4 v[68:71], v53, s[32:35], s15 offen offset:2048 sc1\n"
        "s_waitcnt lgkmcnt(2)\n"
        "v_mfma_f32_32x32x16_bf16 v[0:15], v[184:187], v[16:19], v[0:15]\n"
        "ds_read_b128 v[16:19], v59 offset:9216\n"
        "s_waitcnt lgkmcnt(2)\n"
        "v_mfma_f32_32x32x16_bf16 v[0:15], v[188:191], v[20:23], v[0:15]\n"
        "ds_read_b128 v[20:23], v59 offset:10240\n"
        "buffer_load_dwordx4 v[72:75], v53, s[32:35], s11 offen offset:3072 sc1\n"
        "s_waitcnt lgkmcnt(2)\n"
        "v_mfma_f32_32x32x16_bf16 v[0:15], v[192:195], v[24:27], v[0:15]\n"
        "ds_read_b128 v[24:27], v59 offset:11264\n"
        "s_waitcnt lgkmcnt(2)\n"
        "v_mfma_f32_32x32x16_bf16 v[0:15], v[196:199], v[16:19], v[0:15]\n"
        "ds_read_b128 v[16:19], v59 offset:12288\n"
        "buffer_load_dwordx4 v[76:79], v53, s[32:35], s15 offen offset:3072 sc1\n"
        "s_waitcnt lgkmcnt(2)\n"
        "v_mfma_f32_32x32x16_bf16 v[0:15], v[200:203], v[20:23], v[0:15]\n"
        "ds_read_b128 v[20:23], v59 offset:13312\n"
        "chk_125:\n"
        "s_waitcnt vmcnt(14)\n"
        "v_max_u32 v46, v48, v49\n"
        "v_max3_u32 v46, v46, v50, v51\n"
        "v_cmp_eq_u32 vcc, -1, v46\n"
        "s_cbranch_vccnz ry1_127\n"
        "go_126:\n"
        "s_waitcnt lgkmcnt(2)\n"
        "v_mfma_f32_32x32x16_bf16 v[0:15], v[204:207], v[24:27], v[0:15]\n"
        "ds_read_b128 v[24:27], v59 offset:14336\n"
        "s_barrier\n"
        "v_permlane32_swap_b32 v48, v50\n"
        "v_permlane32_swap_b32 v49, v51\n"
        "v_add_f32 v48, v48, v50\n"
        "buffer_load_dwordx4 v[80:83], v53, s[32:35], s12 offen offset:0 sc1\n"
        "s_waitcnt lgkmcnt(2)\n"
        "v_mfma_f32_32x32x16_bf16 v[0:15], v[208:211], v[16:19], v[0:15]\n"
        "ds_read_b128 v[16:19], v59 offset:15360\n"
        "v_add_f32 v49, v49, v51\n"
        "s_nop 1\n"
        "v_permlane16_swap_b32 v48, v49\n"
        "v_add_f32 v48, v48, v49\n"
        "s_waitcnt lgkmcnt(2)\n"
        "v_mfma_f32_32x32x16_bf16 v[0:15], v[212:215], v[20:23], v[0:15]\n"
        "ds_read_b128 v[20:23], v59 offset:16384\n"
        "ds_write_b32 v62, v48 offset:4096\n"
        "chk_128:\n"
        "s_waitcnt vmcnt(10)\n"
        "v_max_u32 v46, v28, v29\n"
        "v_max3_u32 v46, v46, v30, v31\n"
        "v_cmp_eq_u32 vcc, -1, v46\n"
        "s_cbranch_vccnz ry1_130\n"
        "go_129:\n"
        "s_waitcnt lgkmcnt(3)\n"
        "v_mfma_f32_32x32x16_bf16 v[0:15], v[216:219], v[24:27], v[0:15]\n"
        "ds_read_b128 v[24:27], v59 offset:17408\n"
        "v_permlane32_swap_b32 v28, v30\n"
        "v_permlane32_swap_b32 v29, v31\n"
        "v_add_f32 v28, v28, v30\n"
        "s_waitcnt lgkmcnt(3)\n"
        "v_mfma_f32_32x32x16_bf16 v[0:15], v[220:223], v[16:19], v[0:15]\n"
        "ds_read_b128 v[16:19], v59 offset:18432\n"
        "v_add_f32 v29, v29, v31\n"
        "s_nop 1\n"
        "v_permlane16_swap_b32 v28, v29\n"
        "v_add_f32 v28, v28, v29\n"
        "s_waitcnt lgkmcnt(3)\n"
        "v_mfma_f32_32x32x16_bf16 v[0:15], v[224:227], v[20:23], v[0:15]\n"
        "ds_read_b128 v[20:23], v59 offset:19456\n"
        "ds_write_b32 v62, v28 offset:3072\n"
        "ds_read_b128 v[32:35], v58 offset:1024\n"
        "s_waitcnt lgkmcnt(4)\n"
        "v_mfma_f32_32x32x16_bf16 v[0:15], v[228:231], v[24:27], v[0:15]\n"
        "ds_read_b128 v[24:27], v59 offset:20480\n"
        "ds_read_b128 v[36:39], v58 offset:2048\n"
        "s_waitcnt lgkmcnt(5)\n"
        "v_mfma_f32_32x32x16_bf16 v[0:15], v[232:235], v[16:19], v[0:15]\n"
        "ds_read_b128 v[16:19], v59 offset:21504\n"
        "ds_read_b128 v[28:31], v58 offset:0\n"
        "s_waitcnt lgkmcnt(6)\n"
        "v_mfma_f32_32x32x16_bf16 v[0:15], v[236:239], v[20:23], v[0:15]\n"
        "ds_read_b128 v[20:23], v59 offset:22528\n"
        "s_sub_u32 s89, s62, 16384\n"
        "v_lshl_add_u32 v47, v53, 2, s89\n"
        "s_waitcnt vmcnt(6)\n"
        "ds_read_b32 v46, v47 offset:49152\n"
        "ds_read_b32 v45, v47 offset:32768\n"
        "s_waitcnt lgkmcnt(7)\n"
        "v_mul_f32 v32, v32, v60\n"
        "v_mul_f32 v33, v33, v60\n"
        "v_mul_f32 v34, v34, v60\n"
        "v_mul_f32 v35, v35, v60\n"
        "s_waitcnt lgkmcnt(6)\n"
        "v_mfma_f32_32x32x16_bf16 v[0:15], v[240:243], v[24:27], v[0:15]\n"
        "ds_read_b128 v[24:27], v59 offset:23552\n"
        "s_waitcnt lgkmcnt(6)\n"
        "v_mul_f32 v36, v36, v60\n"
        "v_mul_f32 v37, v37, v60\n"
        "v_mul_f32 v38, v38, v60\n"
        "v_mul_f32 v39, v39, v60\n"
        "s_waitcnt lgkmcnt(5)\n"
        "v_mfma_f32_32x32x16_bf16 v[0:15], v[244:247], v[16:19], v[0:15]\n"
        "chk_131:\n"
        "s_waitcnt lgkmcnt(1)\n"
        "v_max_u32 v46, v46, v45\n"
        "v_cmp_eq_u32 vcc, -1, v46\n"
        "s_cbranch_vccnz ryp1_133\n"
        "go_132:\n"
        "s_waitcnt lgkmcnt(0)\n"
        "s_barrier\n"
        "s_setprio 3\n"
        "ds_read_b128 v[16:19], v58 offset:3072\n"
        "v_mfma_f32_32x32x16_bf16 v[0:15], v[248:251], v[20:23], v[0:15]\n"
        "v_mul_f32 v28, v28, v60\n"
        "v_mul_f32 v29, v29, v60\n"
        "v_mul_f32 v30, v30, v60\n"
        "v_mul_f32 v31, v31, v60\n"
        "v_mfma_f32_32x32x16_bf16 v[0:15], v[252:255], v[24:27], v[0:15]\n"
        "s_waitcnt lgkmcnt(0)\n"
        "v_mul_f32 v16, v16, v60\n"
        "v_mul_f32 v17, v17, v60\n"
        "v_mul_f32 v18, v18, v60\n"
        "v_mul_f32 v19, v19, v60\n"
        "v_mfma_f32_32x32x8bf16_1k v[0:15], v[54:55], v[40:41], v[0:15]\n"
        "s_setprio 0\n"
        "s_nop 10\n"
        "v_cmp_u_f32 vcc, v0, v28\n"
        "s_cbranch_vccnz f1slow_134\n"
        "go_135:\n"
        "s_nop 10\n"
        "v_fma_f32 v0, v0, v60, v28\n"
        "v_fma_f32 v1, v1, v60, v29\n"
        "v_fma_f32 v2, v2, v60, v30\n"
        "v_fma_f32 v3, v3, v60, v31\n"
        "v_fma_f32 v4, v4, v60, v32\n"
        "v_fma_f32 v5, v5, v60, v33\n"
        "v_fma_f32 v6, v6, v60, v34\n"
        "v_fma_f32 v7, v7, v60, v35\n"
        "v_exp_f32 v20, v0\n"
        "buffer_load_dwordx4 v[84:87], v53, s[32:35], s16 offen offset:0 sc1\n"
        "v_exp_f32 v21, v1\n"
        "v_exp_f32 v22, v2\n"
        "v_exp_f32 v23, v3\n"
        "v_exp_f32 v24, v4\n"
        "v_exp_f32 v25, v5\n"
        "v_exp_f32 v26, v6\n"
        "v_exp_f32 v27, v7\n"
        "v_add_f32 v20, 1.0, v20\n"
        "v_add_f32 v21, 1.0, v21\n"
        "buffer_load_dwordx4 v[88:91], v53, s[32:35], s12 offen offset:1024 sc1\n"
        "v_add_f32 v22, 1.0, v22\n"
        "v_add_f32 v23, 1.0, v23\n"
        "v_add_f32 v24, 1.0, v24\n"
        "v_add_f32 v25, 1.0, v25\n"
        "v_add_f32 v26, 1.0, v26\n"
        "v_add_f32 v27, 1.0, v27\n"
        "v_rcp_f32 v20, v20\n"
        "v_rcp_f32 v21, v21\n"
        "v_rcp_f32 v22, v22\n"
        "buffer_load_dwordx4 v[92:95], v53, s[32:35], s16 offen offset:1024 sc1\n"
        "v_rcp_f32 v23, v23\n"
        "v_rcp_f32 v24, v24\n"
        "v_rcp_f32 v25, v25\n"
        "v_rcp_f32 v26, v26\n"
        "v_rcp_f32 v27, v27\n"
        "v_mul_f32 v0, v0, v20\n"
        "v_mul_f32 v1, v1, v21\n"
        "v_mul_f32 v2, v2, v22\n"
        "v_mul_f32 v3, v3, v23\n"
        "v_mul_f32 v4, v4, v24\n"
        "v_mul_f32 v5, v5, v25\n"
        "v_mul_f32 v6, v6, v26\n"
        "v_mul_f32 v7, v7, v27\n"
        "v_cvt_pk_bf16_f32 v44, v0, v1\n"
        "v_cvt_pk_bf16_f32 v45, v2, v3\n"
        "v_cvt_pk_bf16_f32 v46, v4, v5\n"
        "v_cvt_pk_bf16_f32 v47, v6, v7\n"
        "v_min_u32 v44, -2, v44\n"
        "v_min_u32 v45, -2, v45\n"
        "v_min_u32 v46, -2, v46\n"
        "v_min_u32 v47, -2, v47\n"
        "buffer_store_dwordx4 v[44:47], v53, s[36:39], s58 offen offset:0 sc1\n"
        "v_fma_f32 v8, v8, v60, v36\n"
        "v_fma_f32 v9, v9, v60, v37\n"
        "v_fma_f32 v10, v10, v60, v38\n"
        "v_fma_f32 v11, v11, v60, v39\n"
        "v_fma_f32 v12, v12, v60, v16\n"
        "v_fma_f32 v13, v13, v60, v17\n"
        "v_fma_f32 v14, v14, v60, v18\n"
        "v_fma_f32 v15, v15, v60, v19\n"
        "v_exp_f32 v20, v8\n"
        "v_exp_f32 v21, v9\n"
        "v_exp_f32 v22, v10\n"
        "v_exp_f32 v23, v11\n"
        "v_exp_f32 v24, v12\n"
        "v_exp_f32 v25, v13\n"
        "v_exp_f32 v26, v14\n"
        "v_exp_f32 v27, v15\n"
        "v_add_f32 v20, 1.0, v20\n"
        "v_add_f32 v21, 1.0, v21\n"
        "v_add_f32 v22, 1.0, v22\n"
        "v_add_f32 v23, 1.0, v23\n"
        "v_add_f32 v24, 1.0, v24\n"
        "v_add_f32 v25, 1.0, v25\n"
        "v_add_f32 v26, 1.0, v26\n"
        "v_add_f32 v27, 1.0, v27\n"
        "v_rcp_f32 v20, v20\n"
        "v_rcp_f32 v21, v21\n"
        "v_rcp_f32 v22, v22\n"
        "v_rcp_f32 v23, v23\n"
        "v_rcp_f32 v24, v24\n"
        "v_rcp_f32 v25, v25\n"
        "v_rcp_f32 v26, v26\n"
        "v_rcp_f32 v27, v27\n"
        "v_mul_f32 v8, v8, v20\n"
        "v_mul_f32 v9, v9, v21\n"
        "v_mul_f32 v10, v10, v22\n"
        "v_mul_f32 v11, v11, v23\n"
        "v_mul_f32 v12, v12, v24\n"
        "v_mul_f32 v13, v13, v25\n"
        "v_mul_f32 v14, v14, v26\n"
        "v_mul_f32 v15, v15, v27\n"
        "v_cvt_pk_bf16_f32 v48, v8, v9\n"
        "v_cvt_pk_bf16_f32 v49, v10, v11\n"
        "v_cvt_pk_bf16_f32 v50, v12, v13\n"
        "v_cvt_pk_bf16_f32 v51, v14, v15\n"
        "v_min_u32 v48, -2, v48\n"
        "v_min_u32 v49, -2, v49\n"
        "v_min_u32 v50, -2, v50\n"
        "v_min_u32 v51, -2, v51\n"
        "buffer_store_dwordx4 v[48:51], v53, s[36:39], s58 offen offset:1024 sc1\n"
        "s_setprio 0\n"
        "ds_read_b128 v[16:19], v59 offset:32768\n"
        "ds_read_b128 v[20:23], v59 offset:33792\n"
        "ds_read_b128 v[24:27], v59 offset:34816\n"
        "ds_read2st64_b32 v[32:33], v52 offset0:16 offset1:17\n"
        "ds_read2st64_b32 v[34:35], v52 offset0:18 offset1:19\n"
        "ds_read2st64_b32 v[36:37], v52 offset0:20 offset1:21\n"
        "ds_read2st64_b32 v[38:39], v52 offset0:22 offset1:23\n"
        "s_waitcnt lgkmcnt(6)\n"
        "v_mfma_f32_32x32x16_bf16 v[0:15], v[160:163], v[16:19], 0\n"
        "ds_read_b128 v[16:19], v59 offset:35840\n"
        "s_waitcnt lgkmcnt(6)\n"
        "v_mfma_f32_32x32x16_bf16 v[0:15], v[164:167], v[20:23], v[0:15]\n"
        "ds_read_b128 v[20:23], v59 offset:36864\n"
        "s_setprio 1\n"
        "s_waitcnt lgkmcnt(6)\n"
        "v_mfma_f32_32x32x16_bf16 v[0:15], v[168:171], v[24:27], v[0:15]\n"
        "ds_read_b128 v[24:27], v59 offset:37888\n"
        "s_waitcnt lgkmcnt(5)\n"
        "v_add_f32 v32, v32, v34\n"
        "v_add_f32 v33, v33, v35\n"
        "s_waitcnt lgkmcnt(3)\n"
        "v_add_f32 v36, v36, v38\n"
        "v_add_f32 v37, v37, v39\n"
        "s_waitcnt lgkmcnt(2)\n"
        "v_mfma_f32_32x32x16_bf16 v[0:15], v[172:175], v[16:19], v[0:15]\n"
        "ds_read_b128 v[16:19], v59 offset:38912\n"
        "v_add_f32 v32, v32, v36\n"
        "v_add_f32 v33, v33, v37\n"
        "v_add_f32 v34, v32, v33\n"
        "v_add_f32 v33, v32, v33\n"
        "s_waitcnt lgkmcnt(2)\n"
        "v_mfma_f32_32x32x16_bf16 v[0:15], v[176:179], v[20:23], v[0:15]\n"
        "ds_read_b128 v[20:23], v59 offset:39936\n"
        "v_permlane32_swap_b32 v34, v33\n"
        "v_mul_f32 v32, 0xbb000000, v34\n"
        "v_mul_f32 v34, v32, v32\n"
        "v_mov_b32 v35, 0x3b000000\n"
        "s_waitcnt lgkmcnt(2)\n"
        "v_mfma_f32_32x32x16_bf16 v[0:15], v[180:183], v[24:27], v[0:15]\n"
        "ds_read_b128 v[24:27], v59 offset:40960\n"
        "v_fma_f32 v33, v33, v35, -v34\n"
        "v_max_f32 v33, 0, v33\n"
        "v_add_f32 v33, 0x3727c5ac, v33\n"
        "v_rsq_f32 v60, v33\n"
        "s_waitcnt lgkmcnt(2)\n"
        "v_mfma_f32_32x32x16_bf16 v[0:15], v[184:187], v[16:19], v[0:15]\n"
        "ds_read_b128 v[16:19], v59 offset:41984\n"
        "v_mul_f32 v33, v33, v60\n"
        "v_sub_f32 v60, 0, v60\n"
        "v_cvt_pk_bf16_f32 v40, v32, v33\n"
        "v_lshlrev_b32 v34, 16, v40\n"
        "s_waitcnt lgkmcnt(2)\n"
        "v_mfma_f32_32x32x16_bf16 v[0:15], v[188:191], v[20:23], v[0:15]\n"
        "ds_read_b128 v[20:23], v59 offset:43008\n"
        "v_and_b32 v35, 0xffff0000, v40\n"
        "v_sub_f32 v32, v32, v34\n"
        "v_sub_f32 v33, v33, v35\n"
        "v_cvt_pk_bf16_f32 v41, v32, v33\n"
        "s_waitcnt lgkmcnt(2)\n"
        "v_mfma_f32_32x32x16_bf16 v[0:15], v[192:195], v[24:27], v[0:15]\n"
        "ds_read_b128 v[24:27], v59 offset:44032\n"
        "s_waitcnt lgkmcnt(2)\n"
        "v_mfma_f32_32x32x16_bf16 v[0:15], v[196:199], v[16:19], v[0:15]\n"
        "ds_read_b128 v[16:19], v59 offset:45056\n"
        "s_barrier\n"
        "s_waitcnt lgkmcnt(2)\n"
        "v_mfma_f32_32x32x16_bf16 v[0:15], v[200:203], v[20:23], v[0:15]\n"
        "ds_read_b128 v[20:23], v59 offset:46080\n"
        "s_waitcnt lgkmcnt(2)\n"
        "v_mfma_f32_32x32x16_bf16 v[0:15], v[204:207], v[24:27], v[0:15]\n"
        "ds_read_b128 v[24:27], v59 offset:47104\n"
        "s_waitcnt lgkmcnt(2)\n"
        "v_mfma_f32_32x32x16_bf16 v[0:15], v[208:211], v[16:19], v[0:15]\n"
        "ds_read_b128 v[16:19], v59 offset:48128\n"
        "s_waitcnt lgkmcnt(2)\n"
        "v_mfma_f32_32x32x16_bf16 v[0:15], v[212:215], v[20:23], v[0:15]\n"
        "ds_read_b128 v[20:23], v59 offset:49152\n"
        "s_waitcnt lgkmcnt(2)\n"
        "v_mfma_f32_32x32x16_bf16 v[0:15], v[216:219], v[24:27], v[0:15]\n"
        "ds_read_b128 v[24:27], v59 offset:50176\n"
        "s_waitcnt lgkmcnt(2)\n"
        "v_mfma_f32_32x32x16_bf16 v[0:15], v[220:223], v[16:19], v[0:15]\n"
        "ds_read_b128 v[16:19], v59 offset:51200\n"
        "buffer_load_dwordx4 v[168:171], v53, s[36:39], s49 offen offset:0 sc1\n"
        "s_waitcnt lgkmcnt(2)\n"
        "v_mfma_f32_32x32x16_bf16 v[0:15], v[224:227], v[20:23], v[0:15]\n"
        "ds_read_b128 v[20:23], v59 offset:52224\n"
        "ds_read_b128 v[32:35], v58 offset:33792\n"
        "s_waitcnt lgkmcnt(3)\n"
        "v_mfma_f32_32x32x16_bf16 v[0:15], v[228:231], v[24:27], v[0:15]\n"
        "ds_read_b128 v[24:27], v59 offset:53248\n"
        "buffer_load_dwordx4 v[172:175], v53, s[36:39], s50 offen offset:0 sc1\n"
        "ds_read_b128 v[36:39], v58 offset:34816\n"
        "s_waitcnt lgkmcnt(4)\n"
        "v_mfma_f32_32x32x16_bf16 v[0:15], v[232:235], v[16:19], v[0:15]\n"
        "ds_read_b128 v[16:19], v59 offset:54272\n"
        "ds_read_b128 v[28:31], v58 offset:32768\n"
        "s_waitcnt lgkmcnt(5)\n"
        "v_mfma_f32_32x32x16_bf16 v[0:15], v[236:239], v[20:23], v[0:15]\n"
        "ds_read_b128 v[20:23], v59 offset:55296\n"
        "buffer_load_dwordx4 v[176:179], v53, s[36:39], s51 offen offset:0 sc1\n"
        "s_waitcnt lgkmcnt(5)\n"
        "v_mul_f32 v32, v32, v60\n"
        "v_mul_f32 v33, v33, v60\n"
        "v_mul_f32 v34, v34, v60\n"
        "v_mul_f32 v35, v35, v60\n"
        "s_waitcnt lgkmcnt(4)\n"
        "v_mfma_f32_32x32x16_bf16 v[0:15], v[240:243], v[24:27], v[0:15]\n"
        "ds_read_b128 v[24:27], v59 offset:56320\n"
        "s_waitcnt lgkmcnt(4)\n"
        "v_mul_f32 v36, v36, v60\n"
        "v_mul_f32 v37, v37, v60\n"
        "v_mul_f32 v38, v38, v60\n"
        "v_mul_f32 v39, v39, v60\n"
        "s_waitcnt lgkmcnt(3)\n"
        "v_mfma_f32_32x32x16_bf16 v[0:15], v[244:247], v[16:19], v[0:15]\n"
        "buffer_load_dwordx4 v[180:183], v53, s[36:39], s52 offen offset:0 sc1\n"
        "ds_read_b128 v[16:19], v58 offset:35840\n"
        "s_waitcnt lgkmcnt(2)\n"
        "v_mfma_f32_32x32x16_bf16 v[0:15], v[248:251], v[20:23], v[0:15]\n"
        "v_mul_f32 v28, v28, v60\n"
        "v_mul_f32 v29, v29, v60\n"
        "v_mul_f32 v30, v30, v60\n"
        "v_mul_f32 v31, v31, v60\n"
        "s_waitcnt lgkmcnt(1)\n"
        "v_mfma_f32_32x32x16_bf16 v[0:15], v[252:255], v[24:27], v[0:15]\n"
        "s_waitcnt lgkmcnt(0)\n"
        "v_mul_f32 v16, v16, v60\n"
        "v_mul_f32 v17, v17, v60\n"
        "v_mul_f32 v18, v18, v60\n"
        "v_mul_f32 v19, v19, v60\n"
        "v_mfma_f32_32x32x8bf16_1k v[0:15], v[54:55], v[40:41], v[0:15]\n"
        "s_nop 11\n"
        "v_cmp_u_f32 vcc, v0, v28\n"
        "s_cbranch_vccnz f1slow_136\n"
        "go_137:\n"
        "s_nop 10\n"
        "v_pk_fma_f32 v[0:1], v[0:1], v[60:61], v[28:29] op_sel_hi:[1,0,1]\n"
        "v_pk_fma_f32 v[2:3], v[2:3], v[60:61], v[30:31] op_sel_hi:[1,0,1]\n"
        "v_pk_fma_f32 v[4:5], v[4:5], v[60:61], v[32:33] op_sel_hi:[1,0,1]\n"
        "v_pk_fma_f32 v[6:7], v[6:7], v[60:61], v[34:35] op_sel_hi:[1,0,1]\n"
        "v_exp_f32 v20, v0\n"
        "v_exp_f32 v21, v1\n"
        "v_exp_f32 v22, v2\n"
        "v_exp_f32 v23, v3\n"
        "v_exp_f32 v24, v4\n"
        "v_exp_f32 v25, v5\n"
        "v_exp_f32 v26, v6\n"
        "v_exp_f32 v27, v7\n"
        "v_pk_add_f32 v[20:21], v[20:21], 1.0 op_sel_hi:[1,0]\n"
        "v_pk_add_f32 v[22:23], v[22:23], 1.0 op_sel_hi:[1,0]\n"
        "v_pk_add_f32 v[24:25], v[24:25], 1.0 op_sel_hi:[1,0]\n"
        "v_pk_add_f32 v[26:27], v[26:27], 1.0 op_sel_hi:[1,0]\n"
        "v_rcp_f32 v20, v20\n"
        "v_rcp_f32 v21, v21\n"
        "v_rcp_f32 v22, v22\n"
        "v_rcp_f32 v23, v23\n"
        "v_rcp_f32 v24, v24\n"
        "v_rcp_f32 v25, v25\n"
        "v_rcp_f32 v26, v26\n"
        "v_rcp_f32 v27, v27\n"
        "v_pk_mul_f32 v[0:1], v[0:1], v[20:21]\n"
        "v_pk_mul_f32 v[2:3], v[2:3], v[22:23]\n"
        "v_pk_mul_f32 v[4:5], v[4:5], v[24:25]\n"
        "v_pk_mul_f32 v[6:7], v[6:7], v[26:27]\n"
        "v_cvt_pk_bf16_f32 v204, v0, v1\n"
        "v_cvt_pk_bf16_f32 v205, v2, v3\n"
        "v_cvt_pk_bf16_f32 v206, v4, v5\n"
        "v_cvt_pk_bf16_f32 v207, v6, v7\n"
        "v_min_u32 v204, -2, v204\n"
        "v_min_u32 v205, -2, v205\n"
        "v_min_u32 v206, -2, v206\n"
        "v_min_u32 v207, -2, v207\n"
        "buffer_store_dwordx4 v[204:207], v53, s[36:39], s59 offen offset:0 sc1\n"
        "v_pk_fma_f32 v[8:9], v[8:9], v[60:61], v[36:37] op_sel_hi:[1,0,1]\n"
        "v_pk_fma_f32 v[10:11], v[10:11], v[60:61], v[38:39] op_sel_hi:[1,0,1]\n"
        "v_pk_fma_f32 v[12:13], v[12:13], v[60:61], v[16:17] op_sel_hi:[1,0,1]\n"
        "v_pk_fma_f32 v[14:15], v[14:15], v[60:61], v[18:19] op_sel_hi:[1,0,1]\n"
        "v_exp_f32 v20, v8\n"
        "v_exp_f32 v21, v9\n"
        "v_exp_f32 v22, v10\n"
        "v_exp_f32 v23, v11\n"
        "v_exp_f32 v24, v12\n"
        "v_exp_f32 v25, v13\n"
        "v_exp_f32 v26, v14\n"
        "v_exp_f32 v27, v15\n"
        "v_pk_add_f32 v[20:21], v[20:21], 1.0 op_sel_hi:[1,0]\n"
        "v_pk_add_f32 v[22:23], v[22:23], 1.0 op_sel_hi:[1,0]\n"
        "v_pk_add_f32 v[24:25], v[24:25], 1.0 op_sel_hi:[1,0]\n"
        "v_pk_add_f32 v[26:27], v[26:27], 1.0 op_sel_hi:[1,0]\n"
        "v_rcp_f32 v20, v20\n"
        "v_rcp_f32 v21, v21\n"
        "v_rcp_f32 v22, v22\n"
        "v_rcp_f32 v23, v23\n"
        "v_rcp_f32 v24, v24\n"
        "v_rcp_f32 v25, v25\n"
        "v_rcp_f32 v26, v26\n"
        "v_rcp_f32 v27, v27\n"
        "v_pk_mul_f32 v[8:9], v[8:9], v[20:21]\n"
        "v_pk_mul_f32 v[10:11], v[10:11], v[22:23]\n"
        "v_pk_mul_f32 v[12:13], v[12:13], v[24:25]\n"
        "v_pk_mul_f32 v[14:15], v[14:15], v[26:27]\n"
        "v_cvt_pk_bf16_f32 v208, v8, v9\n"
        "v_cvt_pk_bf16_f32 v209, v10, v11\n"
        "v_cvt_pk_bf16_f32 v210, v12, v13\n"
        "v_cvt_pk_bf16_f32 v211, v14, v15\n"
        "v_min_u32 v208, -2, v208\n"
        "v_min_u32 v209, -2, v209\n"
        "v_min_u32 v210, -2, v210\n"
        "v_min_u32 v211, -2, v211\n"
        "buffer_store_dwordx4 v[208:211], v53, s[36:39], s59 offen offset:1024 sc1\n"
        "s_setprio 0\n"
        "s_barrier\n"
        "buffer_load_dwordx4 v[184:187], v53, s[36:39], s53 offen offset:0 sc1\n"
        "buffer_load_dwordx4 v[188:191], v53, s[36:39], s54 offen offset:0 sc1\n"
        "buffer_load_dwordx4 v[192:195], v53, s[36:39], s55 offen offset:0 sc1\n"
        "buffer_load_dwordx4 v[196:199], v53, s[36:39], s56 offen offset:0 sc1\n"
        "buffer_load_dwordx4 v[200:203], v53, s[36:39], s57 offen offset:0 sc1\n"
        "v_add_u32 v54, s70, v53\n"
        "v_add_u32 v55, 65536, v54\n"
        "s_add_u32 s88, s71, 139264\n"
        "v_add_u32 v59, s88, v53\n"
        "v_lshrrev_b32 v32, 8, v53\n"
        "v_and_b32 v33, 0xf0, v53\n"
        "v_lshl_or_b32 v56, v32, 9, v33\n"
        "s_add_u32 s88, s71, 8192\n"
        "v_add_u32 v56, s88, v56\n"
        "v_lshlrev_b32 v33, 6, v33\n"
        "v_lshl_or_b32 v58, v32, 3, v33\n"
        "v_lshl_add_u32 v32, v32, 4, s69\n"
        "ds_read_b128 v[248:251], v32 offset:512\n"
        "ds_write_b128 v59, v[152:155]\n"
        "s_setprio 1\n"
        "v_mfma_f32_32x32x16_bf16 v[0:15], v[96:99], v[44:47], 0\n"
        "v_mfma_f32_32x32x16_bf16 v[16:31], v[112:115], v[44:47], 0\n"
        "v_mfma_f32_32x32x16_bf16 v[0:15], v[100:103], v[48:51], v[0:15]\n"
        "v_mfma_f32_32x32x16_bf16 v[16:31], v[116:119], v[48:51], v[16:31]\n"
        "s_waitcnt vmcnt(10)\n"
        "v_mfma_f32_32x32x16_bf16 v[0:15], v[104:107], v[168:171], v[0:15]\n"
        "v_max3_u32 v36, v168, v169, v170\n"
        "v_max_u32 v36, v36, v171\n"
        "v_mfma_f32_32x32x16_bf16 v[16:31], v[120:123], v[168:171], v[16:31]\n"
        "s_waitcnt vmcnt(9)\n"
        "v_mfma_f32_32x32x16_bf16 v[0:15], v[108:111], v[172:175], v[0:15]\n"
        "v_max3_u32 v36, v36, v172, v173\n"
        "v_max3_u32 v36, v36, v174, v175\n"
        "v_mfma_f32_32x32x16_bf16 v[16:31], v[124:127], v[172:175], v[16:31]\n"
        "s_waitcnt vmcnt(8)\n"
        "v_mfma_f32_32x32x16_bf16 v[0:15], v[128:131], v[176:179], v[0:15]\n"
        "v_max3_u32 v36, v36, v176, v177\n"
        "v_max3_u32 v36, v36, v178, v179\n"
        "v_mfma_f32_32x32x16_bf16 v[16:31], v[132:135], v[176:179], v[16:31]\n"
        "s_waitcnt vmcnt(7)\n"
        "v_mfma_f32_32x32x16_bf16 v[0:15], v[136:139], v[180:183], v[0:15]\n"
        "v_max3_u32 v36, v36, v180, v181\n"
        "v_max3_u32 v36, v36, v182, v183\n"
        "v_mfma_f32_32x32x16_bf16 v[16:31], v[140:143], v[180:183], v[16:31]\n"
        "s_waitcnt vmcnt(4)\n"
        "v_mfma_f32_32x32x16_bf16 v[0:15], v[144:147], v[184:187], v[0:15]\n"
        "v_max3_u32 v36, v36, v184, v185\n"
        "v_max3_u32 v36, v36, v186, v187\n"
        "v_mfma_f32_32x32x16_bf16 v[16:31], v[148:151], v[184:187], v[16:31]\n"
        "s_waitcnt vmcnt(3)\n"
        "v_mfma_f32_32x32x16_bf16 v[0:15], v[64:67], v[188:191], v[0:15]\n"
        "v_max3_u32 v36, v36, v188, v189\n"
        "v_max3_u32 v36, v36, v190, v191\n"
        "v_mfma_f32_32x32x16_bf16 v[16:31], v[68:71], v[188:191], v[16:31]\n"
        "buffer_load_dwordx4 v[212:215], v53, s[40:43], s49 offen offset:0 sc1\n"
        "s_waitcnt vmcnt(3)\n"
        "v_mfma_f32_32x32x16_bf16 v[0:15], v[72:75], v[192:195], v[0:15]\n"
        "v_max3_u32 v36, v36, v192, v193\n"
        "v_max3_u32 v36, v36, v194, v195\n"
        "v_mfma_f32_32x32x16_bf16 v[16:31], v[76:79], v[192:195], v[16:31]\n"
        "buffer_load_dwordx4 v[216:219], v53, s[40:43], s50 offen offset:0 sc1\n"
        "s_waitcnt vmcnt(3)\n"
        "v_mfma_f32_32x32x16_bf16 v[0:15], v[80:83], v[196:199], v[0:15]\n"
        "v_max3_u32 v36, v36, v196, v197\n"
        "v_max3_u32 v36, v36, v198, v199\n"
        "v_mfma_f32_32x32x16_bf16 v[16:31], v[84:87], v[196:199], v[16:31]\n"
        "buffer_load_dwordx4 v[220:223], v53, s[40:43], s51 offen offset:0 sc1\n"
        "s_waitcnt vmcnt(3)\n"
        "v_mfma_f32_32x32x16_bf16 v[0:15], v[88:91], v[200:203], v[0:15]\n"
        "v_max3_u32 v36, v36, v200, v201\n"
        "v_max3_u32 v36, v36, v202, v203\n"
        "v_mfma_f32_32x32x16_bf16 v[16:31], v[92:95], v[200:203], v[16:31]\n"
        "buffer_load_dwordx4 v[224:227], v53, s[40:43], s52 offen offset:0 sc1\n"
        "chk_138:\n"
        "v_cmp_eq_u32 vcc, -1, v36\n"
        "s_cbranch_vccnz rh0_140\n"
        "go_139:\n"
        "s_setprio 0\n"
        "buffer_load_dwordx4 v[228:231], v53, s[40:43], s53 offen offset:0 sc1\n"
        "buffer_load_dwordx4 v[232:235], v53, s[40:43], s54 offen offset:0 sc1\n"
        "buffer_load_dwordx4 v[236:239], v53, s[40:43], s55 offen offset:0 sc1\n"
        "buffer_load_dwordx4 v[240:243], v53, s[40:43], s56 offen offset:0 sc1\n"
        "buffer_load_dwordx4 v[244:247], v53, s[40:43], s57 offen offset:0 sc1\n"
        "ds_write_b128 v54, v[0:3] offset:0\n"
        "ds_write_b128 v54, v[4:7] offset:1024\n"
        "ds_write_b128 v54, v[8:11] offset:2048\n"
        "ds_write_b128 v54, v[12:15] offset:3072\n"
        "ds_write_b128 v54, v[16:19] offset:4096\n"
        "ds_write_b128 v54, v[20:23] offset:5120\n"
        "v_mfma_f32_32x32x16_bf16 v[160:175], v[96:99], v[204:207], 0\n"
        "v_mfma_f32_32x32x16_bf16 v[176:191], v[112:115], v[204:207], 0\n"
        "ds_write_b128 v54, v[24:27] offset:6144\n"
        "v_mfma_f32_32x32x16_bf16 v[160:175], v[100:103], v[208:211], v[160:175]\n"
        "v_mfma_f32_32x32x16_bf16 v[176:191], v[116:119], v[208:211], v[176:191]\n"
        "ds_write_b128 v54, v[28:31] offset:7168\n"
        "s_waitcnt vmcnt(8)\n"
        "v_mfma_f32_32x32x16_bf16 v[160:175], v[104:107], v[212:215], v[160:175]\n"
        "v_max3_u32 v36, v212, v213, v214\n"
        "v_max_u32 v36, v36, v215\n"
        "v_mfma_f32_32x32x16_bf16 v[176:191], v[120:123], v[212:215], v[176:191]\n"
        "s_waitcnt vmcnt(7)\n"
        "v_mfma_f32_32x32x16_bf16 v[160:175], v[108:111], v[216:219], v[160:175]\n"
        "v_max3_u32 v36, v36, v216, v217\n"
        "v_max3_u32 v36, v36, v218, v219\n"
        "v_mfma_f32_32x32x16_bf16 v[176:191], v[124:127], v[216:219], v[176:191]\n"
        "s_waitcnt lgkmcnt(0)\n"
        "s_barrier\n"
        "s_setprio 1\n"
        "s_waitcnt vmcnt(6)\n"
        "v_mfma_f32_32x32x16_bf16 v[160:175], v[128:131], v[220:223], v[160:175]\n"
        "v_max3_u32 v36, v36, v220, v221\n"
        "v_max3_u32 v36, v36, v222, v223\n"
        "v_mfma_f32_32x32x16_bf16 v[176:191], v[132:135], v[220:223], v[176:191]\n"
        "s_waitcnt vmcnt(5)\n"
        "v_mfma_f32_32x32x16_bf16 v[160:175], v[136:139], v[224:227], v[160:175]\n"
        "v_max3_u32 v36, v36, v224, v225\n"
        "v_max3_u32 v36, v36, v226, v227\n"
        "v_mfma_f32_32x32x16_bf16 v[176:191], v[140:143], v[224:227], v[176:191]\n"
        "s_waitcnt vmcnt(4)\n"
        "v_mfma_f32_32x32x16_bf16 v[160:175], v[144:147], v[228:231], v[160:175]\n"
        "v_max3_u32 v36, v36, v228, v229\n"
        "v_max3_u32 v36, v36, v230, v231\n"
        "v_mfma_f32_32x32x16_bf16 v[176:191], v[148:151], v[228:231], v[176:191]\n"
        "s_waitcnt vmcnt(3)\n"
        "v_mfma_f32_32x32x16_bf16 v[160:175], v[64:67], v[232:235], v[160:175]\n"
        "v_max3_u32 v36, v36, v232, v233\n"
        "v_max3_u32 v36, v36, v234, v235\n"
        "page8:\n"
        "s_cbranch_execz warm_back\n"
        "v_mfma_f32_32x32x16_bf16 v[176:191], v[68:71], v[232:235], v[176:191]\n"
        "ds_read_b128 v[0:3], v56 offset:256\n"
        "ds_read_b128 v[4:7], v56 offset:8448\n"
        "s_waitcnt vmcnt(2)\n"
        "v_mfma_f32_32x32x16_bf16 v[160:175], v[72:75], v[236:239], v[160:175]\n"
        "v_max3_u32 v36, v36, v236, v237\n"
        "v_max3_u32 v36, v36, v238, v239\n"
        "v_mfma_f32_32x32x16_bf16 v[176:191], v[76:79], v[236:239], v[176:191]\n"
        "ds_read_b128 v[8:11], v56 offset:16640\n"
        "ds_read_b128 v[12:15], v56 offset:24832\n"
        "s_waitcnt vmcnt(1)\n"
        "v_mfma_f32_32x32x16_bf16 v[160:175], v[80:83], v[240:243], v[160:175]\n"
        "v_max3_u32 v36, v36, v240, v241\n"
        "v_max3_u32 v36, v36, v242, v243\n"
        "v_mfma_f32_32x32x16_bf16 v[176:191], v[84:87], v[240:243], v[176:191]\n"
        "ds_read_b128 v[16:19], v56 offset:33024\n"
        "ds_read_b128 v[20:23], v56 offset:41216\n"
        "s_waitcnt vmcnt(0)\n"
        "v_mfma_f32_32x32x16_bf16 v[160:175], v[88:91], v[244:247], v[160:175]\n"
        "v_max3_u32 v36, v36, v244, v245\n"
        "v_max3_u32 v36, v36, v246, v247\n"
        "v_mfma_f32_32x32x16_bf16 v[176:191], v[92:95], v[244:247], v[176:191]\n"
        "ds_read_b128 v[24:27], v56 offset:49408\n"
        "ds_read_b128 v[28:31], v56 offset:57600\n"
        "chk_141:\n"
        "v_cmp_eq_u32 vcc, -1, v36\n"
        "s_cbranch_vccnz rh1_143\n"
        "go_142:\n"
        "s_nop 4\n"
        "ds_write_b128 v55, v[160:163] offset:0\n"
        "ds_write_b128 v55, v[164:167] offset:1024\n"
        "ds_write_b128 v55, v[168:171] offset:2048\n"
        "ds_write_b128 v55, v[172:175] offset:3072\n"
        "ds_write_b128 v55, v[176:179] offset:4096\n"
        "ds_write_b128 v55, v[180:183] offset:5120\n"
        "ds_write_b128 v55, v[184:187] offset:6144\n"
        "ds_write_b128 v55, v[188:191] offset:7168\n"
        "s_waitcnt lgkmcnt(0)\n"
        "s_barrier\n"
        "s_setprio 0\n"
        "v_pk_add_f32 v[0:1], v[0:1], v[4:5]\n"
        "v_pk_add_f32 v[2:3], v[2:3], v[6:7]\n"
        "v_pk_add_f32 v[8:9], v[8:9], v[12:13]\n"
        "v_pk_add_f32 v[10:11], v[10:11], v[14:15]\n"
        "v_pk_add_f32 v[0:1], v[0:1], v[8:9]\n"
        "v_pk_add_f32 v[2:3], v[2:3], v[10:11]\n"
        "v_pk_add_f32 v[16:17], v[16:17], v[20:21]\n"
        "v_pk_add_f32 v[18:19], v[18:19], v[22:23]\n"
        "v_pk_add_f32 v[24:25], v[24:25], v[28:29]\n"
        "v_pk_add_f32 v[26:27], v[26:27], v[30:31]\n"
        "v_pk_add_f32 v[16:17], v[16:17], v[24:25]\n"
        "v_pk_add_f32 v[18:19], v[18:19], v[26:27]\n"
        "v_pk_add_f32 v[0:1], v[0:1], v[16:17]\n"
        "v_pk_add_f32 v[2:3], v[2:3], v[18:19]\n"
        "v_fmac_f32 v156, v0, v248\n"
        "v_fmac_f32 v157, v1, v249\n"
        "v_fmac_f32 v158, v2, v250\n"
        "v_fmac_f32 v159, v3, v251\n"
        "v_cvt_pk_bf16_f32 v0, v156, v157\n"
        "v_cvt_pk_bf16_f32 v1, v158, v159\n"
        "buffer_store_dwordx2 v[0:1], v58, s[28:31], s85 offen\n"
        "end_72:\n"
        "s_endpgm\n"
        "fallback_73:\n"
        "s_waitcnt vmcnt(0) lgkmcnt(0)\n"
        "v_bfe_u32 v28, v53, 4, 4\n"
        "v_and_b32 v28, 11, v28\n"
        "v_cmp_eq_u32 vcc, 0, v28\n"
        "v_mov_b32 v28, s94\n"
        "v_lshrrev_b32 v54, 9, v53\n"
        "v_cndmask_b32 v20, 0, v28, vcc\n"
        "v_and_b32 v28, 0xf0, v53\n"
        "v_lshl_or_b32 v54, v54, 8, v28\n"
        "v_and_b32 v55, 0x1ff, v53\n"
        "v_mov_b32 v21, v20\n"
        "v_mov_b32 v22, v20\n"
        "v_mov_b32 v23, v20\n"
        "v_mov_b32 v24, v20\n"
        "v_mov_b32 v25, v20\n"
        "v_mov_b32 v26, v20\n"
        "v_mov_b32 v27, v20\n"
        "fbitem_144:\n"
        "s_ff1_i32_b32 s88, s47\n"
        "s_bitset0_b32 s47, s88\n"
        "s_lshl_b32 s89, s88, 1\n"
        "s_bfe_u32 s90, s3, 0x10001\n"
        "s_add_u32 s89, s89, s90\n"
        "s_lshl_b32 s89, s89, 10\n"
        "v_add_u32 v28, s89, v53\n"
        "ds_read_b128 v[48:51], v28 offset:0\n"
        "v_mov_b32 v29, 0xff7fffff\n"
        "s_mov_b32 s91, 32768\n"
        "v_mov_b64 v[32:33], 0\n"
        "v_mov_b64 v[34:35], 0\n"
        "v_mov_b64 v[36:37], 0\n"
        "v_mov_b64 v[38:39], 0\n"
        "v_mov_b64 v[40:41], 0\n"
        "v_mov_b64 v[42:43], 0\n"
        "v_mov_b64 v[44:45], 0\n"
        "v_mov_b64 v[46:47], 0\n"
        "fbloop_145:\n"
        "buffer_load_dwordx4 v[16:19], v53, s[32:35], s91 offen sc1\n"
        "s_mov_b32 exec_lo, 0xffff\n"
        "s_mov_b32 exec_hi, 0xffff\n"
        "buffer_load_dwordx4 v[20:23], v54, s[32:35], s91 offen offset:1024 sc1\n"
        "buffer_load_dwordx4 v[24:27], v54, s[32:35], s91 offen offset:1536 sc1\n"
        "s_mov_b64 exec, -1\n"
        "chk_146:\n"
        "s_waitcnt vmcnt(2)\n"
        "v_max_u32 v30, v16, v17\n"
        "v_max3_u32 v30, v30, v18, v19\n"
        "s_waitcnt vmcnt(1)\n"
        "v_max3_u32 v30, v30, v20, v21\n"
        "v_max3_u32 v30, v30, v22, v23\n"
        "s_waitcnt vmcnt(0)\n"
        "v_max3_u32 v30, v30, v24, v25\n"
        "v_max3_u32 v30, v30, v26, v27\n"
        "v_cmp_eq_u32 vcc, -1, v30\n"
        "s_cbranch_vccnz rfb_148\n"
        "go_147:\n"
        "s_waitcnt lgkmcnt(0)\n"
        "v_mfma_f32_32x32x16_bf16 v[0:15], v[16:19], v[48:51], 0\n"
        "s_nop 11\n"
        "v_max3_f32 v30, v29, v0, v1\n"
        "v_max3_f32 v30, v30, v2, v3\n"
        "v_max3_f32 v30, v30, v4, v5\n"
        "v_max3_f32 v30, v30, v6, v7\n"
        "v_max3_f32 v30, v30, v8, v9\n"
        "v_max3_f32 v30, v30, v10, v11\n"
        "v_max3_f32 v30, v30, v12, v13\n"
        "v_max3_f32 v30, v30, v14, v15\n"
        "v_mov_b32 v31, v30\n"
        "s_nop 1\n"
        "v_permlane32_swap_b32 v30, v31\n"
        "v_max_f32 v30, v30, v31\n"
        "v_sub_f32 v31, v29, v30\n"
        "v_exp_f32 v31, v31\n"
        "v_mov_b32 v29, v30\n"
        "v_sub_f32 v0, v0, v29\n"
        "v_sub_f32 v1, v1, v29\n"
        "v_sub_f32 v2, v2, v29\n"
        "v_sub_f32 v3, v3, v29\n"
        "v_sub_f32 v4, v4, v29\n"
        "v_sub_f32 v5, v5, v29\n"
        "v_sub_f32 v6, v6, v29\n"
        "v_sub_f32 v7, v7, v29\n"
        "v_sub_f32 v8, v8, v29\n"
        "v_sub_f32 v9, v9, v29\n"
        "v_sub_f32 v10, v10, v29\n"
        "v_sub_f32 v11, v11, v29\n"
        "v_sub_f32 v12, v12, v29\n"
        "v_sub_f32 v13, v13, v29\n"
        "v_sub_f32 v14, v14, v29\n"
        "v_sub_f32 v15, v15, v29\n"
        "v_mul_f32 v32, v32, v31\n"
        "v_mul_f32 v33, v33, v31\n"
        "v_mul_f32 v34, v34, v31\n"
        "v_mul_f32 v35, v35, v31\n"
        "v_mul_f32 v36, v36, v31\n"
        "v_mul_f32 v37, v37, v31\n"
        "v_mul_f32 v38, v38, v31\n"
        "v_mul_f32 v39, v39, v31\n"
        "v_mul_f32 v40, v40, v31\n"
        "v_exp_f32 v0, v0\n"
        "v_exp_f32 v1, v1\n"
        "v_exp_f32 v2, v2\n"
        "v_exp_f32 v3, v3\n"
        "v_exp_f32 v4, v4\n"
        "v_exp_f32 v5, v5\n"
        "v_exp_f32 v6, v6\n"
        "v_exp_f32 v7, v7\n"
        "v_exp_f32 v8, v8\n"
        "v_exp_f32 v9, v9\n"
        "v_exp_f32 v10, v10\n"
        "v_exp_f32 v11, v11\n"
        "v_exp_f32 v12, v12\n"
        "v_exp_f32 v13, v13\n"
        "v_exp_f32 v14, v14\n"
        "v_exp_f32 v15, v15\n"
        "v_smfmac_f32_32x32x32_bf16 v[32:47], v[20:23], v[0:7], v52\n"
        "s_nop 0\n"
        "v_smfmac_f32_32x32x32_bf16 v[32:47], v[24:27], v[8:15], v52\n"
        "s_add_u32 s91, s91, 2048\n"
        "s_cmp_lt_u32 s91, 163840\n"
        "s_cbranch_scc1 fbloop_145\n"
        "s_nop 8\n"
        "v_rcp_f32 v30, v40\n"
        "s_lshr_b32 s89, s88, 1\n"
        "s_lshl_b32 s89, s89, 15\n"
        "s_lshr_b32 s90, s88, 1\n"
        "s_xor_b32 s90, s90, s88\n"
        "s_and_b32 s90, s90, 1\n"
        "s_lshl_b32 s90, s90, 11\n"
        "s_add_u32 s89, s89, s90\n"
        "s_add_u32 s89, s89, s72\n"
        "v_mul_f32 v32, v32, v30\n"
        "v_mul_f32 v33, v33, v30\n"
        "v_mul_f32 v34, v34, v30\n"
        "v_mul_f32 v35, v35, v30\n"
        "v_mul_f32 v36, v36, v30\n"
        "v_mul_f32 v37, v37, v30\n"
        "v_mul_f32 v38, v38, v30\n"
        "v_mul_f32 v39, v39, v30\n"
        "v_cvt_pk_bf16_f32 v16, v32, v33\n"
        "v_cvt_pk_bf16_f32 v17, v34, v35\n"
        "v_cvt_pk_bf16_f32 v18, v36, v37\n"
        "v_cvt_pk_bf16_f32 v19, v38, v39\n"
        "v_min_u32 v16, -2, v16\n"
        "v_min_u32 v17, -2, v17\n"
        "v_min_u32 v18, -2, v18\n"
        "v_min_u32 v19, -2, v19\n"
        "s_bitcmp1_b32 s3, 0\n"
        "s_cselect_b32 exec_lo, 0, -1\n"
        "s_cselect_b32 exec_hi, -1, 0\n"
        "buffer_store_dwordx4 v[16:19], v55, s[36:39], s89 offen sc1\n"
        "s_mov_b64 exec, -1\n"
        "s_cmp_lg_u32 s47, 0\n"
        "s_cbranch_scc1 fbitem_144\n"
        "v_add_u32 v48, 160768, v53\n"
        "ds_read_b128 v[48:51], v48\n"
        "s_branch fbret_74\n"
        "woslow_84:\n"
        "s_waitcnt vmcnt(0) lgkmcnt(0)\n"
        "s_add_u32 s88, s17, 8192\n"
        "s_mov_b32 s89, 34816\n"
        "v_mov_b64 v[0:1], 0\n"
        "v_mov_b64 v[2:3], 0\n"
        "v_mov_b64 v[4:5], 0\n"
        "v_mov_b64 v[6:7], 0\n"
        "slow_150:\n"
        "buffer_load_dwordx4 v[8:11], v53, s[32:35], s88 offen offset:0 sc1\n"
        "buffer_load_dwordx4 v[12:15], v48, s[36:39], s89 offen offset:0 sc1\n"
        "buffer_load_dwordx4 v[16:19], v48, s[36:39], s89 offen offset:256 sc1\n"
        "chk_151:\n"
        "s_waitcnt vmcnt(1)\n"
        "v_max_u32 v20, v12, v13\n"
        "v_max3_u32 v20, v20, v14, v15\n"
        "s_waitcnt vmcnt(0)\n"
        "v_max3_u32 v20, v20, v16, v17\n"
        "v_max3_u32 v20, v20, v18, v19\n"
        "v_cmp_eq_u32 vcc, -1, v20\n"
        "s_cbranch_vccnz rsw_153\n"
        "go_152:\n"
        "v_mfma_f32_16x16x32_bf16 v[0:3], v[8:11], v[12:15], v[0:3]\n"
        "v_mfma_f32_16x16x32_bf16 v[4:7], v[8:11], v[16:19], v[4:7]\n"
        "s_add_u32 s88, s88, 1024\n"
        "s_add_u32 s89, s89, 4096\n"
        "s_cmp_lt_u32 s89, 67584\n"
        "s_cbranch_scc1 slow_150\n"
        "s_add_u32 s88, s17, 0\n"
        "s_mov_b32 s89, 32768\n"
        "slow_154:\n"
        "buffer_load_dwordx4 v[8:11], v53, s[32:35], s88 offen offset:0 sc1\n"
        "buffer_load_dwordx4 v[12:15], v48, s[36:39], s89 offen offset:0 sc1\n"
        "buffer_load_dwordx4 v[16:19], v48, s[36:39], s89 offen offset:256 sc1\n"
        "chk_155:\n"
        "s_waitcnt vmcnt(1)\n"
        "v_max_u32 v20, v12, v13\n"
        "v_max3_u32 v20, v20, v14, v15\n"
        "s_waitcnt vmcnt(0)\n"
        "v_max3_u32 v20, v20, v16, v17\n"
        "v_max3_u32 v20, v20, v18, v19\n"
        "v_cmp_eq_u32 vcc, -1, v20\n"
        "s_cbranch_vccnz rsw_157\n"
        "go_156:\n"
        "v_mfma_f32_16x16x32_bf16 v[0:3], v[8:11], v[12:15], v[0:3]\n"
        "v_mfma_f32_16x16x32_bf16 v[4:7], v[8:11], v[16:19], v[4:7]\n"
        "s_add_u32 s88, s88, 1024\n"
        "s_add_u32 s89, s89, 4096\n"
        "s_cmp_lt_u32 s89, 65536\n"
        "s_cbranch_scc1 slow_154\n"
        "s_branch go_85\n"
        "f1slow_90:\n"
        "s_waitcnt vmcnt(0) lgkmcnt(0)\n"
        "s_add_u32 s88, s26, 0\n"
        "s_mov_b32 s89, 65536\n"
        "v_mov_b64 v[0:1], 0\n"
        "v_mov_b64 v[2:3], 0\n"
        "v_mov_b64 v[4:5], 0\n"
        "v_mov_b64 v[6:7], 0\n"
        "v_mov_b64 v[8:9], 0\n"
        "v_mov_b64 v[10:11], 0\n"
        "v_mov_b64 v[12:13], 0\n"
        "v_mov_b64 v[14:15], 0\n"
        "slow_159:\n"
        "buffer_load_dwordx4 v[20:23], v53, s[32:35], s88 offen offset:0 sc1\n"
        "buffer_load_dwordx4 v[24:27], v53, s[36:39], s89 offen offset:0 sc1\n"
        "chk_160:\n"
        "s_waitcnt vmcnt(0)\n"
        "v_max_u32 v42, v24, v25\n"
        "v_max3_u32 v42, v42, v26, v27\n"
        "v_cmp_eq_u32 vcc, -1, v42\n"
        "s_cbranch_vccnz rsf_162\n"
        "go_161:\n"
        "v_mfma_f32_32x32x16_bf16 v[0:15], v[20:23], v[24:27], v[0:15]\n"
        "s_add_u32 s88, s88, 1024\n"
        "s_add_u32 s89, s89, 1024\n"
        "s_cmp_lt_u32 s89, 81920\n"
        "s_cbranch_scc1 slow_159\n"
        "s_bitcmp1_b32 s3, 0\n"
        "s_cbranch_scc1 odd_163\n"
        "v_mfma_f32_32x32x8bf16_1k v[0:15], v[54:55], v[40:41], v[0:15]\n"
        "s_nop 11\n"
        "v_mov_b32 v32, v0\n"
        "v_mov_b32 v33, v1\n"
        "v_mov_b32 v34, v2\n"
        "v_mov_b32 v35, v3\n"
        "v_mov_b32 v36, v4\n"
        "v_mov_b32 v37, v5\n"
        "v_mov_b32 v38, v6\n"
        "v_mov_b32 v39, v7\n"
        "s_branch par_164\n"
        "odd_163:\n"
        "s_nop 5\n"
        "v_mov_b32 v32, v8\n"
        "v_mov_b32 v33, v9\n"
        "v_mov_b32 v34, v10\n"
        "v_mov_b32 v35, v11\n"
        "v_mov_b32 v36, v12\n"
        "v_mov_b32 v37, v13\n"
        "v_mov_b32 v38, v14\n"
        "v_mov_b32 v39, v15\n"
        "par_164:\n"
        "s_add_u32 s88, s26, 16384\n"
        "s_mov_b32 s89, 81920\n"
        "v_mov_b64 v[0:1], 0\n"
        "v_mov_b64 v[2:3], 0\n"
        "v_mov_b64 v[4:5], 0\n"
        "v_mov_b64 v[6:7], 0\n"
        "v_mov_b64 v[8:9], 0\n"
        "v_mov_b64 v[10:11], 0\n"
        "v_mov_b64 v[12:13], 0\n"
        "v_mov_b64 v[14:15], 0\n"
        "slow_165:\n"
        "buffer_load_dwordx4 v[20:23], v53, s[32:35], s88 offen offset:0 sc1\n"
        "buffer_load_dwordx4 v[24:27], v53, s[36:39], s89 offen offset:0 sc1\n"
        "chk_166:\n"
        "s_waitcnt vmcnt(0)\n"
        "v_max_u32 v42, v24, v25\n"
        "v_max3_u32 v42, v42, v26, v27\n"
        "v_cmp_eq_u32 vcc, -1, v42\n"
        "s_cbranch_vccnz rsf_168\n"
        "go_167:\n"
        "v_mfma_f32_32x32x16_bf16 v[0:15], v[20:23], v[24:27], v[0:15]\n"
        "s_add_u32 s88, s88, 1024\n"
        "s_add_u32 s89, s89, 1024\n"
        "s_cmp_lt_u32 s89, 98304\n"
        "s_cbranch_scc1 slow_165\n"
        "s_bitcmp1_b32 s3, 0\n"
        "s_cbranch_scc1 odd_169\n"
        "s_nop 5\n"
        "v_add_f32 v0, v0, v32\n"
        "v_add_f32 v1, v1, v33\n"
        "v_add_f32 v2, v2, v34\n"
        "v_add_f32 v3, v3, v35\n"
        "v_add_f32 v4, v4, v36\n"
        "v_add_f32 v5, v5, v37\n"
        "v_add_f32 v6, v6, v38\n"
        "v_add_f32 v7, v7, v39\n"
        "s_branch par_170\n"
        "odd_169:\n"
        "v_mfma_f32_32x32x8bf16_1k v[0:15], v[54:55], v[40:41], v[0:15]\n"
        "s_nop 11\n"
        "v_add_f32 v0, v8, v32\n"
        "v_add_f32 v1, v9, v33\n"
        "v_add_f32 v2, v10, v34\n"
        "v_add_f32 v3, v11, v35\n"
        "v_add_f32 v4, v12, v36\n"
        "v_add_f32 v5, v13, v37\n"
        "v_add_f32 v6, v14, v38\n"
        "v_add_f32 v7, v15, v39\n"
        "par_170:\n"
        "s_branch go_91\n"
        "f1slow_96:\n"
        "s_waitcnt vmcnt(0) lgkmcnt(0)\n"
        "s_add_u32 s88, s26, 0\n"
        "s_mov_b32 s89, 98304\n"
        "v_mov_b64 v[0:1], 0\n"
        "v_mov_b64 v[2:3], 0\n"
        "v_mov_b64 v[4:5], 0\n"
        "v_mov_b64 v[6:7], 0\n"
        "v_mov_b64 v[8:9], 0\n"
        "v_mov_b64 v[10:11], 0\n"
        "v_mov_b64 v[12:13], 0\n"
        "v_mov_b64 v[14:15], 0\n"
        "slow_172:\n"
        "buffer_load_dwordx4 v[20:23], v53, s[32:35], s88 offen offset:0 sc1\n"
        "buffer_load_dwordx4 v[24:27], v53, s[36:39], s89 offen offset:0 sc1\n"
        "chk_173:\n"
        "s_waitcnt vmcnt(0)\n"
        "v_max_u32 v42, v24, v25\n"
        "v_max3_u32 v42, v42, v26, v27\n"
        "v_cmp_eq_u32 vcc, -1, v42\n"
        "s_cbranch_vccnz rsf_175\n"
        "go_174:\n"
        "v_mfma_f32_32x32x16_bf16 v[0:15], v[20:23], v[24:27], v[0:15]\n"
        "s_add_u32 s88, s88, 1024\n"
        "s_add_u32 s89, s89, 1024\n"
        "s_cmp_lt_u32 s89, 114688\n"
        "s_cbranch_scc1 slow_172\n"
        "s_bitcmp1_b32 s3, 0\n"
        "s_cbranch_scc1 odd_176\n"
        "v_mfma_f32_32x32x8bf16_1k v[0:15], v[54:55], v[40:41], v[0:15]\n"
        "s_nop 11\n"
        "v_mov_b32 v32, v0\n"
        "v_mov_b32 v33, v1\n"
        "v_mov_b32 v34, v2\n"
        "v_mov_b32 v35, v3\n"
        "v_mov_b32 v36, v4\n"
        "v_mov_b32 v37, v5\n"
        "v_mov_b32 v38, v6\n"
        "v_mov_b32 v39, v7\n"
        "s_branch par_177\n"
        "odd_176:\n"
        "s_nop 5\n"
        "v_mov_b32 v32, v8\n"
        "v_mov_b32 v33, v9\n"
        "v_mov_b32 v34, v10\n"
        "v_mov_b32 v35, v11\n"
        "v_mov_b32 v36, v12\n"
        "v_mov_b32 v37, v13\n"
        "v_mov_b32 v38, v14\n"
        "v_mov_b32 v39, v15\n"
        "par_177:\n"
        "s_add_u32 s88, s26, 16384\n"
        "s_mov_b32 s89, 114688\n"
        "v_mov_b64 v[0:1], 0\n"
        "v_mov_b64 v[2:3], 0\n"
        "v_mov_b64 v[4:5], 0\n"
        "v_mov_b64 v[6:7], 0\n"
        "v_mov_b64 v[8:9], 0\n"
        "v_mov_b64 v[10:11], 0\n"
        "v_mov_b64 v[12:13], 0\n"
        "v_mov_b64 v[14:15], 0\n"
        "slow_178:\n"
        "buffer_load_dwordx4 v[20:23], v53, s[32:35], s88 offen offset:0 sc1\n"
        "buffer_load_dwordx4 v[24:27], v53, s[36:39], s89 offen offset:0 sc1\n"
        "chk_179:\n"
        "s_waitcnt vmcnt(0)\n"
        "v_max_u32 v42, v24, v25\n"
        "v_max3_u32 v42, v42, v26, v27\n"
        "v_cmp_eq_u32 vcc, -1, v42\n"
        "s_cbranch_vccnz rsf_181\n"
        "go_180:\n"
        "v_mfma_f32_32x32x16_bf16 v[0:15], v[20:23], v[24:27], v[0:15]\n"
        "s_add_u32 s88, s88, 1024\n"
        "s_add_u32 s89, s89, 1024\n"
        "s_cmp_lt_u32 s89, 131072\n"
        "s_cbranch_scc1 slow_178\n"
        "s_bitcmp1_b32 s3, 0\n"
        "s_cbranch_scc1 odd_182\n"
        "s_nop 5\n"
        "v_add_f32 v0, v0, v32\n"
        "v_add_f32 v1, v1, v33\n"
        "v_add_f32 v2, v2, v34\n"
        "v_add_f32 v3, v3, v35\n"
        "v_add_f32 v4, v4, v36\n"
        "v_add_f32 v5, v5, v37\n"
        "v_add_f32 v6, v6, v38\n"
        "v_add_f32 v7, v7, v39\n"
        "s_branch par_183\n"
        "odd_182:\n"
        "v_mfma_f32_32x32x8bf16_1k v[0:15], v[54:55], v[40:41], v[0:15]\n"
        "s_nop 11\n"
        "v_add_f32 v0, v8, v32\n"
        "v_add_f32 v1, v9, v33\n"
        "v_add_f32 v2, v10, v34\n"
        "v_add_f32 v3, v11, v35\n"
        "v_add_f32 v4, v12, v36\n"
        "v_add_f32 v5, v13, v37\n"
        "v_add_f32 v6, v14, v38\n"
        "v_add_f32 v7, v15, v39\n"
        "par_183:\n"
        "s_branch go_97\n"
        "woslow_111:\n"
        "s_waitcnt vmcnt(0) lgkmcnt(0)\n"
        "s_add_u32 s88, s17, 0\n"
        "s_mov_b32 s89, 0\n"
        "v_mov_b64 v[0:1], 0\n"
        "v_mov_b64 v[2:3], 0\n"
        "v_mov_b64 v[4:5], 0\n"
        "v_mov_b64 v[6:7], 0\n"
        "slow_185:\n"
        "buffer_load_dwordx4 v[8:11], v53, s[32:35], s88 offen offset:0 sc1\n"
        "buffer_load_dwordx4 v[12:15], v48, s[36:39], s89 offen offset:0 sc1\n"
        "buffer_load_dwordx4 v[16:19], v48, s[36:39], s89 offen offset:256 sc1\n"
        "chk_186:\n"
        "s_waitcnt vmcnt(1)\n"
        "v_max_u32 v20, v12, v13\n"
        "v_max3_u32 v20, v20, v14, v15\n"
        "s_waitcnt vmcnt(0)\n"
        "v_max3_u32 v20, v20, v16, v17\n"
        "v_max3_u32 v20, v20, v18, v19\n"
        "v_cmp_eq_u32 vcc, -1, v20\n"
        "s_cbranch_vccnz rsw_188\n"
        "go_187:\n"
        "v_mfma_f32_16x16x32_bf16 v[0:3], v[8:11], v[12:15], v[0:3]\n"
        "v_mfma_f32_16x16x32_bf16 v[4:7], v[8:11], v[16:19], v[4:7]\n"
        "s_add_u32 s88, s88, 1024\n"
        "s_add_u32 s89, s89, 4096\n"
        "s_cmp_lt_u32 s89, 32768\n"
        "s_cbranch_scc1 slow_185\n"
        "s_add_u32 s88, s17, 8192\n"
        "s_mov_b32 s89, 2048\n"
        "slow_189:\n"
        "buffer_load_dwordx4 v[8:11], v53, s[32:35], s88 offen offset:0 sc1\n"
        "buffer_load_dwordx4 v[12:15], v48, s[36:39], s89 offen offset:0 sc1\n"
        "buffer_load_dwordx4 v[16:19], v48, s[36:39], s89 offen offset:256 sc1\n"
        "chk_190:\n"
        "s_waitcnt vmcnt(1)\n"
        "v_max_u32 v20, v12, v13\n"
        "v_max3_u32 v20, v20, v14, v15\n"
        "s_waitcnt vmcnt(0)\n"
        "v_max3_u32 v20, v20, v16, v17\n"
        "v_max3_u32 v20, v20, v18, v19\n"
        "v_cmp_eq_u32 vcc, -1, v20\n"
        "s_cbranch_vccnz rsw_192\n"
        "go_191:\n"
        "v_mfma_f32_16x16x32_bf16 v[0:3], v[8:11], v[12:15], v[0:3]\n"
        "v_mfma_f32_16x16x32_bf16 v[4:7], v[8:11], v[16:19], v[4:7]\n"
        "s_add_u32 s88, s88, 1024\n"
        "s_add_u32 s89, s89, 4096\n"
        "s_cmp_lt_u32 s89, 34816\n"
        "s_cbranch_scc1 slow_189\n"
        "s_branch go_112\n"
        "f1slow_134:\n"
        "s_waitcnt vmcnt(0) lgkmcnt(0)\n"
        "s_add_u32 s88, s26, 24576\n"
        "s_mov_b32 s89, 90112\n"
        "v_mov_b64 v[0:1], 0\n"
        "v_mov_b64 v[2:3], 0\n"
        "v_mov_b64 v[4:5], 0\n"
        "v_mov_b64 v[6:7], 0\n"
        "v_mov_b64 v[8:9], 0\n"
        "v_mov_b64 v[10:11], 0\n"
        "v_mov_b64 v[12:13], 0\n"
        "v_mov_b64 v[14:15], 0\n"
        "slow_194:\n"
        "buffer_load_dwordx4 v[20:23], v53, s[32:35], s88 offen offset:0 sc1\n"
        "buffer_load_dwordx4 v[24:27], v53, s[36:39], s89 offen offset:0 sc1\n"
        "chk_195:\n"
        "s_waitcnt vmcnt(0)\n"
        "v_max_u32 v42, v24, v25\n"
        "v_max3_u32 v42, v42, v26, v27\n"
        "v_cmp_eq_u32 vcc, -1, v42\n"
        "s_cbranch_vccnz rsf_197\n"
        "go_196:\n"
        "v_mfma_f32_32x32x16_bf16 v[0:15], v[20:23], v[24:27], v[0:15]\n"
        "s_add_u32 s88, s88, 1024\n"
        "s_add_u32 s89, s89, 1024\n"
        "s_cmp_lt_u32 s89, 98304\n"
        "s_cbranch_scc1 slow_194\n"
        "s_nop 7\n"
        "v_mul_f32 v28, v0, v60\n"
        "v_mul_f32 v29, v1, v60\n"
        "v_mul_f32 v30, v2, v60\n"
        "v_mul_f32 v31, v3, v60\n"
        "v_mul_f32 v32, v4, v60\n"
        "v_mul_f32 v33, v5, v60\n"
        "v_mul_f32 v34, v6, v60\n"
        "v_mul_f32 v35, v7, v60\n"
        "v_mul_f32 v36, v8, v60\n"
        "v_mul_f32 v37, v9, v60\n"
        "v_mul_f32 v38, v10, v60\n"
        "v_mul_f32 v39, v11, v60\n"
        "v_mul_f32 v16, v12, v60\n"
        "v_mul_f32 v17, v13, v60\n"
        "v_mul_f32 v18, v14, v60\n"
        "v_mul_f32 v19, v15, v60\n"
        "s_add_u32 s88, s26, 0\n"
        "s_mov_b32 s89, 65536\n"
        "v_mov_b64 v[0:1], 0\n"
        "v_mov_b64 v[2:3], 0\n"
        "v_mov_b64 v[4:5], 0\n"
        "v_mov_b64 v[6:7], 0\n"
        "v_mov_b64 v[8:9], 0\n"
        "v_mov_b64 v[10:11], 0\n"
        "v_mov_b64 v[12:13], 0\n"
        "v_mov_b64 v[14:15], 0\n"
        "slow_198:\n"
        "buffer_load_dwordx4 v[20:23], v53, s[32:35], s88 offen offset:0 sc1\n"
        "buffer_load_dwordx4 v[24:27], v53, s[36:39], s89 offen offset:0 sc1\n"
        "chk_199:\n"
        "s_waitcnt vmcnt(0)\n"
        "v_max_u32 v42, v24, v25\n"
        "v_max3_u32 v42, v42, v26, v27\n"
        "v_cmp_eq_u32 vcc, -1, v42\n"
        "s_cbranch_vccnz rsf_201\n"
        "go_200:\n"
        "v_mfma_f32_32x32x16_bf16 v[0:15], v[20:23], v[24:27], v[0:15]\n"
        "s_add_u32 s88, s88, 1024\n"
        "s_add_u32 s89, s89, 1024\n"
        "s_cmp_lt_u32 s89, 90112\n"
        "s_cbranch_scc1 slow_198\n"
        "v_mfma_f32_32x32x8bf16_1k v[0:15], v[54:55], v[40:41], v[0:15]\n"
        "s_branch go_135\n"
        "f1slow_136:\n"
        "s_waitcnt vmcnt(0) lgkmcnt(0)\n"
        "s_add_u32 s88, s26, 24576\n"
        "s_mov_b32 s89, 122880\n"
        "v_mov_b64 v[0:1], 0\n"
        "v_mov_b64 v[2:3], 0\n"
        "v_mov_b64 v[4:5], 0\n"
        "v_mov_b64 v[6:7], 0\n"
        "v_mov_b64 v[8:9], 0\n"
        "v_mov_b64 v[10:11], 0\n"
        "v_mov_b64 v[12:13], 0\n"
        "v_mov_b64 v[14:15], 0\n"
        "slow_203:\n"
        "buffer_load_dwordx4 v[20:23], v53, s[32:35], s88 offen offset:0 sc1\n"
        "buffer_load_dwordx4 v[24:27], v53, s[36:39], s89 offen offset:0 sc1\n"
        "chk_204:\n"
        "s_waitcnt vmcnt(0)\n"
        "v_max_u32 v42, v24, v25\n"
        "v_max3_u32 v42, v42, v26, v27\n"
        "v_cmp_eq_u32 vcc, -1, v42\n"
        "s_cbranch_vccnz rsf_206\n"
        "go_205:\n"
        "v_mfma_f32_32x32x16_bf16 v[0:15], v[20:23], v[24:27], v[0:15]\n"
        "s_add_u32 s88, s88, 1024\n"
        "s_add_u32 s89, s89, 1024\n"
        "s_cmp_lt_u32 s89, 131072\n"
        "s_cbranch_scc1 slow_203\n"
        "s_nop 7\n"
        "v_mul_f32 v28, v0, v60\n"
        "v_mul_f32 v29, v1, v60\n"
        "v_mul_f32 v30, v2, v60\n"
        "v_mul_f32 v31, v3, v60\n"
        "v_mul_f32 v32, v4, v60\n"
        "v_mul_f32 v33, v5, v60\n"
        "v_mul_f32 v34, v6, v60\n"
        "v_mul_f32 v35, v7, v60\n"
        "v_mul_f32 v36, v8, v60\n"
        "v_mul_f32 v37, v9, v60\n"
        "v_mul_f32 v38, v10, v60\n"
        "v_mul_f32 v39, v11, v60\n"
        "v_mul_f32 v16, v12, v60\n"
        "v_mul_f32 v17, v13, v60\n"
        "v_mul_f32 v18, v14, v60\n"
        "v_mul_f32 v19, v15, v60\n"
        "s_add_u32 s88, s26, 0\n"
        "s_mov_b32 s89, 98304\n"
        "v_mov_b64 v[0:1], 0\n"
        "v_mov_b64 v[2:3], 0\n"
        "v_mov_b64 v[4:5], 0\n"
        "v_mov_b64 v[6:7], 0\n"
        "v_mov_b64 v[8:9], 0\n"
        "v_mov_b64 v[10:11], 0\n"
        "v_mov_b64 v[12:13], 0\n"
        "v_mov_b64 v[14:15], 0\n"
        "slow_207:\n"
        "buffer_load_dwordx4 v[20:23], v53, s[32:35], s88 offen offset:0 sc1\n"
        "buffer_load_dwordx4 v[24:27], v53, s[36:39], s89 offen offset:0 sc1\n"
        "chk_208:\n"
        "s_waitcnt vmcnt(0)\n"
        "v_max_u32 v42, v24, v25\n"
        "v_max3_u32 v42, v42, v26, v27\n"
        "v_cmp_eq_u32 vcc, -1, v42\n"
        "s_cbranch_vccnz rsf_210\n"
        "go_209:\n"
        "v_mfma_f32_32x32x16_bf16 v[0:15], v[20:23], v[24:27], v[0:15]\n"
        "s_add_u32 s88, s88, 1024\n"
        "s_add_u32 s89, s89, 1024\n"
        "s_cmp_lt_u32 s89, 122880\n"
        "s_cbranch_scc1 slow_207\n"
        "v_mfma_f32_32x32x8bf16_1k v[0:15], v[54:55], v[40:41], v[0:15]\n"
        "s_branch go_137\n"
        "rmod_20:\n"
        "s_cmp_eq_u32 s46, 0\n"
        "s_cbranch_scc1 go_19\n"
        "s_sub_u32 s46, s46, 1\n"
        "buffer_load_dwordx4 v[16:19], v53, s[32:35], s63 offen sc1\n"
        "buffer_load_dwordx4 v[20:23], v53, s[32:35], s64 offen sc1\n"
        "s_waitcnt vmcnt(0) lgkmcnt(0)\n"
        "s_branch chk_18\n"
        "rmodr_34:\n"
        "s_cmp_eq_u32 s46, 0\n"
        "s_cbranch_scc1 go_33\n"
        "s_sub_u32 s46, s46, 1\n"
        "buffer_load_dwordx4 v[224:227], v53, s[32:35], s65 offen sc1\n"
        "buffer_load_dwordx4 v[228:231], v53, s[32:35], s66 offen sc1\n"
        "s_waitcnt vmcnt(0) lgkmcnt(0)\n"
        "s_branch chk_32\n"
        "rgate_38:\n"
        "s_cmp_eq_u32 s46, 0\n"
        "s_cbranch_scc1 go_37\n"
        "s_sub_u32 s46, s46, 1\n"
        "buffer_load_dwordx4 v[236:239], v10, s[32:35], s23 offen sc1\n"
        "buffer_load_dwordx4 v[240:243], v10, s[32:35], s89 offen sc1\n"
        "s_waitcnt vmcnt(0) lgkmcnt(0)\n"
        "s_branch chk_36\n"
        "rkv0_46:\n"
        "s_cmp_eq_u32 s46, 0\n"
        "s_cbranch_scc1 go_45\n"
        "s_sub_u32 s46, s46, 1\n"
        "buffer_load_dwordx4 v[64:67], v53, s[32:35], s24 offen offset:0 sc1\n"
        "buffer_load_dwordx4 v[68:71], v53, s[32:35], s24 offen offset:2048 sc1\n"
        "s_mov_b32 exec_lo, 0xffff\n"
        "s_mov_b32 exec_hi, 0xffff\n"
        "buffer_load_dwordx4 v[96:99], v61, s[32:35], s24 offen offset:1024 sc1\n"
        "buffer_load_dwordx4 v[100:103], v61, s[32:35], s24 offen offset:1536 sc1\n"
        "buffer_load_dwordx4 v[104:107], v61, s[32:35], s24 offen offset:3072 sc1\n"
        "buffer_load_dwordx4 v[108:111], v61, s[32:35], s24 offen offset:3584 sc1\n"
        "s_mov_b64 exec, -1\n"
        "s_waitcnt vmcnt(0) lgkmcnt(0)\n"
        "s_branch chk_44\n"
        "rkv1_49:\n"
        "s_cmp_eq_u32 s46, 0\n"
        "s_cbranch_scc1 go_48\n"
        "s_sub_u32 s46, s46, 1\n"
        "buffer_load_dwordx4 v[72:75], v53, s[32:35], s25 offen offset:0 sc1\n"
        "buffer_load_dwordx4 v[76:79], v53, s[32:35], s25 offen offset:2048 sc1\n"
        "s_mov_b32 exec_lo, 0xffff\n"
        "s_mov_b32 exec_hi, 0xffff\n"
        "buffer_load_dwordx4 v[112:115], v61, s[32:35], s25 offen offset:1024 sc1\n"
        "buffer_load_dwordx4 v[116:119], v61, s[32:35], s25 offen offset:1536 sc1\n"
        "buffer_load_dwordx4 v[120:123], v61, s[32:35], s25 offen offset:3072 sc1\n"
        "buffer_load_dwordx4 v[124:127], v61, s[32:35], s25 offen offset:3584 sc1\n"
        "s_mov_b64 exec, -1\n"
        "s_waitcnt vmcnt(0) lgkmcnt(0)\n"
        "s_branch chk_47\n"
        "rkv2_52:\n"
        "s_cmp_eq_u32 s46, 0\n"
        "s_cbranch_scc1 go_51\n"
        "s_sub_u32 s46, s46, 1\n"
        "buffer_load_dwordx4 v[80:83], v53, s[32:35], s26 offen offset:0 sc1\n"
        "buffer_load_dwordx4 v[84:87], v53, s[32:35], s26 offen offset:2048 sc1\n"
        "s_mov_b32 exec_lo, 0xffff\n"
        "s_mov_b32 exec_hi, 0xffff\n"
        "buffer_load_dwordx4 v[128:131], v61, s[32:35], s26 offen offset:1024 sc1\n"
        "buffer_load_dwordx4 v[132:135], v61, s[32:35], s26 offen offset:1536 sc1\n"
        "buffer_load_dwordx4 v[136:139], v61, s[32:35], s26 offen offset:3072 sc1\n"
        "buffer_load_dwordx4 v[140:143], v61, s[32:35], s26 offen offset:3584 sc1\n"
        "s_mov_b64 exec, -1\n"
        "s_waitcnt vmcnt(0) lgkmcnt(0)\n"
        "s_branch chk_50\n"
        "rkv3_55:\n"
        "s_cmp_eq_u32 s46, 0\n"
        "s_cbranch_scc1 go_54\n"
        "s_sub_u32 s46, s46, 1\n"
        "buffer_load_dwordx4 v[88:91], v53, s[32:35], s27 offen offset:0 sc1\n"
        "buffer_load_dwordx4 v[92:95], v53, s[32:35], s27 offen offset:2048 sc1\n"
        "s_mov_b32 exec_lo, 0xffff\n"
        "s_mov_b32 exec_hi, 0xffff\n"
        "buffer_load_dwordx4 v[144:147], v61, s[32:35], s27 offen offset:1024 sc1\n"
        "buffer_load_dwordx4 v[148:151], v61, s[32:35], s27 offen offset:1536 sc1\n"
        "buffer_load_dwordx4 v[152:155], v61, s[32:35], s27 offen offset:3072 sc1\n"
        "buffer_load_dwordx4 v[156:159], v61, s[32:35], s27 offen offset:3584 sc1\n"
        "s_mov_b64 exec, -1\n"
        "s_waitcnt vmcnt(0) lgkmcnt(0)\n"
        "s_branch chk_53\n"
        "rflag_63:\n"
        "s_cmp_eq_u32 s46, 0\n"
        "s_cbranch_scc1 go_62\n"
        "s_sub_u32 s46, s46, 1\n"
        "s_sleep 4\n"
        "buffer_load_dwordx4 v[184:187], v53, s[32:35], 0 offen offset:1024 sc1\n"
        "s_waitcnt vmcnt(0) lgkmcnt(0)\n"
        "s_branch chk_61\n"
        "robe_77:\n"
        "s_cmp_eq_u32 s93, 0\n"
        "s_cbranch_scc1 go_76\n"
        "s_sub_u32 s93, s93, 1\n"
        "s_add_u32 s88, s86, 2048\n"
        "s_mov_b32 m0, s87\n"
        "s_nop 0\n"
        "buffer_load_dwordx4 v53, s[36:39], s86 offen offset:0 sc1 lds\n"
        "buffer_load_dwordx4 v53, s[36:39], s86 offen offset:1024 sc1 lds\n"
        "buffer_load_dwordx4 v53, s[36:39], s88 offen offset:2048 sc1 lds\n"
        "buffer_load_dwordx4 v53, s[36:39], s88 offen offset:3072 sc1 lds\n"
        "s_waitcnt vmcnt(0)\n"
        "ds_read_b32 v43, v42\n"
        "s_waitcnt vmcnt(0) lgkmcnt(0)\n"
        "s_branch chk_75\n"
        "rau_80:\n"
        "s_cmp_eq_u32 s46, 0\n"
        "s_cbranch_scc1 go_79\n"
        "s_sub_u32 s46, s46, 1\n"
        "v_and_b32 v37, 0x1f0, v53\n"
        "buffer_load_dwordx4 v[54:57], v37, s[32:35], s27 offen sc1\n"
        "s_waitcnt vmcnt(0) lgkmcnt(0)\n"
        "s_branch chk_78\n"
        "robl_83:\n"
        "s_cmp_eq_u32 s46, 0\n"
        "s_cbranch_scc1 go_82\n"
        "s_sub_u32 s46, s46, 1\n"
        "s_mov_b32 m0, s67\n"
        "s_nop 0\n"
        "buffer_load_dwordx4 v53, s[36:39], s20 offen offset:0 sc1 lds\n"
        "buffer_load_dwordx4 v53, s[36:39], s20 offen offset:1024 sc1 lds\n"
        "buffer_load_dwordx4 v53, s[36:39], s21 offen offset:2048 sc1 lds\n"
        "buffer_load_dwordx4 v53, s[36:39], s21 offen offset:3072 sc1 lds\n"
        "s_waitcnt vmcnt(0)\n"
        "ds_read_b32 v36, v38\n"
        "s_waitcnt vmcnt(0) lgkmcnt(0)\n"
        "s_branch chk_81\n"
        "rh0_100:\n"
        "s_cmp_eq_u32 s46, 0\n"
        "s_cbranch_scc1 go_99\n"
        "s_sub_u32 s46, s46, 1\n"
        "buffer_load_dwordx4 v[164:167], v53, s[36:39], s48 offen offset:0 sc1\n"
        "buffer_load_dwordx4 v[168:171], v53, s[36:39], s49 offen offset:0 sc1\n"
        "buffer_load_dwordx4 v[172:175], v53, s[36:39], s50 offen offset:0 sc1\n"
        "buffer_load_dwordx4 v[176:179], v53, s[36:39], s51 offen offset:0 sc1\n"
        "buffer_load_dwordx4 v[180:183], v53, s[36:39], s52 offen offset:0 sc1\n"
        "buffer_load_dwordx4 v[184:187], v53, s[36:39], s53 offen offset:0 sc1\n"
        "buffer_load_dwordx4 v[188:191], v53, s[36:39], s54 offen offset:0 sc1\n"
        "buffer_load_dwordx4 v[192:195], v53, s[36:39], s55 offen offset:0 sc1\n"
        "buffer_load_dwordx4 v[196:199], v53, s[36:39], s56 offen offset:0 sc1\n"
        "buffer_load_dwordx4 v[200:203], v53, s[36:39], s57 offen offset:0 sc1\n"
        "v_mfma_f32_32x32x16_bf16 v[0:15], v[96:99], v[48:51], 0\n"
        "v_mfma_f32_32x32x16_bf16 v[16:31], v[112:115], v[48:51], 0\n"
        "s_waitcnt vmcnt(9)\n"
        "v_mfma_f32_32x32x16_bf16 v[0:15], v[100:103], v[164:167], v[0:15]\n"
        "v_max3_u32 v36, v164, v165, v166\n"
        "v_max_u32 v36, v36, v167\n"
        "v_mfma_f32_32x32x16_bf16 v[16:31], v[116:119], v[164:167], v[16:31]\n"
        "s_waitcnt vmcnt(8)\n"
        "s_nop 0\n"
        "page9:\n"
        "s_cbranch_execz warm_back\n"
        "v_mfma_f32_32x32x16_bf16 v[0:15], v[104:107], v[168:171], v[0:15]\n"
        "v_max3_u32 v36, v36, v168, v169\n"
        "v_max3_u32 v36, v36, v170, v171\n"
        "v_mfma_f32_32x32x16_bf16 v[16:31], v[120:123], v[168:171], v[16:31]\n"
        "s_waitcnt vmcnt(7)\n"
        "v_mfma_f32_32x32x16_bf16 v[0:15], v[108:111], v[172:175], v[0:15]\n"
        "v_max3_u32 v36, v36, v172, v173\n"
        "v_max3_u32 v36, v36, v174, v175\n"
        "v_mfma_f32_32x32x16_bf16 v[16:31], v[124:127], v[172:175], v[16:31]\n"
        "s_waitcnt vmcnt(6)\n"
        "v_mfma_f32_32x32x16_bf16 v[0:15], v[128:131], v[176:179], v[0:15]\n"
        "v_max3_u32 v36, v36, v176, v177\n"
        "v_max3_u32 v36, v36, v178, v179\n"
        "v_mfma_f32_32x32x16_bf16 v[16:31], v[132:135], v[176:179], v[16:31]\n"
        "s_waitcnt vmcnt(5)\n"
        "v_mfma_f32_32x32x16_bf16 v[0:15], v[136:139], v[180:183], v[0:15]\n"
        "v_max3_u32 v36, v36, v180, v181\n"
        "v_max3_u32 v36, v36, v182, v183\n"
        "v_mfma_f32_32x32x16_bf16 v[16:31], v[140:143], v[180:183], v[16:31]\n"
        "s_waitcnt vmcnt(4)\n"
        "v_mfma_f32_32x32x16_bf16 v[0:15], v[144:147], v[184:187], v[0:15]\n"
        "v_max3_u32 v36, v36, v184, v185\n"
        "v_max3_u32 v36, v36, v186, v187\n"
        "v_mfma_f32_32x32x16_bf16 v[16:31], v[148:151], v[184:187], v[16:31]\n"
        "s_waitcnt vmcnt(3)\n"
        "v_mfma_f32_32x32x16_bf16 v[0:15], v[64:67], v[188:191], v[0:15]\n"
        "v_max3_u32 v36, v36, v188, v189\n"
        "v_max3_u32 v36, v36, v190, v191\n"
        "v_mfma_f32_32x32x16_bf16 v[16:31], v[68:71], v[188:191], v[16:31]\n"
        "s_waitcnt vmcnt(2)\n"
        "v_mfma_f32_32x32x16_bf16 v[0:15], v[72:75], v[192:195], v[0:15]\n"
        "v_max3_u32 v36, v36, v192, v193\n"
        "v_max3_u32 v36, v36, v194, v195\n"
        "v_mfma_f32_32x32x16_bf16 v[16:31], v[76:79], v[192:195], v[16:31]\n"
        "s_waitcnt vmcnt(1)\n"
        "v_mfma_f32_32x32x16_bf16 v[0:15], v[80:83], v[196:199], v[0:15]\n"
        "v_max3_u32 v36, v36, v196, v197\n"
        "v_max3_u32 v36, v36, v198, v199\n"
        "v_mfma_f32_32x32x16_bf16 v[16:31], v[84:87], v[196:199], v[16:31]\n"
        "s_waitcnt vmcnt(0)\n"
        "v_mfma_f32_32x32x16_bf16 v[0:15], v[88:91], v[200:203], v[0:15]\n"
        "v_max3_u32 v36, v36, v200, v201\n"
        "v_max3_u32 v36, v36, v202, v203\n"
        "v_mfma_f32_32x32x16_bf16 v[16:31], v[92:95], v[200:203], v[16:31]\n"
        "s_waitcnt vmcnt(0) lgkmcnt(0)\n"
        "s_branch chk_98\n"
        "rh1_103:\n"
        "s_cmp_eq_u32 s46, 0\n"
        "s_cbranch_scc1 go_102\n"
        "s_sub_u32 s46, s46, 1\n"
        "buffer_load_dwordx4 v[208:211], v53, s[40:43], s48 offen offset:0 sc1\n"
        "buffer_load_dwordx4 v[212:215], v53, s[40:43], s49 offen offset:0 sc1\n"
        "buffer_load_dwordx4 v[216:219], v53, s[40:43], s50 offen offset:0 sc1\n"
        "buffer_load_dwordx4 v[220:223], v53, s[40:43], s51 offen offset:0 sc1\n"
        "buffer_load_dwordx4 v[224:227], v53, s[40:43], s52 offen offset:0 sc1\n"
        "buffer_load_dwordx4 v[228:231], v53, s[40:43], s53 offen offset:0 sc1\n"
        "buffer_load_dwordx4 v[232:235], v53, s[40:43], s54 offen offset:0 sc1\n"
        "buffer_load_dwordx4 v[236:239], v53, s[40:43], s55 offen offset:0 sc1\n"
        "buffer_load_dwordx4 v[240:243], v53, s[40:43], s56 offen offset:0 sc1\n"
        "buffer_load_dwordx4 v[244:247], v53, s[40:43], s57 offen offset:0 sc1\n"
        "v_mfma_f32_32x32x16_bf16 v[160:175], v[96:99], v[204:207], 0\n"
        "v_mfma_f32_32x32x16_bf16 v[176:191], v[112:115], v[204:207], 0\n"
        "s_waitcnt vmcnt(9)\n"
        "v_mfma_f32_32x32x16_bf16 v[160:175], v[100:103], v[208:211], v[160:175]\n"
        "v_max3_u32 v36, v208, v209, v210\n"
        "v_max_u32 v36, v36, v211\n"
        "v_mfma_f32_32x32x16_bf16 v[176:191], v[116:119], v[208:211], v[176:191]\n"
        "s_waitcnt vmcnt(8)\n"
        "v_mfma_f32_32x32x16_bf16 v[160:175], v[104:107], v[212:215], v[160:175]\n"
        "v_max3_u32 v36, v36, v212, v213\n"
        "v_max3_u32 v36, v36, v214, v215\n"
        "v_mfma_f32_32x32x16_bf16 v[176:191], v[120:123], v[212:215], v[176:191]\n"
        "s_waitcnt vmcnt(7)\n"
        "v_mfma_f32_32x32x16_bf16 v[160:175], v[108:111], v[216:219], v[160:175]\n"
        "v_max3_u32 v36, v36, v216, v217\n"
        "v_max3_u32 v36, v36, v218, v219\n"
        "v_mfma_f32_32x32x16_bf16 v[176:191], v[124:127], v[216:219], v[176:191]\n"
        "s_waitcnt vmcnt(6)\n"
        "v_mfma_f32_32x32x16_bf16 v[160:175], v[128:131], v[220:223], v[160:175]\n"
        "v_max3_u32 v36, v36, v220, v221\n"
        "v_max3_u32 v36, v36, v222, v223\n"
        "v_mfma_f32_32x32x16_bf16 v[176:191], v[132:135], v[220:223], v[176:191]\n"
        "s_waitcnt vmcnt(5)\n"
        "v_mfma_f32_32x32x16_bf16 v[160:175], v[136:139], v[224:227], v[160:175]\n"
        "v_max3_u32 v36, v36, v224, v225\n"
        "v_max3_u32 v36, v36, v226, v227\n"
        "v_mfma_f32_32x32x16_bf16 v[176:191], v[140:143], v[224:227], v[176:191]\n"
        "s_waitcnt vmcnt(4)\n"
        "v_mfma_f32_32x32x16_bf16 v[160:175], v[144:147], v[228:231], v[160:175]\n"
        "v_max3_u32 v36, v36, v228, v229\n"
        "v_max3_u32 v36, v36, v230, v231\n"
        "v_mfma_f32_32x32x16_bf16 v[176:191], v[148:151], v[228:231], v[176:191]\n"
        "s_waitcnt vmcnt(3)\n"
        "v_mfma_f32_32x32x16_bf16 v[160:175], v[64:67], v[232:235], v[160:175]\n"
        "v_max3_u32 v36, v36, v232, v233\n"
        "v_max3_u32 v36, v36, v234, v235\n"
        "v_mfma_f32_32x32x16_bf16 v[176:191], v[68:71], v[232:235], v[176:191]\n"
        "s_waitcnt vmcnt(2)\n"
        "v_mfma_f32_32x32x16_bf16 v[160:175], v[72:75], v[236:239], v[160:175]\n"
        "v_max3_u32 v36, v36, v236, v237\n"
        "v_max3_u32 v36, v36, v238, v239\n"
        "v_mfma_f32_32x32x16_bf16 v[176:191], v[76:79], v[236:239], v[176:191]\n"
        "s_waitcnt vmcnt(1)\n"
        "v_mfma_f32_32x32x16_bf16 v[160:175], v[80:83], v[240:243], v[160:175]\n"
        "v_max3_u32 v36, v36, v240, v241\n"
        "v_max3_u32 v36, v36, v242, v243\n"
        "v_mfma_f32_32x32x16_bf16 v[176:191], v[84:87], v[240:243], v[176:191]\n"
        "s_waitcnt vmcnt(0)\n"
        "v_mfma_f32_32x32x16_bf16 v[160:175], v[88:91], v[244:247], v[160:175]\n"
        "v_max3_u32 v36, v36, v244, v245\n"
        "v_max3_u32 v36, v36, v246, v247\n"
        "v_mfma_f32_32x32x16_bf16 v[176:191], v[92:95], v[244:247], v[176:191]\n"
        "s_waitcnt vmcnt(0) lgkmcnt(0)\n"
        "s_branch chk_101\n"
        "rclk_108:\n"
        "s_cmp_eq_u32 s46, 0\n"
        "s_cbranch_scc1 go_107\n"
        "s_sub_u32 s46, s46, 1\n"
        "s_mov_b32 s88, 323584\n"
        "buffer_load_dwordx2 v[66:67], v64, s[36:39], s88 offen sc1\n"
        "s_waitcnt vmcnt(0) lgkmcnt(0)\n"
        "s_branch chk_106\n"
        "rau_115:\n"
        "s_cmp_eq_u32 s46, 0\n"
        "s_cbranch_scc1 go_114\n"
        "s_sub_u32 s46, s46, 1\n"
        "v_and_b32 v37, 0x1f0, v53\n"
        "buffer_load_dwordx4 v[54:57], v37, s[32:35], s27 offen sc1\n"
        "s_waitcnt vmcnt(0) lgkmcnt(0)\n"
        "s_branch chk_113\n"
        "ry0_118:\n"
        "s_cmp_eq_u32 s46, 0\n"
        "s_cbranch_scc1 go_117\n"
        "s_sub_u32 s46, s46, 1\n"
        "buffer_load_dwordx4 v[32:35], v53, s[36:39], s63 offen offset:0 sc1\n"
        "s_add_u32 m0, s62, 0\n"
        "s_nop 0\n"
        "buffer_load_dwordx4 v53, s[36:39], s60 offen offset:0 sc1 lds\n"
        "buffer_load_dwordx4 v53, s[36:39], s60 offen offset:1024 sc1 lds\n"
        "buffer_load_dwordx4 v53, s[36:39], s60 offen offset:2048 sc1 lds\n"
        "buffer_load_dwordx4 v53, s[36:39], s60 offen offset:3072 sc1 lds\n"
        "s_waitcnt vmcnt(0) lgkmcnt(0)\n"
        "s_branch chk_116\n"
        "ry0_121:\n"
        "s_cmp_eq_u32 s46, 0\n"
        "s_cbranch_scc1 go_120\n"
        "s_sub_u32 s46, s46, 1\n"
        "s_sub_u32 s88, s63, 4096\n"
        "buffer_load_dwordx4 v[28:31], v53, s[36:39], s88 offen offset:0 sc1\n"
        "s_sub_u32 s88, s60, 16384\n"
        "s_add_u32 m0, s62, -16384\n"
        "s_nop 0\n"
        "buffer_load_dwordx4 v53, s[36:39], s88 offen offset:0 sc1 lds\n"
        "buffer_load_dwordx4 v53, s[36:39], s88 offen offset:1024 sc1 lds\n"
        "buffer_load_dwordx4 v53, s[36:39], s88 offen offset:2048 sc1 lds\n"
        "buffer_load_dwordx4 v53, s[36:39], s88 offen offset:3072 sc1 lds\n"
        "s_waitcnt vmcnt(0) lgkmcnt(0)\n"
        "s_branch chk_119\n"
        "ryp0_124:\n"
        "s_cmp_eq_u32 s46, 0\n"
        "s_cbranch_scc1 go_123\n"
        "s_sub_u32 s46, s46, 1\n"
        "s_add_u32 m0, s62, 0\n"
        "s_nop 0\n"
        "buffer_load_dwordx4 v53, s[36:39], s60 offen offset:0 sc1 lds\n"
        "buffer_load_dwordx4 v53, s[36:39], s60 offen offset:1024 sc1 lds\n"
        "buffer_load_dwordx4 v53, s[36:39], s60 offen offset:2048 sc1 lds\n"
        "buffer_load_dwordx4 v53, s[36:39], s60 offen offset:3072 sc1 lds\n"
        "s_sub_u32 s88, s60, 16384\n"
        "s_add_u32 m0, s62, -16384\n"
        "s_nop 0\n"
        "buffer_load_dwordx4 v53, s[36:39], s88 offen offset:0 sc1 lds\n"
        "buffer_load_dwordx4 v53, s[36:39], s88 offen offset:1024 sc1 lds\n"
        "buffer_load_dwordx4 v53, s[36:39], s88 offen offset:2048 sc1 lds\n"
        "buffer_load_dwordx4 v53, s[36:39], s88 offen offset:3072 sc1 lds\n"
        "s_waitcnt vmcnt(0)\n"
        "ds_read_b32 v46, v47 offset:16384\n"
        "ds_read_b32 v45, v47 offset:0\n"
        "s_waitcnt vmcnt(0) lgkmcnt(0)\n"
        "s_branch chk_122\n"
        "ry1_127:\n"
        "s_cmp_eq_u32 s46, 0\n"
        "s_cbranch_scc1 go_126\n"
        "s_sub_u32 s46, s46, 1\n"
        "buffer_load_dwordx4 v[48:51], v53, s[36:39], s64 offen offset:0 sc1\n"
        "s_add_u32 m0, s62, 32768\n"
        "s_nop 0\n"
        "buffer_load_dwordx4 v53, s[36:39], s61 offen offset:0 sc1 lds\n"
        "buffer_load_dwordx4 v53, s[36:39], s61 offen offset:1024 sc1 lds\n"
        "buffer_load_dwordx4 v53, s[36:39], s61 offen offset:2048 sc1 lds\n"
        "buffer_load_dwordx4 v53, s[36:39], s61 offen offset:3072 sc1 lds\n"
        "s_waitcnt vmcnt(0) lgkmcnt(0)\n"
        "s_branch chk_125\n"
        "ry1_130:\n"
        "s_cmp_eq_u32 s46, 0\n"
        "s_cbranch_scc1 go_129\n"
        "s_sub_u32 s46, s46, 1\n"
        "s_sub_u32 s88, s64, 4096\n"
        "buffer_load_dwordx4 v[28:31], v53, s[36:39], s88 offen offset:0 sc1\n"
        "s_sub_u32 s88, s61, 16384\n"
        "s_add_u32 m0, s62, 16384\n"
        "s_nop 0\n"
        "buffer_load_dwordx4 v53, s[36:39], s88 offen offset:0 sc1 lds\n"
        "buffer_load_dwordx4 v53, s[36:39], s88 offen offset:1024 sc1 lds\n"
        "buffer_load_dwordx4 v53, s[36:39], s88 offen offset:2048 sc1 lds\n"
        "buffer_load_dwordx4 v53, s[36:39], s88 offen offset:3072 sc1 lds\n"
        "s_waitcnt vmcnt(0) lgkmcnt(0)\n"
        "s_branch chk_128\n"
        "ryp1_133:\n"
        "s_cmp_eq_u32 s46, 0\n"
        "s_cbranch_scc1 go_132\n"
        "s_sub_u32 s46, s46, 1\n"
        "s_add_u32 m0, s62, 32768\n"
        "s_nop 0\n"
        "buffer_load_dwordx4 v53, s[36:39], s61 offen offset:0 sc1 lds\n"
        "buffer_load_dwordx4 v53, s[36:39], s61 offen offset:1024 sc1 lds\n"
        "buffer_load_dwordx4 v53, s[36:39], s61 offen offset:2048 sc1 lds\n"
        "buffer_load_dwordx4 v53, s[36:39], s61 offen offset:3072 sc1 lds\n"
        "s_sub_u32 s88, s61, 16384\n"
        "s_add_u32 m0, s62, 16384\n"
        "s_nop 0\n"
        "buffer_load_dwordx4 v53, s[36:39], s88 offen offset:0 sc1 lds\n"
        "buffer_load_dwordx4 v53, s[36:39], s88 offen offset:1024 sc1 lds\n"
        "buffer_load_dwordx4 v53, s[36:39], s88 offen offset:2048 sc1 lds\n"
        "buffer_load_dwordx4 v53, s[36:39], s88 offen offset:3072 sc1 lds\n"
        "s_waitcnt vmcnt(0)\n"
        "ds_read_b32 v46, v47 offset:49152\n"
        "ds_read_b32 v45, v47 offset:32768\n"
        "s_waitcnt vmcnt(0) lgkmcnt(0)\n"
        "s_branch chk_131\n"
        "rh0_140:\n"
        "s_cmp_eq_u32 s46, 0\n"
        "s_cbranch_scc1 go_139\n"
        "s_sub_u32 s46, s46, 1\n"
        "buffer_load_dwordx4 v[168:171], v53, s[36:39], s49 offen offset:0 sc1\n"
        "buffer_load_dwordx4 v[172:175], v53, s[36:39], s50 offen offset:0 sc1\n"
        "buffer_load_dwordx4 v[176:179], v53, s[36:39], s51 offen offset:0 sc1\n"
        "buffer_load_dwordx4 v[180:183], v53, s[36:39], s52 offen offset:0 sc1\n"
        "buffer_load_dwordx4 v[184:187], v53, s[36:39], s53 offen offset:0 sc1\n"
        "buffer_load_dwordx4 v[188:191], v53, s[36:39], s54 offen offset:0 sc1\n"
        "buffer_load_dwordx4 v[192:195], v53, s[36:39], s55 offen offset:0 sc1\n"
        "buffer_load_dwordx4 v[196:199], v53, s[36:39], s56 offen offset:0 sc1\n"
        "buffer_load_dwordx4 v[200:203], v53, s[36:39], s57 offen offset:0 sc1\n"
        "v_mfma_f32_32x32x16_bf16 v[0:15], v[96:99], v[44:47], 0\n"
        "v_mfma_f32_32x32x16_bf16 v[16:31], v[112:115], v[44:47], 0\n"
        "v_mfma_f32_32x32x16_bf16 v[0:15], v[100:103], v[48:51], v[0:15]\n"
        "v_mfma_f32_32x32x16_bf16 v[16:31], v[116:119], v[48:51], v[16:31]\n"
        "s_waitcnt vmcnt(8)\n"
        "v_mfma_f32_32x32x16_bf16 v[0:15], v[104:107], v[168:171], v[0:15]\n"
        "v_max3_u32 v36, v168, v169, v170\n"
        "v_max_u32 v36, v36, v171\n"
        "v_mfma_f32_32x32x16_bf16 v[16:31], v[120:123], v[168:171], v[16:31]\n"
        "s_waitcnt vmcnt(7)\n"
        "v_mfma_f32_32x32x16_bf16 v[0:15], v[108:111], v[172:175], v[0:15]\n"
        "v_max3_u32 v36, v36, v172, v173\n"
        "v_max3_u32 v36, v36, v174, v175\n"
        "v_mfma_f32_32x32x16_bf16 v[16:31], v[124:127], v[172:175], v[16:31]\n"
        "s_waitcnt vmcnt(6)\n"
        "v_mfma_f32_32x32x16_bf16 v[0:15], v[128:131], v[176:179], v[0:15]\n"
        "v_max3_u32 v36, v36, v176, v177\n"
        "v_max3_u32 v36, v36, v178, v179\n"
        "v_mfma_f32_32x32x16_bf16 v[16:31], v[132:135], v[176:179], v[16:31]\n"
        "s_waitcnt vmcnt(5)\n"
        "v_mfma_f32_32x32x16_bf16 v[0:15], v[136:139], v[180:183], v[0:15]\n"
        "v_max3_u32 v36, v36, v180, v181\n"
        "v_max3_u32 v36, v36, v182, v183\n"
        "v_mfma_f32_32x32x16_bf16 v[16:31], v[140:143], v[180:183], v[16:31]\n"
        "s_waitcnt vmcnt(4)\n"
        "v_mfma_f32_32x32x16_bf16 v[0:15], v[144:147], v[184:187], v[0:15]\n"
        "v_max3_u32 v36, v36, v184, v185\n"
        "v_max3_u32 v36, v36, v186, v187\n"
        "v_mfma_f32_32x32x16_bf16 v[16:31], v[148:151], v[184:187], v[16:31]\n"
        "s_waitcnt vmcnt(3)\n"
        "v_mfma_f32_32x32x16_bf16 v[0:15], v[64:67], v[188:191], v[0:15]\n"
        "v_max3_u32 v36, v36, v188, v189\n"
        "v_max3_u32 v36, v36, v190, v191\n"
        "v_mfma_f32_32x32x16_bf16 v[16:31], v[68:71], v[188:191], v[16:31]\n"
        "s_waitcnt vmcnt(2)\n"
        "v_mfma_f32_32x32x16_bf16 v[0:15], v[72:75], v[192:195], v[0:15]\n"
        "v_max3_u32 v36, v36, v192, v193\n"
        "v_max3_u32 v36, v36, v194, v195\n"
        "v_mfma_f32_32x32x16_bf16 v[16:31], v[76:79], v[192:195], v[16:31]\n"
        "s_waitcnt vmcnt(1)\n"
        "v_mfma_f32_32x32x16_bf16 v[0:15], v[80:83], v[196:199], v[0:15]\n"
        "v_max3_u32 v36, v36, v196, v197\n"
        "v_max3_u32 v36, v36, v198, v199\n"
        "v_mfma_f32_32x32x16_bf16 v[16:31], v[84:87], v[196:199], v[16:31]\n"
        "s_waitcnt vmcnt(0)\n"
        "v_mfma_f32_32x32x16_bf16 v[0:15], v[88:91], v[200:203], v[0:15]\n"
        "v_max3_u32 v36, v36, v200, v201\n"
        "v_max3_u32 v36, v36, v202, v203\n"
        "v_mfma_f32_32x32x16_bf16 v[16:31], v[92:95], v[200:203], v[16:31]\n"
        "s_waitcnt vmcnt(0) lgkmcnt(0)\n"
        "s_branch chk_138\n"
        "rh1_143:\n"
        "s_cmp_eq_u32 s46, 0\n"
        "s_cbranch_scc1 go_142\n"
        "s_sub_u32 s46, s46, 1\n"
        "buffer_load_dwordx4 v[212:215], v53, s[40:43], s49 offen offset:0 sc1\n"
        "buffer_load_dwordx4 v[216:219], v53, s[40:43], s50 offen offset:0 sc1\n"
        "buffer_load_dwordx4 v[220:223], v53, s[40:43], s51 offen offset:0 sc1\n"
        "buffer_load_dwordx4 v[224:227], v53, s[40:43], s52 offen offset:0 sc1\n"
        "buffer_load_dwordx4 v[228:231], v53, s[40:43], s53 offen offset:0 sc1\n"
        "buffer_load_dwordx4 v[232:235], v53, s[40:43], s54 offen offset:0 sc1\n"
        "buffer_load_dwordx4 v[236:239], v53, s[40:43], s55 offen offset:0 sc1\n"
        "buffer_load_dwordx4 v[240:243], v53, s[40:43], s56 offen offset:0 sc1\n"
        "buffer_load_dwordx4 v[244:247], v53, s[40:43], s57 offen offset:0 sc1\n"
        "v_mfma_f32_32x32x16_bf16 v[160:175], v[96:99], v[204:207], 0\n"
        "v_mfma_f32_32x32x16_bf16 v[176:191], v[112:115], v[204:207], 0\n"
        "v_mfma_f32_32x32x16_bf16 v[160:175], v[100:103], v[208:211], v[160:175]\n"
        "v_mfma_f32_32x32x16_bf16 v[176:191], v[116:119], v[208:211], v[176:191]\n"
        "s_waitcnt vmcnt(8)\n"
        "v_mfma_f32_32x32x16_bf16 v[160:175], v[104:107], v[212:215], v[160:175]\n"
        "v_max3_u32 v36, v212, v213, v214\n"
        "v_max_u32 v36, v36, v215\n"
        "v_mfma_f32_32x32x16_bf16 v[176:191], v[120:123], v[212:215], v[176:191]\n"
        "s_waitcnt vmcnt(7)\n"
        "v_mfma_f32_32x32x16_bf16 v[160:175], v[108:111], v[216:219], v[160:175]\n"
        "v_max3_u32 v36, v36, v216, v217\n"
        "v_max3_u32 v36, v36, v218, v219\n"
        "v_mfma_f32_32x32x16_bf16 v[176:191], v[124:127], v[216:219], v[176:191]\n"
        "s_waitcnt vmcnt(6)\n"
        "v_mfma_f32_32x32x16_bf16 v[160:175], v[128:131], v[220:223], v[160:175]\n"
        "v_max3_u32 v36, v36, v220, v221\n"
        "v_max3_u32 v36, v36, v222, v223\n"
        "v_mfma_f32_32x32x16_bf16 v[176:191], v[132:135], v[220:223], v[176:191]\n"
        "s_waitcnt vmcnt(5)\n"
        "v_mfma_f32_32x32x16_bf16 v[160:175], v[136:139], v[224:227], v[160:175]\n"
        "v_max3_u32 v36, v36, v224, v225\n"
        "v_max3_u32 v36, v36, v226, v227\n"
        "v_mfma_f32_32x32x16_bf16 v[176:191], v[140:143], v[224:227], v[176:191]\n"
        "s_waitcnt vmcnt(4)\n"
        "v_mfma_f32_32x32x16_bf16 v[160:175], v[144:147], v[228:231], v[160:175]\n"
        "v_max3_u32 v36, v36, v228, v229\n"
        "v_max3_u32 v36, v36, v230, v231\n"
        "v_mfma_f32_32x32x16_bf16 v[176:191], v[148:151], v[228:231], v[176:191]\n"
        "s_waitcnt vmcnt(3)\n"
        "v_mfma_f32_32x32x16_bf16 v[160:175], v[64:67], v[232:235], v[160:175]\n"
        "v_max3_u32 v36, v36, v232, v233\n"
        "v_max3_u32 v36, v36, v234, v235\n"
        "v_mfma_f32_32x32x16_bf16 v[176:191], v[68:71], v[232:235], v[176:191]\n"
        "s_waitcnt vmcnt(2)\n"
        "v_mfma_f32_32x32x16_bf16 v[160:175], v[72:75], v[236:239], v[160:175]\n"
        "v_max3_u32 v36, v36, v236, v237\n"
        "v_max3_u32 v36, v36, v238, v239\n"
        "v_mfma_f32_32x32x16_bf16 v[176:191], v[76:79], v[236:239], v[176:191]\n"
        "s_waitcnt vmcnt(1)\n"
        "v_mfma_f32_32x32x16_bf16 v[160:175], v[80:83], v[240:243], v[160:175]\n"
        "v_max3_u32 v36, v36, v240, v241\n"
        "v_max3_u32 v36, v36, v242, v243\n"
        "v_mfma_f32_32x32x16_bf16 v[176:191], v[84:87], v[240:243], v[176:191]\n"
        "s_waitcnt vmcnt(0)\n"
        "v_mfma_f32_32x32x16_bf16 v[160:175], v[88:91], v[244:247], v[160:175]\n"
        "v_max3_u32 v36, v36, v244, v245\n"
        "v_max3_u32 v36, v36, v246, v247\n"
        "v_mfma_f32_32x32x16_bf16 v[176:191], v[92:95], v[244:247], v[176:191]\n"
        "s_waitcnt vmcnt(0) lgkmcnt(0)\n"
        "s_branch chk_141\n"
        "rfb_148:\n"
        "s_cmp_eq_u32 s46, 0\n"
        "s_cbranch_scc1 go_147\n"
        "s_sub_u32 s46, s46, 1\n"
        "buffer_load_dwordx4 v[16:19], v53, s[32:35], s91 offen sc1\n"
        "s_mov_b32 exec_lo, 0xffff\n"
        "s_mov_b32 exec_hi, 0xffff\n"
        "buffer_load_dwordx4 v[20:23], v54, s[32:35], s91 offen offset:1024 sc1\n"
        "buffer_load_dwordx4 v[24:27], v54, s[32:35], s91 offen offset:1536 sc1\n"
        "s_mov_b64 exec, -1\n"
        "s_waitcnt vmcnt(0) lgkmcnt(0)\n"
        "s_branch chk_146\n"
        "rsw_153:\n"
        "s_cmp_eq_u32 s46, 0\n"
        "s_cbranch_scc1 go_152\n"
        "s_sub_u32 s46, s46, 1\n"
        "buffer_load_dwordx4 v[12:15], v48, s[36:39], s89 offen offset:0 sc1\n"
        "buffer_load_dwordx4 v[16:19], v48, s[36:39], s89 offen offset:256 sc1\n"
        "s_waitcnt vmcnt(0) lgkmcnt(0)\n"
        "s_branch chk_151\n"
        "rsw_157:\n"
        "s_cmp_eq_u32 s46, 0\n"
        "s_cbranch_scc1 go_156\n"
        "s_sub_u32 s46, s46, 1\n"
        "buffer_load_dwordx4 v[12:15], v48, s[36:39], s89 offen offset:0 sc1\n"
        "buffer_load_dwordx4 v[16:19], v48, s[36:39], s89 offen offset:256 sc1\n"
        "s_waitcnt vmcnt(0) lgkmcnt(0)\n"
        "s_branch chk_155\n"
        "rsf_162:\n"
        "s_cmp_eq_u32 s46, 0\n"
        "s_cbranch_scc1 go_161\n"
        "s_sub_u32 s46, s46, 1\n"
        "buffer_load_dwordx4 v[24:27], v53, s[36:39], s89 offen offset:0 sc1\n"
        "s_waitcnt vmcnt(0) lgkmcnt(0)\n"
        "s_branch chk_160\n"
        "rsf_168:\n"
        "s_cmp_eq_u32 s46, 0\n"
        "s_cbranch_scc1 go_167\n"
        "s_sub_u32 s46, s46, 1\n"
        "buffer_load_dwordx4 v[24:27], v53, s[36:39], s89 offen offset:0 sc1\n"
        "s_waitcnt vmcnt(0) lgkmcnt(0)\n"
        "s_branch chk_166\n"
        "rsf_175:\n"
        "s_cmp_eq_u32 s46, 0\n"
        "s_cbranch_scc1 go_174\n"
        "s_sub_u32 s46, s46, 1\n"
        "buffer_load_dwordx4 v[24:27], v53, s[36:39], s89 offen offset:0 sc1\n"
        "s_waitcnt vmcnt(0) lgkmcnt(0)\n"
        "s_branch chk_173\n"
        "rsf_181:\n"
        "s_cmp_eq_u32 s46, 0\n"
        "s_cbranch_scc1 go_180\n"
        "s_sub_u32 s46, s46, 1\n"
        "buffer_load_dwordx4 v[24:27], v53, s[36:39], s89 offen offset:0 sc1\n"
        "s_waitcnt vmcnt(0) lgkmcnt(0)\n"
        "s_branch chk_179\n"
        "rsw_188:\n"
        "s_cmp_eq_u32 s46, 0\n"
        "s_cbranch_scc1 go_187\n"
        "s_sub_u32 s46, s46, 1\n"
        "buffer_load_dwordx4 v[12:15], v48, s[36:39], s89 offen offset:0 sc1\n"
        "buffer_load_dwordx4 v[16:19], v48, s[36:39], s89 offen offset:256 sc1\n"
        "s_waitcnt vmcnt(0) lgkmcnt(0)\n"
        "s_branch chk_186\n"
        "rsw_192:\n"
        "s_cmp_eq_u32 s46, 0\n"
        "s_cbranch_scc1 go_191\n"
        "s_sub_u32 s46, s46, 1\n"
        "buffer_load_dwordx4 v[12:15], v48, s[36:39], s89 offen offset:0 sc1\n"
        "buffer_load_dwordx4 v[16:19], v48, s[36:39], s89 offen offset:256 sc1\n"
        "s_waitcnt vmcnt(0) lgkmcnt(0)\n"
        "s_branch chk_190\n"
        "rsf_197:\n"
        "s_cmp_eq_u32 s46, 0\n"
        "s_cbranch_scc1 go_196\n"
        "s_sub_u32 s46, s46, 1\n"
        "buffer_load_dwordx4 v[24:27], v53, s[36:39], s89 offen offset:0 sc1\n"
        "s_waitcnt vmcnt(0) lgkmcnt(0)\n"
        "s_branch chk_195\n"
        "rsf_201:\n"
        "s_cmp_eq_u32 s46, 0\n"
        "s_cbranch_scc1 go_200\n"
        "s_sub_u32 s46, s46, 1\n"
        "buffer_load_dwordx4 v[24:27], v53, s[36:39], s89 offen offset:0 sc1\n"
        "s_waitcnt vmcnt(0) lgkmcnt(0)\n"
        "s_branch chk_199\n"
        "rsf_206:\n"
        "s_cmp_eq_u32 s46, 0\n"
        "s_cbranch_scc1 go_205\n"
        "s_sub_u32 s46, s46, 1\n"
        "buffer_load_dwordx4 v[24:27], v53, s[36:39], s89 offen offset:0 sc1\n"
        "s_waitcnt vmcnt(0) lgkmcnt(0)\n"
        "s_branch chk_204\n"
        "rsf_210:\n"
        "s_cmp_eq_u32 s46, 0\n"
        "s_cbranch_scc1 go_209\n"
        "s_sub_u32 s46, s46, 1\n"
        "buffer_load_dwordx4 v[24:27], v53, s[36:39], s89 offen offset:0 sc1\n"
        "s_waitcnt vmcnt(0) lgkmcnt(0)\n"
        "s_branch chk_208\n"
        "code_end:\n"
        : "+{v0}"(tid), "+{s[100:101]}"(tab)
        : "{s[0:1]}"(ka), "{s2}"(bid)
        : "v1", "v2", "v3", "v4", "v5", "v6", "v7", "v8", "v9", "v10", "v11", "v12", "v13", "v14", "v15", "v16", "v17", "v18", "v19", "v20", "v21", "v22", "v23", "v24", "v25", "v26", "v27", "v28", "v29", "v30", "v31", "v32", "v33", "v34", "v35", "v36", "v37", "v38", "v39", "v40", "v41", "v42", "v43", "v44", "v45", "v46", "v47", "v48", "v49", "v50", "v51", "v52", "v53", "v54", "v55", "v56", "v57", "v58", "v59", "v60", "v61", "v62", "v63", "v64", "v65", "v66", "v67", "v68", "v69", "v70", "v71", "v72", "v73", "v74", "v75", "v76", "v77", "v78", "v79", "v80", "v81", "v82", "v83", "v84", "v85", "v86", "v87", "v88", "v89", "v90", "v91", "v92", "v93", "v94", "v95", "v96", "v97", "v98", "v99", "v100", "v101", "v102", "v103", "v104", "v105", "v106", "v107", "v108", "v109", "v110", "v111", "v112", "v113", "v114", "v115", "v116", "v117", "v118", "v119", "v120", "v121", "v122", "v123", "v124", "v125", "v126", "v127", "v128", "v129", "v130", "v131", "v132", "v133", "v134", "v135", "v136", "v137", "v138", "v139", "v140", "v141", "v142", "v143", "v144", "v145", "v146", "v147", "v148", "v149", "v150", "v151", "v152", "v153", "v154", "v155", "v156", "v157", "v158", "v159", "v160", "v161", "v162", "v163", "v164", "v165", "v166", "v167", "v168", "v169", "v170", "v171", "v172", "v173", "v174", "v175", "v176", "v177", "v178", "v179", "v180", "v181", "v182", "v183", "v184", "v185", "v186", "v187", "v188", "v189", "v190", "v191", "v192", "v193", "v194", "v195", "v196", "v197", "v198", "v199", "v200", "v201", "v202", "v203", "v204", "v205", "v206", "v207", "v208", "v209", "v210", "v211", "v212", "v213", "v214", "v215", "v216", "v217", "v218", "v219", "v220", "v221", "v222", "v223", "v224", "v225", "v226", "v227", "v228", "v229", "v230", "v231", "v232", "v233", "v234", "v235", "v236", "v237", "v238", "v239", "v240", "v241", "v242", "v243", "v244", "v245", "v246", "v247", "v248", "v249", "v250", "v251", "v252", "v253", "v254", "v255", "s3", "s4", "s5", "s6", "s7", "s8", "s9", "s10", "s11", "s12", "s13", "s14", "s15", "s16", "s17", "s18", "s19", "s20", "s21", "s22", "s23", "s24", "s25", "s26", "s27", "s28", "s29", "s30", "s31", "s33", "s34", "s35", "s36", "s37", "s38", "s39", "s40", "s41", "s42", "s43", "s44", "s45", "s46", "s47", "s48", "s49", "s50", "s51", "s52", "s53", "s54", "s55", "s56", "s57", "s58", "s59", "s60", "s61", "s62", "s63", "s64", "s65", "s66", "s67", "s68", "s69", "s70", "s71", "s72", "s73", "s74", "s75", "s76", "s77", "s78", "s79", "s80", "s81", "s82", "s83", "s84", "s85", "s86", "s87", "s88", "s89", "s90", "s91", "s92", "s93", "s94", "s95", "s96", "s97", "s98", "s99", "vcc", "scc", "memory");
#endif
}

extern "C" void kernel_launch(void* const* d_in, const int* in_sizes, int n_in, void* d_out, int out_size, void* d_ws, size_t ws_size, hipStream_t stream) {
    (void)hipFuncSetAttribute((const void*)adaln_block, hipFuncAttributeMaxDynamicSharedMemorySize, 163840);
    const unsigned F = 0x20000;
    adaln_block<<<256, 512, 163840, stream>>>(d_in[0], 65536u, F, d_in[1], 524288u, F, d_in[2], 16384u, F, d_in[3], 16384u, F,
                                              d_in[8], 1024u, F, d_in[9], 3145728u, F, d_out, 65536u, F, d_ws, 3954688u, F,
                                              (char*)d_ws + 4194304, 324608u, F, d_in[4], 524288u, F, d_in[6], 1409024u, F, d_in[7], 1409024u, F);
}
```
